```python
import torch

def _softmax(x, axis=-1):
    return torch.softmax(x, dim=axis)
def _gelu(x):
    return torch.nn.functional.gelu(x, approximate="tanh")
def _silu(x):
    return torch.nn.functional.silu(x)
def _sigmoid(x):
    return torch.sigmoid(x)
def _layernorm(x, axis=-1, eps=1e-5):
    m = x.mean(dim=axis, keepdim=True)
    v = x.var(dim=axis, keepdim=True, unbiased=False)
    return (x - m) / torch.sqrt(v + eps)
def _scatter_add(tgt, idx, src, axis=0):
    return tgt.clone().index_add_(axis, idx, src)
def _segment_sum(src, seg, n):
    out = torch.zeros((n,) + src.shape[1:], dtype=src.dtype)
    return out.index_add_(0, seg, src)
def _pad(x, pw):
    flat = tuple(v for pair in reversed(pw) for v in pair)
    return torch.nn.functional.pad(x, flat)

kernel_name = 'sequential_preLN__grouped_kv_mqa__dense_activation_relu__prompt'

def setup_inputs():

    _w = (lambda a: (lambda t: t.to(torch.bfloat16) if t.is_floating_point() else t)(torch.as_tensor(a)))
    def _A_setup():
        B, S, D, H = 1, 2048, 576, 32
        Dh = D // H
        return {
            "x": _w((lambda *s: torch.randn(*s, dtype=torch.bfloat16) * 0.05)(B, S, D)),
            "wq": _w((lambda *s: torch.randn(*s, dtype=torch.bfloat16) * 0.05)(D, H * Dh)),
            "wk": _w((lambda *s: torch.randn(*s, dtype=torch.bfloat16) * 0.05)(D, Dh)),
            "wv": _w((lambda *s: torch.randn(*s, dtype=torch.bfloat16) * 0.05)(D, Dh)),
            "wo": _w((lambda *s: torch.randn(*s, dtype=torch.bfloat16) * 0.05)(H * Dh, D)),
        }
    _A_in = _A_setup()

    def _B_setup():
        B, S, D, Dff = 1, 2048, 576, 4608
        return {
            "x": _w((lambda *s: torch.randn(*s, dtype=torch.bfloat16) * 0.05)(B, S, D)),
            "w1": _w((lambda *s: torch.randn(*s, dtype=torch.bfloat16) * 0.05)(D, Dff)),
            "w2": _w((lambda *s: torch.randn(*s, dtype=torch.bfloat16) * 0.05)(Dff, D)),
        }
    _B_in = _B_setup()

    return {
        "A_x": _A_in["x"],
        "A_wq": _A_in["wq"],
        "A_wk": _A_in["wk"],
        "A_wv": _A_in["wv"],
        "A_wo": _A_in["wo"],
        "B_x": _B_in["x"],
        "B_w1": _B_in["w1"],
        "B_w2": _B_in["w2"],
    }

def reference(A_x, A_wq, A_wk, A_wv, A_wo, B_x, B_w1, B_w2):
    def _A_ref(x, wq, wk, wv, wo):
        B, S, D = x.shape
        H = 32
        Dh = D // H
        q = (lambda x, *d: x.permute(*d))((x @ wq).reshape(B, S, H, Dh), 0, 2, 1, 3)
        k = x @ wk
        v = x @ wv
        scores = torch.einsum("bhqd,bkd->bhqk", q, k) * (Dh ** -0.5)
        p = _softmax(scores, axis=-1)
        o = torch.einsum("bhqk,bkd->bhqd", p, v)
        return (lambda x, *d: x.permute(*d))(o, 0, 2, 1, 3).reshape(B, S, H * Dh) @ wo

    def _B_ref(x, w1, w2):
        h = torch.einsum("bsd,df->bsf", x, w1)
        zero = torch.zeros_like(h)
        h = torch.maximum(h, zero)
        return torch.einsum("bsf,fd->bsd", h, w2)

    x = A_x
    h = x + _A_ref(_layernorm(x, axis=-1), A_wq, A_wk, A_wv, A_wo)
    h2 = h + _B_ref(_layernorm(h, axis=-1), B_w1, B_w2)
    return h2
```

```cpp
#include <hip/hip_runtime.h>
#include <hip/hip_bf16.h>
typedef unsigned short u16;
__device__ __forceinline__ float b2f(u16 v){ return __uint_as_float(((unsigned)v)<<16); }
__device__ __forceinline__ u16 f2b(float f){ unsigned u=__float_as_uint(f); u+=0x7fffu+((u>>16)&1u); return (u16)(u>>16); }
constexpr int S=2048,D=576,H=32,DH=18,F=4608;
__global__ void ln_k(const u16* x, u16* y){
  int r=blockIdx.x; __shared__ float red[256]; int t=threadIdx.x;
  float s=0; for(int i=t;i<D;i+=256) s+=b2f(x[r*D+i]); red[t]=s; __syncthreads();
  for(int o=128;o>0;o>>=1){ if(t<o) red[t]+=red[t+o]; __syncthreads(); }
  float m=red[0]/D; __syncthreads();
  s=0; for(int i=t;i<D;i+=256){ float d=b2f(x[r*D+i])-m; s+=d*d;} red[t]=s; __syncthreads();
  for(int o=128;o>0;o>>=1){ if(t<o) red[t]+=red[t+o]; __syncthreads(); }
  float rs=rsqrtf(red[0]/D+1e-5f);
  for(int i=t;i<D;i+=256) y[r*D+i]=f2b((b2f(x[r*D+i])-m)*rs);
}
__global__ void gemm_k(const u16* A,const u16* B,u16* C,const u16* res,int M,int N,int K,int relu){
  __shared__ float As[16][17],Bs[16][17];
  int tx=threadIdx.x,ty=threadIdx.y; int row=blockIdx.y*16+ty,col=blockIdx.x*16+tx; float acc=0;
  for(int k0=0;k0<K;k0+=16){
    As[ty][tx]=(k0+tx<K)?b2f(A[row*K+k0+tx]):0.f;
    Bs[ty][tx]=(k0+ty<K&&col<N)?b2f(B[(k0+ty)*N+col]):0.f;
    __syncthreads();
    for(int k=0;k<16;k++) acc+=As[ty][k]*Bs[k][tx];
    __syncthreads();
  }
  if(col<N){ if(relu) acc=fmaxf(acc,0.f); if(res) acc=b2f(f2b(acc))+b2f(res[row*N+col]); C[row*N+col]=f2b(acc);}
}
__global__ void attn_k(const u16* q,const u16* k,const u16* v,u16* o){
  int idx=blockIdx.x*blockDim.x+threadIdx.x; int tok=idx/H,h=idx%H;
  float qq[DH],acc[DH]; for(int d=0;d<DH;d++){qq[d]=b2f(q[tok*D+h*DH+d])*0.23570226f; acc[d]=0;}
  float m=-1e30f,l=0;
  for(int j=0;j<S;j++){ float s=0; for(int d=0;d<DH;d++) s+=qq[d]*b2f(k[j*DH+d]);
    float mn=fmaxf(m,s); float a=__expf(m-mn),p=__expf(s-mn); l=l*a+p; for(int d=0;d<DH;d++) acc[d]=acc[d]*a+p*b2f(v[j*DH+d]); m=mn; }
  for(int d=0;d<DH;d++) o[tok*D+h*DH+d]=f2b(acc[d]/l);
}
extern "C" void kernel_launch(void* const* d_in, const int* in_sizes, int n_in, void* d_out, int out_size, void* d_ws, size_t ws_size, hipStream_t stream){
  const u16* x=(const u16*)d_in[0]; const u16* wq=(const u16*)d_in[1]; const u16* wk=(const u16*)d_in[2]; const u16* wv=(const u16*)d_in[3]; const u16* wo=(const u16*)d_in[4];
  const u16* w1=(const u16*)d_in[6]; const u16* w2=(const u16*)d_in[7];
  u16* ws=(u16*)d_ws; u16* xn=ws; u16* q=xn+S*D; u16* k=q+S*D; u16* v=k+S*32; u16* o=v+S*32; u16* h=o+S*D; u16* hn=h+S*D; u16* a=hn+S*D;
  dim3 b(16,16);
  ln_k<<<S,256,0,stream>>>(x,xn);
  gemm_k<<<dim3(D/16,S/16),b,0,stream>>>(xn,wq,q,nullptr,S,D,D,0);
  gemm_k<<<dim3(2,S/16),b,0,stream>>>(xn,wk,k,nullptr,S,DH,D,0);
  gemm_k<<<dim3(2,S/16),b,0,stream>>>(xn,wv,v,nullptr,S,DH,D,0);
  attn_k<<<S*H/256,256,0,stream>>>(q,k,v,o);
  gemm_k<<<dim3(D/16,S/16),b,0,stream>>>(o,wo,h,x,S,D,D,0);
  ln_k<<<S,256,0,stream>>>(h,hn);
  gemm_k<<<dim3(F/16,S/16),b,0,stream>>>(hn,w1,a,nullptr,S,F,D,1);
  gemm_k<<<dim3(D/16,S/16),b,0,stream>>>(a,w2,(u16*)d_out,h,S,D,F,0);
}
```

```cpp
#include <hip/hip_runtime.h>
#include <stdint.h>

typedef unsigned short u16;
typedef unsigned u32;
typedef __bf16 bf16x2 __attribute__((ext_vector_type(2)));
typedef __bf16 bf16x8 __attribute__((ext_vector_type(8)));
typedef __bf16 bf16x16 __attribute__((ext_vector_type(16)));
typedef short s16x4 __attribute__((ext_vector_type(4)));
typedef float f32x2 __attribute__((ext_vector_type(2)));
typedef float f32x4 __attribute__((ext_vector_type(4)));
typedef float f32x8 __attribute__((ext_vector_type(8)));
typedef float f32x16 __attribute__((ext_vector_type(16)));
typedef u32 u32x2 __attribute__((ext_vector_type(2)));
typedef u32 u32x4 __attribute__((ext_vector_type(4)));
typedef u32 u32x8 __attribute__((ext_vector_type(8)));
typedef u32 u32x16 __attribute__((ext_vector_type(16)));
#define DI __device__ __forceinline__
#define LDSP __attribute__((address_space(3)))

constexpr int S_ = 2048, D_ = 576, F_ = 4608;
constexpr int CH = 19456;
constexpr size_t SZ_ACT = (size_t)S_ * D_ * 2;
constexpr size_t OFF_CNT = 262144;
constexpr size_t OFF_CT = 34816;
constexpr size_t OFF_HF = 36864;
constexpr size_t OFF_KVMAX = 32768;
constexpr size_t OFF_MEAN1 = 247808;
constexpr size_t OFF_SS = 327680;
constexpr size_t OFF_KV = 65536;
constexpr size_t OFF_RM = 229376;
constexpr size_t OFF_MU = 0;
constexpr size_t OFF_Q = 589824;
constexpr size_t OFF_O = OFF_Q + SZ_ACT;
constexpr size_t OFF_HC = OFF_O + SZ_ACT;
constexpr size_t OFF_WOT = OFF_HC + SZ_ACT;
constexpr size_t OFF_W1T = OFF_WOT + 663552;
constexpr size_t OFF_W2T = OFF_W1T + 5308416;
constexpr size_t OFF_A = OFF_W2T + 5308416;
constexpr size_t OFF_SLAB = OFF_A + 18874368;
constexpr size_t WS_USED = OFF_SLAB + 7340032;

DI u32 rfl(u32 x) { return __builtin_amdgcn_readfirstlane(x); }
DI u32 cvtpk(float lo, float hi) { f32x2 v = {lo, hi}; return __builtin_bit_cast(u32, __builtin_convertvector(v, bf16x2)); }
DI float bflo(u32 w) { return __uint_as_float(w << 16); }
DI float bfhi(u32 w) { return __uint_as_float(w & 0xffff0000u); }
DI float rbf(float x) { return bflo(cvtpk(x, x)); }
DI u32x4 mkrsrc(const void* p, u32 bytes) { const uint64_t a = (uint64_t)p; return u32x4{(u32)a, (u32)(a >> 32) & 0xffffu, bytes, 0x00020000u}; }
DI void dma16(u32 lds, u32 voff, u32x4 rsrc, u32 soff) {
  asm volatile("s_nop 2\n\ts_mov_b32 m0, %0\n\ts_nop 0\n\tbuffer_load_dwordx4 %1, %2, %3 offen lds" :: "s"(lds), "v"(voff), "s"(rsrc), "s"(soff) : "memory");
}
DI void dma16s(u32 lds, u32 voff, u32x4 rsrc, u32 soff) {
  asm volatile("s_nop 2\n\ts_mov_b32 m0, %0\n\ts_nop 0\n\tbuffer_load_dwordx4 %1, %2, %3 offen sc1 lds" :: "s"(lds), "v"(voff), "s"(rsrc), "s"(soff) : "memory");
}
DI s16x4 trread(u32 addr) {
  typedef short v4i16 __attribute__((ext_vector_type(4)));
  return __builtin_amdgcn_ds_read_tr16_b64_v4i16((LDSP v4i16*)(uintptr_t)addr);
}
DI bf16x8 cat8(s16x4 a, s16x4 b) { return __builtin_bit_cast(bf16x8, __builtin_shufflevector(a, b, 0, 1, 2, 3, 4, 5, 6, 7)); }
DI bf16x8 ldsrd(u32 addr) { return *(const LDSP bf16x8*)(uintptr_t)addr; }
DI float xmax(float v, int m) { return fmaxf(v, __shfl_xor(v, m)); }
template <int CTRL, int RM = 0xf> DI float dppf(float v) { return __builtin_bit_cast(float, __builtin_amdgcn_update_dpp(__builtin_bit_cast(int, v), __builtin_bit_cast(int, v), CTRL, RM, 0xf, false)); }
DI float swapadd32(float v) { const auto r = __builtin_amdgcn_permlane32_swap(__float_as_uint(v), __float_as_uint(v), false, false); return __uint_as_float(r[0]) + __uint_as_float(r[1]); }
DI float swapadd16(float v) { const auto r = __builtin_amdgcn_permlane16_swap(__float_as_uint(v), __float_as_uint(v), false, false); return __uint_as_float(r[0]) + __uint_as_float(r[1]); }
DI float sum8(float v) { v += dppf<0xB1>(v); v += dppf<0x4E>(v); v += dppf<0x141>(v); return v; }
DI float wavemax(float v) {
  v = fmaxf(v, dppf<0x111>(v)); v = fmaxf(v, dppf<0x112>(v)); v = fmaxf(v, dppf<0x114>(v)); v = fmaxf(v, dppf<0x118>(v));
  v = fmaxf(v, dppf<0x142, 0xa>(v)); v = fmaxf(v, dppf<0x143, 0xc>(v));
  return __uint_as_float(__builtin_amdgcn_readlane(__float_as_uint(v), 63)); }
DI float wavesum(float v) {
  v = sum8(v); v += dppf<0x140>(v); v = swapadd32(swapadd16(v));
  return __uint_as_float(rfl(__float_as_uint(v))); }
typedef __amdgpu_buffer_rsrc_t rsrc_t;
DI rsrc_t mkr(const void* p, u32 bytes) { return __builtin_amdgcn_make_buffer_rsrc((void*)p, 0, bytes, 0x00020000); }
#define ST16(v, r, vo, so) __builtin_amdgcn_raw_buffer_store_b128(__builtin_bit_cast(u32x4, v), r, (vo) + (so), 0, 16)
#define ST8(v, r, vo, so) __builtin_amdgcn_raw_buffer_store_b64(__builtin_bit_cast(u32x2, v), r, (vo) + (so), 0, 16)
DI void touch(u32 lds, u32 voff, u32x4 rsrc, u32 soff) {
  asm volatile("s_mov_b32 m0, %0\n\ts_nop 0\n\tbuffer_load_dword %1, %2, %3 offen lds" :: "s"(lds), "v"(voff), "s"(rsrc), "s"(soff) : "memory");
}
#define CODE_RSRC(r, first, last) do { const char *b_, *e_; \
  asm volatile("s_getpc_b64 s[40:41]\n\ts_add_u32 s40, s40, " #first "@rel32@lo+4\n\ts_addc_u32 s41, s41, " #first "@rel32@hi+12" : "={s[40:41]}"(b_) :: "scc"); \
  asm volatile("s_getpc_b64 s[42:43]\n\ts_add_u32 s42, s42, " #last "@rel32@lo+4\n\ts_addc_u32 s43, s43, " #last "@rel32@hi+12" : "={s[42:43]}"(e_) :: "scc"); \
  r = mkrsrc(b_, (u32)(e_ - b_)); } while (0)
#define G1 __attribute__((address_space(1)))
template <class T> DI void stw(void* p, T v) { __hip_atomic_store((G1 T*)p, v, __ATOMIC_RELAXED, __HIP_MEMORY_SCOPE_AGENT); }
#define BAR() asm volatile("s_barrier" ::: "memory")
#define MFMA16(a, b, c) __builtin_amdgcn_mfma_f32_16x16x32_bf16(a, b, c, 0, 0, 0)

constexpr int K1_W = 36864, K1_SLOT = 24576, K1_RI = K1_W + 5 * K1_SLOT, K1_LDS = K1_RI + 1024, K1_CP = 21504;
constexpr float QSCALE = 0.23570226039551584f * 1.4426950408889634f;

extern "C" __global__ __launch_bounds__(704) void k1_qkv(const u16* x, const u16* wq, const u16* wk, const u16* wv, const u16* wo, char* ws) {
#if defined(__HIP_DEVICE_COMPILE__)
  extern __shared__ __attribute__((aligned(16))) char lds_[];
  const u32 lds = (u32)(uintptr_t)(LDSP char*)lds_;
  const int b = blockIdx.x, rt = (b & 7) * 8 + (b >> 5), role = (b >> 3) & 3;
  const int tid = threadIdx.x, lane = tid & 63, w = rfl(tid >> 6);
  if (w == 8 || w == 9) { u32x4 rc; CODE_RSRC(rc, k1_qkv, k2_rest); touch(lds + K1_LDS - 256 * (1 + (w & 1)), lane * 128, rc, (w & 1) * 8192); }
  if (w >= 4 && w < 8) {
    const int lw = w - 4;
    if (role < 3) {
      const u32x4 rq = mkrsrc(wq, D_ * D_ * 2);
      u32 wvo[6];
#pragma unroll
      for (int i = 0; i < 6; ++i) { const int c = i * 64 + lane, r = c / 24, cc = c % 24; wvo[i] = (u32)(r * 1152 + role * 384 + (cc ^ ((r & 2) | ((r >> 1) & 4))) * 16); }
#define K1_DMA(c) do { _Pragma("unroll") for (int i = 0; i < 6; ++i) dma16(lds + K1_W + ((c) % 5) * K1_SLOT + (lw * 6 + i) * 1024, wvo[i], rq, (u32)(((c) * 64 + lw * 16) * 1152)); } while (0)
#pragma unroll
      for (int c = 0; c < 4; ++c) K1_DMA(c);
#pragma unroll
      for (int k = 0; k < 9; ++k) {
        const int issued = k < 5 - 4 ? 4 + k : (k + 3 < 5 ? 5 : (k + 3 > 9 ? 9 : k + 3));
        asm volatile("s_waitcnt vmcnt(%0)\n\ts_barrier" :: "i"((issued - 1 - k) * 6) : "memory");
        if (k < 5 - 4) K1_DMA(4 + k);
        if (k >= 2 && k + 3 < 9) K1_DMA(k + 3); }
    } else {
      const u32x4 rk = mkrsrc((const char*)((lw & 2) ? wv : wk) + (lw & 1) * 4, 20736 - (lw & 1) * 4);
      const u32 dead = lds + K1_W + 4 * K1_CP + lw * 256;
      const int i32 = (rt & 7) * 4 + lw;
#pragma unroll
      for (int i = 0; i < 21; ++i) dma16(lds + K1_W + lw * K1_CP + i * 1024, (u32)(lane * 16), rk, (u32)(i * 1024));
      if (rt < 2) stw(ws + OFF_CNT + ((rt * 4 + lw) * 64 + lane) * 128, 0u);
      if (lw == 3 && lane < 32) stw(ws + OFF_MU + (rt * 4 + (lane >> 3)) * 128 + (lane & 7) * 8, (uint64_t)0);
      if (lw == 2 && lane < 8) stw(ws + OFF_HF + (rt * 8 + lane) * 32, 0u);
      if (lw == 2 && rt == 2 && lane >= 8 && lane < 16) stw(ws + OFF_CT + (lane - 8) * 128, 0u);
      asm volatile("s_waitcnt vmcnt(0)\n\ts_barrier" ::: "memory");
      if (i32 < 8) { u32x4 rc; CODE_RSRC(rc, k2_rest, k2_code_end); touch(dead, lane * 128, rc, i32 * 8192); }
      const int hid = rt + 64 * lw;
      if (hid < 72) {
        const int sg = hid >> 2, g = hid & 3, e = 4 * (sg - 16) + g, q8 = lane >> 3;
        const int rw = (sg < 16) ? (2 * sg + (g >> 1)) * 18 + 4 * (g & 1) + (q8 & 3) + 8 * (q8 >> 2) : (4 * e + (q8 >> 1)) * 18 + 16 + (q8 & 1);
        u32x4 v[9];
#pragma unroll
        for (int i = 0; i < 9; ++i) v[i] = *(const u32x4*)((const char*)wo + rw * 1152 + i * 128 + (lane & 7) * 16);
        BAR();
        const u32 slot = lds + K1_W + 4 * K1_CP + 1024 + lw * 2048, ta = slot + ((lane & 15) >> 2) * 128 + (lane >> 4) * 32 + 8 * (lane & 3);
        const rsrc_t rd = mkr(ws + OFF_WOT, 663552);
        float s = 0.f;
#pragma unroll
        for (int i = 0; i < 9; ++i) {
#pragma unroll
          for (int e2 = 0; e2 < 4; ++e2) s += bflo(v[i][e2]) + bfhi(v[i][e2]);
          *(LDSP u32x4*)(uintptr_t)(slot + (i & 1) * 1024 + lane * 16) = v[i];
          ST16(cat8(trread(ta + (i & 1) * 1024), trread(ta + (i & 1) * 1024 + 512)), rd, ((lane >> 5) * 2 + ((lane >> 2) & 1)) * 18432 + (((lane >> 3) & 3) * 4 + (lane & 3)) * 16, (4 * i * 18 + sg) * 1024 + g * 256); }
        s = sum8(s) * (1.0f / D_);
        if ((lane & 7) == 0) stw(ws + OFF_RM + rw * 4, __float_as_uint(s));
      } else BAR();
    }
    return;
  }
  if (w < 4) { const int row = tid >> 3, part = tid & 7;
    u32x4 xv[9];
#pragma unroll
    for (int i = 0; i < 9; ++i) xv[i] = __builtin_amdgcn_raw_buffer_load_b128(mkr((const char*)x + (size_t)rt * 36864, 36864), row * 1152 + part * 16, i * 128, 0);
    f32x2 xf[36], s4[4] = {};
#pragma unroll
    for (int i = 0; i < 36; ++i) { xf[i] = f32x2{bflo(xv[i >> 2][i & 3]), bfhi(xv[i >> 2][i & 3])}; s4[i & 3] += xf[i]; }
    const f32x2 s2 = (s4[0] + s4[1]) + (s4[2] + s4[3]);
    const float m = sum8(s2[0] + s2[1]) * (1.0f / D_), mb = rbf(m);
    const u32 wa = lds + row * 1152 + ((part ^ ((row >> 1) & 7)) * 16);
#pragma unroll
    for (int i = 0; i < 9; ++i) { u32x4 y;
#pragma unroll
      for (int e = 0; e < 4; ++e) { xf[4 * i + e] -= mb; y[e] = cvtpk(xf[4 * i + e][0], xf[4 * i + e][1]); }
      *(LDSP u32x4*)(uintptr_t)(wa + i * 128) = y; }
    asm volatile("s_waitcnt lgkmcnt(0)\n\ts_barrier" ::: "memory");
    f32x2 q4_[4] = {};
#pragma unroll
    for (int i = 0; i < 36; ++i) q4_[i & 3] += xf[i] * xf[i];
    const f32x2 q2 = (q4_[0] + q4_[1]) + (q4_[2] + q4_[3]);
    const float var = fmaxf(sum8(q2[0] + q2[1]) * (1.0f / D_) - (m - mb) * (m - mb), 0.f);
    if (part == 0) { *(LDSP float*)(uintptr_t)(lds + K1_RI + row * 4) = __builtin_amdgcn_rcpf(rbf(__builtin_amdgcn_sqrtf(rbf(rbf(var) + 1e-5f))));
      if (role == 3) stw(ws + OFF_MEAN1 + (rt * 32 + row) * 4, __float_as_uint(m)); }
    asm volatile("s_waitcnt lgkmcnt(0)\n\ts_barrier" ::: "memory");
    return;
  }
  const int cw = w - 8, tok = lane & 15, g = lane >> 4, q4 = (lane & 15) >> 2, p4 = lane & 3;
  const u32 xa0 = lds + tok * 1152 + ((g ^ ((tok >> 1) & 7)) * 16), xa1 = lds + tok * 1152 + (((4 + g) ^ ((tok >> 1) & 7)) * 16);
  if (role < 3) {
    const u32 wa0 = lds + K1_W + (8 * g + q4) * 384 + (8 * cw + ((q4 & 2) | ((g & 1) << 2)) + (p4 >> 1)) * 16 + 8 * (p4 & 1);
    u32x4 qad = {wa0, wa0 ^ 32, wa0 ^ 64, wa0 ^ 96}; const u32x2 xad = {xa0, xa1}; f32x16 q0, q1;
    asm volatile(
    "s_barrier\n"
    "ds_read_b128 v[32:35], v132 offset:0\n"
    "ds_read_b128 v[36:39], v132 offset:18432\n"
    "ds_read_b64_tr_b16 v[56:57], v128 offset:0\n"
    "ds_read_b64_tr_b16 v[58:59], v128 offset:1536\n"
    "ds_read_b64_tr_b16 v[60:61], v129 offset:0\n"
    "ds_read_b64_tr_b16 v[62:63], v129 offset:1536\n"
    "ds_read_b64_tr_b16 v[64:65], v130 offset:0\n"
    "ds_read_b64_tr_b16 v[66:67], v130 offset:1536\n"
    "ds_read_b64_tr_b16 v[68:69], v131 offset:0\n"
    "ds_read_b64_tr_b16 v[70:71], v131 offset:1536\n"
    "s_waitcnt lgkmcnt(0)\n"
    "v_mfma_f32_16x16x32_bf16 v[0:3], v[56:59], v[32:35], 0\n"
    "ds_read_b128 v[40:43], v133 offset:0\n"
    "ds_read_b128 v[44:47], v133 offset:18432\n"
    "v_mfma_f32_16x16x32_bf16 v[4:7], v[56:59], v[36:39], 0\n"
    "ds_read_b64_tr_b16 v[72:73], v128 offset:12288\n"
    "ds_read_b64_tr_b16 v[74:75], v128 offset:13824\n"
    "v_mfma_f32_16x16x32_bf16 v[8:11], v[60:63], v[32:35], 0\n"
    "s_barrier\n"
    "ds_read_b64_tr_b16 v[76:77], v129 offset:12288\n"
    "ds_read_b64_tr_b16 v[78:79], v129 offset:13824\n"
    "v_mfma_f32_16x16x32_bf16 v[12:15], v[60:63], v[36:39], 0\n"
    "ds_read_b64_tr_b16 v[80:81], v130 offset:12288\n"
    "ds_read_b64_tr_b16 v[82:83], v130 offset:13824\n"
    "v_mfma_f32_16x16x32_bf16 v[16:19], v[64:67], v[32:35], 0\n"
    "ds_read_b64_tr_b16 v[84:85], v131 offset:12288\n"
    "ds_read_b64_tr_b16 v[86:87], v131 offset:13824\n"
    "v_mfma_f32_16x16x32_bf16 v[20:23], v[64:67], v[36:39], 0\n"
    "ds_read_b128 v[48:51], v132 offset:128\n"
    "ds_read_b128 v[52:55], v132 offset:18560\n"
    "v_mfma_f32_16x16x32_bf16 v[24:27], v[68:71], v[32:35], 0\n"
    "ds_read_b64_tr_b16 v[88:89], v128 offset:24576\n"
    "ds_read_b64_tr_b16 v[90:91], v128 offset:26112\n"
    "v_mfma_f32_16x16x32_bf16 v[28:31], v[68:71], v[36:39], 0\n"
    "ds_read_b64_tr_b16 v[92:93], v129 offset:24576\n"
    "s_waitcnt lgkmcnt(5)\n"
    "v_mfma_f32_16x16x32_bf16 v[0:3], v[72:75], v[40:43], v[0:3]\n"
    "ds_read_b64_tr_b16 v[94:95], v129 offset:26112\n"
    "ds_read_b64_tr_b16 v[96:97], v130 offset:24576\n"
    "v_mfma_f32_16x16x32_bf16 v[4:7], v[72:75], v[44:47], v[4:7]\n"
    "ds_read_b64_tr_b16 v[98:99], v130 offset:26112\n"
    "v_mfma_f32_16x16x32_bf16 v[8:11], v[76:79], v[40:43], v[8:11]\n"
    "ds_read_b64_tr_b16 v[100:101], v131 offset:24576\n"
    "v_mfma_f32_16x16x32_bf16 v[12:15], v[76:79], v[44:47], v[12:15]\n"
    "ds_read_b64_tr_b16 v[102:103], v131 offset:26112\n"
    "v_mfma_f32_16x16x32_bf16 v[16:19], v[80:83], v[40:43], v[16:19]\n"
    "ds_read_b128 v[32:35], v133 offset:128\n"
    "ds_read_b128 v[36:39], v133 offset:18560\n"
    "v_mfma_f32_16x16x32_bf16 v[20:23], v[80:83], v[44:47], v[20:23]\n"
    "ds_read_b64_tr_b16 v[56:57], v128 offset:36864\n"
    "v_mfma_f32_16x16x32_bf16 v[24:27], v[84:87], v[40:43], v[24:27]\n"
    "ds_read_b64_tr_b16 v[58:59], v128 offset:38400\n"
    "v_mfma_f32_16x16x32_bf16 v[28:31], v[84:87], v[44:47], v[28:31]\n"
    "ds_read_b64_tr_b16 v[60:61], v129 offset:36864\n"
    "s_waitcnt lgkmcnt(5)\n"
    "v_mfma_f32_16x16x32_bf16 v[0:3], v[88:91], v[48:51], v[0:3]\n"
    "ds_read_b64_tr_b16 v[62:63], v129 offset:38400\n"
    "ds_read_b64_tr_b16 v[64:65], v130 offset:36864\n"
    "v_mfma_f32_16x16x32_bf16 v[4:7], v[88:91], v[52:55], v[4:7]\n"
    "ds_read_b64_tr_b16 v[66:67], v130 offset:38400\n"
    "v_mfma_f32_16x16x32_bf16 v[8:11], v[92:95], v[48:51], v[8:11]\n"
    "s_barrier\n"
    "ds_read_b64_tr_b16 v[68:69], v131 offset:36864\n"
    "v_mfma_f32_16x16x32_bf16 v[12:15], v[92:95], v[52:55], v[12:15]\n"
    "ds_read_b64_tr_b16 v[70:71], v131 offset:38400\n"
    "v_mfma_f32_16x16x32_bf16 v[16:19], v[96:99], v[48:51], v[16:19]\n"
    "v_add_u32 v128, 49152, v128\n"
    "v_add_u32 v129, 49152, v129\n"
    "v_add_u32 v130, 49152, v130\n"
    "v_add_u32 v131, 49152, v131\n"
    "ds_read_b128 v[40:43], v132 offset:256\n"
    "ds_read_b128 v[44:47], v132 offset:18688\n"
    "v_mfma_f32_16x16x32_bf16 v[20:23], v[96:99], v[52:55], v[20:23]\n"
    "ds_read_b64_tr_b16 v[72:73], v128 offset:0\n"
    "v_mfma_f32_16x16x32_bf16 v[24:27], v[100:103], v[48:51], v[24:27]\n"
    "ds_read_b64_tr_b16 v[74:75], v128 offset:1536\n"
    "v_mfma_f32_16x16x32_bf16 v[28:31], v[100:103], v[52:55], v[28:31]\n"
    "ds_read_b64_tr_b16 v[76:77], v129 offset:0\n"
    "s_waitcnt lgkmcnt(5)\n"
    "v_mfma_f32_16x16x32_bf16 v[0:3], v[56:59], v[32:35], v[0:3]\n"
    "ds_read_b64_tr_b16 v[78:79], v129 offset:1536\n"
    "ds_read_b64_tr_b16 v[80:81], v130 offset:0\n"
    "v_mfma_f32_16x16x32_bf16 v[4:7], v[56:59], v[36:39], v[4:7]\n"
    "ds_read_b64_tr_b16 v[82:83], v130 offset:1536\n"
    "v_mfma_f32_16x16x32_bf16 v[8:11], v[60:63], v[32:35], v[8:11]\n"
    "ds_read_b64_tr_b16 v[84:85], v131 offset:0\n"
    "v_mfma_f32_16x16x32_bf16 v[12:15], v[60:63], v[36:39], v[12:15]\n"
    "ds_read_b64_tr_b16 v[86:87], v131 offset:1536\n"
    "v_mfma_f32_16x16x32_bf16 v[16:19], v[64:67], v[32:35], v[16:19]\n"
    "ds_read_b128 v[48:51], v133 offset:256\n"
    "ds_read_b128 v[52:55], v133 offset:18688\n"
    "v_mfma_f32_16x16x32_bf16 v[20:23], v[64:67], v[36:39], v[20:23]\n"
    "ds_read_b64_tr_b16 v[88:89], v128 offset:12288\n"
    "v_mfma_f32_16x16x32_bf16 v[24:27], v[68:71], v[32:35], v[24:27]\n"
    "ds_read_b64_tr_b16 v[90:91], v128 offset:13824\n"
    "v_mfma_f32_16x16x32_bf16 v[28:31], v[68:71], v[36:39], v[28:31]\n"
    "ds_read_b64_tr_b16 v[92:93], v129 offset:12288\n"
    "s_waitcnt lgkmcnt(5)\n"
    "v_mfma_f32_16x16x32_bf16 v[0:3], v[72:75], v[40:43], v[0:3]\n"
    "ds_read_b64_tr_b16 v[94:95], v129 offset:13824\n"
    "ds_read_b64_tr_b16 v[96:97], v130 offset:12288\n"
    "v_mfma_f32_16x16x32_bf16 v[4:7], v[72:75], v[44:47], v[4:7]\n"
    "ds_read_b64_tr_b16 v[98:99], v130 offset:13824\n"
    "v_mfma_f32_16x16x32_bf16 v[8:11], v[76:79], v[40:43], v[8:11]\n"
    "s_barrier\n"
    "ds_read_b64_tr_b16 v[100:101], v131 offset:12288\n"
    "v_mfma_f32_16x16x32_bf16 v[12:15], v[76:79], v[44:47], v[12:15]\n"
    "ds_read_b64_tr_b16 v[102:103], v131 offset:13824\n"
    "v_mfma_f32_16x16x32_bf16 v[16:19], v[80:83], v[40:43], v[16:19]\n"
    "ds_read_b128 v[32:35], v132 offset:384\n"
    "ds_read_b128 v[36:39], v132 offset:18816\n"
    "v_mfma_f32_16x16x32_bf16 v[20:23], v[80:83], v[44:47], v[20:23]\n"
    "ds_read_b64_tr_b16 v[56:57], v128 offset:24576\n"
    "v_mfma_f32_16x16x32_bf16 v[24:27], v[84:87], v[40:43], v[24:27]\n"
    "ds_read_b64_tr_b16 v[58:59], v128 offset:26112\n"
    "v_mfma_f32_16x16x32_bf16 v[28:31], v[84:87], v[44:47], v[28:31]\n"
    "ds_read_b64_tr_b16 v[60:61], v129 offset:24576\n"
    "s_waitcnt lgkmcnt(5)\n"
    "v_mfma_f32_16x16x32_bf16 v[0:3], v[88:91], v[48:51], v[0:3]\n"
    "ds_read_b64_tr_b16 v[62:63], v129 offset:26112\n"
    "ds_read_b64_tr_b16 v[64:65], v130 offset:24576\n"
    "v_mfma_f32_16x16x32_bf16 v[4:7], v[88:91], v[52:55], v[4:7]\n"
    "ds_read_b64_tr_b16 v[66:67], v130 offset:26112\n"
    "v_mfma_f32_16x16x32_bf16 v[8:11], v[92:95], v[48:51], v[8:11]\n"
    "ds_read_b64_tr_b16 v[68:69], v131 offset:24576\n"
    "v_mfma_f32_16x16x32_bf16 v[12:15], v[92:95], v[52:55], v[12:15]\n"
    "ds_read_b64_tr_b16 v[70:71], v131 offset:26112\n"
    "v_mfma_f32_16x16x32_bf16 v[16:19], v[96:99], v[48:51], v[16:19]\n"
    "ds_read_b128 v[40:43], v133 offset:384\n"
    "ds_read_b128 v[44:47], v133 offset:18816\n"
    "v_mfma_f32_16x16x32_bf16 v[20:23], v[96:99], v[52:55], v[20:23]\n"
    "ds_read_b64_tr_b16 v[72:73], v128 offset:36864\n"
    "v_mfma_f32_16x16x32_bf16 v[24:27], v[100:103], v[48:51], v[24:27]\n"
    "ds_read_b64_tr_b16 v[74:75], v128 offset:38400\n"
    "v_mfma_f32_16x16x32_bf16 v[28:31], v[100:103], v[52:55], v[28:31]\n"
    "ds_read_b64_tr_b16 v[76:77], v129 offset:36864\n"
    "s_waitcnt lgkmcnt(5)\n"
    "v_mfma_f32_16x16x32_bf16 v[0:3], v[56:59], v[32:35], v[0:3]\n"
    "ds_read_b64_tr_b16 v[78:79], v129 offset:38400\n"
    "ds_read_b64_tr_b16 v[80:81], v130 offset:36864\n"
    "v_mfma_f32_16x16x32_bf16 v[4:7], v[56:59], v[36:39], v[4:7]\n"
    "ds_read_b64_tr_b16 v[82:83], v130 offset:38400\n"
    "v_mfma_f32_16x16x32_bf16 v[8:11], v[60:63], v[32:35], v[8:11]\n"
    "s_barrier\n"
    "ds_read_b64_tr_b16 v[84:85], v131 offset:36864\n"
    "v_mfma_f32_16x16x32_bf16 v[12:15], v[60:63], v[36:39], v[12:15]\n"
    "ds_read_b64_tr_b16 v[86:87], v131 offset:38400\n"
    "v_mfma_f32_16x16x32_bf16 v[16:19], v[64:67], v[32:35], v[16:19]\n"
    "v_add_u32 v128, 49152, v128\n"
    "v_add_u32 v129, 49152, v129\n"
    "v_add_u32 v130, 49152, v130\n"
    "v_add_u32 v131, 49152, v131\n"
    "ds_read_b128 v[48:51], v132 offset:512\n"
    "ds_read_b128 v[52:55], v132 offset:18944\n"
    "v_mfma_f32_16x16x32_bf16 v[20:23], v[64:67], v[36:39], v[20:23]\n"
    "ds_read_b64_tr_b16 v[88:89], v128 offset:0\n"
    "v_mfma_f32_16x16x32_bf16 v[24:27], v[68:71], v[32:35], v[24:27]\n"
    "ds_read_b64_tr_b16 v[90:91], v128 offset:1536\n"
    "v_mfma_f32_16x16x32_bf16 v[28:31], v[68:71], v[36:39], v[28:31]\n"
    "ds_read_b64_tr_b16 v[92:93], v129 offset:0\n"
    "s_waitcnt lgkmcnt(5)\n"
    "v_mfma_f32_16x16x32_bf16 v[0:3], v[72:75], v[40:43], v[0:3]\n"
    "ds_read_b64_tr_b16 v[94:95], v129 offset:1536\n"
    "ds_read_b64_tr_b16 v[96:97], v130 offset:0\n"
    "v_mfma_f32_16x16x32_bf16 v[4:7], v[72:75], v[44:47], v[4:7]\n"
    "ds_read_b64_tr_b16 v[98:99], v130 offset:1536\n"
    "v_mfma_f32_16x16x32_bf16 v[8:11], v[76:79], v[40:43], v[8:11]\n"
    "ds_read_b64_tr_b16 v[100:101], v131 offset:0\n"
    "v_mfma_f32_16x16x32_bf16 v[12:15], v[76:79], v[44:47], v[12:15]\n"
    "ds_read_b64_tr_b16 v[102:103], v131 offset:1536\n"
    "v_mfma_f32_16x16x32_bf16 v[16:19], v[80:83], v[40:43], v[16:19]\n"
    "ds_read_b128 v[32:35], v133 offset:512\n"
    "ds_read_b128 v[36:39], v133 offset:18944\n"
    "v_mfma_f32_16x16x32_bf16 v[20:23], v[80:83], v[44:47], v[20:23]\n"
    "ds_read_b64_tr_b16 v[56:57], v128 offset:12288\n"
    "v_mfma_f32_16x16x32_bf16 v[24:27], v[84:87], v[40:43], v[24:27]\n"
    "ds_read_b64_tr_b16 v[58:59], v128 offset:13824\n"
    "v_mfma_f32_16x16x32_bf16 v[28:31], v[84:87], v[44:47], v[28:31]\n"
    "ds_read_b64_tr_b16 v[60:61], v129 offset:12288\n"
    "s_waitcnt lgkmcnt(5)\n"
    "v_mfma_f32_16x16x32_bf16 v[0:3], v[88:91], v[48:51], v[0:3]\n"
    "ds_read_b64_tr_b16 v[62:63], v129 offset:13824\n"
    "ds_read_b64_tr_b16 v[64:65], v130 offset:12288\n"
    "v_mfma_f32_16x16x32_bf16 v[4:7], v[88:91], v[52:55], v[4:7]\n"
    "ds_read_b64_tr_b16 v[66:67], v130 offset:13824\n"
    "v_mfma_f32_16x16x32_bf16 v[8:11], v[92:95], v[48:51], v[8:11]\n"
    "s_barrier\n"
    "ds_read_b64_tr_b16 v[68:69], v131 offset:12288\n"
    "v_mfma_f32_16x16x32_bf16 v[12:15], v[92:95], v[52:55], v[12:15]\n"
    "ds_read_b64_tr_b16 v[70:71], v131 offset:13824\n"
    "v_mfma_f32_16x16x32_bf16 v[16:19], v[96:99], v[48:51], v[16:19]\n"
    "v_add_u32 v128, 4294868992, v128\n"
    "v_add_u32 v129, 4294868992, v129\n"
    "v_add_u32 v130, 4294868992, v130\n"
    "v_add_u32 v131, 4294868992, v131\n"
    "ds_read_b128 v[40:43], v132 offset:640\n"
    "ds_read_b128 v[44:47], v132 offset:19072\n"
    "v_mfma_f32_16x16x32_bf16 v[20:23], v[96:99], v[52:55], v[20:23]\n"
    "ds_read_b64_tr_b16 v[72:73], v128 offset:0\n"
    "v_mfma_f32_16x16x32_bf16 v[24:27], v[100:103], v[48:51], v[24:27]\n"
    "ds_read_b64_tr_b16 v[74:75], v128 offset:1536\n"
    "v_mfma_f32_16x16x32_bf16 v[28:31], v[100:103], v[52:55], v[28:31]\n"
    "ds_read_b64_tr_b16 v[76:77], v129 offset:0\n"
    "s_waitcnt lgkmcnt(5)\n"
    "v_mfma_f32_16x16x32_bf16 v[0:3], v[56:59], v[32:35], v[0:3]\n"
    "ds_read_b64_tr_b16 v[78:79], v129 offset:1536\n"
    "ds_read_b64_tr_b16 v[80:81], v130 offset:0\n"
    "v_mfma_f32_16x16x32_bf16 v[4:7], v[56:59], v[36:39], v[4:7]\n"
    "ds_read_b64_tr_b16 v[82:83], v130 offset:1536\n"
    "v_mfma_f32_16x16x32_bf16 v[8:11], v[60:63], v[32:35], v[8:11]\n"
    "ds_read_b64_tr_b16 v[84:85], v131 offset:0\n"
    "v_mfma_f32_16x16x32_bf16 v[12:15], v[60:63], v[36:39], v[12:15]\n"
    "ds_read_b64_tr_b16 v[86:87], v131 offset:1536\n"
    "v_mfma_f32_16x16x32_bf16 v[16:19], v[64:67], v[32:35], v[16:19]\n"
    "ds_read_b128 v[48:51], v133 offset:640\n"
    "ds_read_b128 v[52:55], v133 offset:19072\n"
    "v_mfma_f32_16x16x32_bf16 v[20:23], v[64:67], v[36:39], v[20:23]\n"
    "ds_read_b64_tr_b16 v[88:89], v128 offset:12288\n"
    "v_mfma_f32_16x16x32_bf16 v[24:27], v[68:71], v[32:35], v[24:27]\n"
    "ds_read_b64_tr_b16 v[90:91], v128 offset:13824\n"
    "v_mfma_f32_16x16x32_bf16 v[28:31], v[68:71], v[36:39], v[28:31]\n"
    "ds_read_b64_tr_b16 v[92:93], v129 offset:12288\n"
    "s_waitcnt lgkmcnt(5)\n"
    "v_mfma_f32_16x16x32_bf16 v[0:3], v[72:75], v[40:43], v[0:3]\n"
    "ds_read_b64_tr_b16 v[94:95], v129 offset:13824\n"
    "ds_read_b64_tr_b16 v[96:97], v130 offset:12288\n"
    "v_mfma_f32_16x16x32_bf16 v[4:7], v[72:75], v[44:47], v[4:7]\n"
    "ds_read_b64_tr_b16 v[98:99], v130 offset:13824\n"
    "v_mfma_f32_16x16x32_bf16 v[8:11], v[76:79], v[40:43], v[8:11]\n"
    "s_barrier\n"
    "ds_read_b64_tr_b16 v[100:101], v131 offset:12288\n"
    "v_mfma_f32_16x16x32_bf16 v[12:15], v[76:79], v[44:47], v[12:15]\n"
    "ds_read_b64_tr_b16 v[102:103], v131 offset:13824\n"
    "v_mfma_f32_16x16x32_bf16 v[16:19], v[80:83], v[40:43], v[16:19]\n"
    "ds_read_b128 v[32:35], v132 offset:768\n"
    "ds_read_b128 v[36:39], v132 offset:19200\n"
    "v_mfma_f32_16x16x32_bf16 v[20:23], v[80:83], v[44:47], v[20:23]\n"
    "ds_read_b64_tr_b16 v[56:57], v128 offset:24576\n"
    "v_mfma_f32_16x16x32_bf16 v[24:27], v[84:87], v[40:43], v[24:27]\n"
    "ds_read_b64_tr_b16 v[58:59], v128 offset:26112\n"
    "v_mfma_f32_16x16x32_bf16 v[28:31], v[84:87], v[44:47], v[28:31]\n"
    "ds_read_b64_tr_b16 v[60:61], v129 offset:24576\n"
    "s_waitcnt lgkmcnt(5)\n"
    "v_mfma_f32_16x16x32_bf16 v[0:3], v[88:91], v[48:51], v[0:3]\n"
    "ds_read_b64_tr_b16 v[62:63], v129 offset:26112\n"
    "ds_read_b64_tr_b16 v[64:65], v130 offset:24576\n"
    "v_mfma_f32_16x16x32_bf16 v[4:7], v[88:91], v[52:55], v[4:7]\n"
    "ds_read_b64_tr_b16 v[66:67], v130 offset:26112\n"
    "v_mfma_f32_16x16x32_bf16 v[8:11], v[92:95], v[48:51], v[8:11]\n"
    "ds_read_b64_tr_b16 v[68:69], v131 offset:24576\n"
    "v_mfma_f32_16x16x32_bf16 v[12:15], v[92:95], v[52:55], v[12:15]\n"
    "ds_read_b64_tr_b16 v[70:71], v131 offset:26112\n"
    "v_mfma_f32_16x16x32_bf16 v[16:19], v[96:99], v[48:51], v[16:19]\n"
    "ds_read_b128 v[40:43], v133 offset:768\n"
    "ds_read_b128 v[44:47], v133 offset:19200\n"
    "v_mfma_f32_16x16x32_bf16 v[20:23], v[96:99], v[52:55], v[20:23]\n"
    "ds_read_b64_tr_b16 v[72:73], v128 offset:36864\n"
    "v_mfma_f32_16x16x32_bf16 v[24:27], v[100:103], v[48:51], v[24:27]\n"
    "ds_read_b64_tr_b16 v[74:75], v128 offset:38400\n"
    "v_mfma_f32_16x16x32_bf16 v[28:31], v[100:103], v[52:55], v[28:31]\n"
    "ds_read_b64_tr_b16 v[76:77], v129 offset:36864\n"
    "s_waitcnt lgkmcnt(5)\n"
    "v_mfma_f32_16x16x32_bf16 v[0:3], v[56:59], v[32:35], v[0:3]\n"
    "ds_read_b64_tr_b16 v[78:79], v129 offset:38400\n"
    "ds_read_b64_tr_b16 v[80:81], v130 offset:36864\n"
    "v_mfma_f32_16x16x32_bf16 v[4:7], v[56:59], v[36:39], v[4:7]\n"
    "ds_read_b64_tr_b16 v[82:83], v130 offset:38400\n"
    "v_mfma_f32_16x16x32_bf16 v[8:11], v[60:63], v[32:35], v[8:11]\n"
    "s_barrier\n"
    "ds_read_b64_tr_b16 v[84:85], v131 offset:36864\n"
    "v_mfma_f32_16x16x32_bf16 v[12:15], v[60:63], v[36:39], v[12:15]\n"
    "ds_read_b64_tr_b16 v[86:87], v131 offset:38400\n"
    "v_mfma_f32_16x16x32_bf16 v[16:19], v[64:67], v[32:35], v[16:19]\n"
    "v_add_u32 v128, 49152, v128\n"
    "v_add_u32 v129, 49152, v129\n"
    "v_add_u32 v130, 49152, v130\n"
    "v_add_u32 v131, 49152, v131\n"
    "ds_read_b128 v[48:51], v132 offset:896\n"
    "ds_read_b128 v[52:55], v132 offset:19328\n"
    "v_mfma_f32_16x16x32_bf16 v[20:23], v[64:67], v[36:39], v[20:23]\n"
    "ds_read_b64_tr_b16 v[88:89], v128 offset:0\n"
    "v_mfma_f32_16x16x32_bf16 v[24:27], v[68:71], v[32:35], v[24:27]\n"
    "ds_read_b64_tr_b16 v[90:91], v128 offset:1536\n"
    "v_mfma_f32_16x16x32_bf16 v[28:31], v[68:71], v[36:39], v[28:31]\n"
    "ds_read_b64_tr_b16 v[92:93], v129 offset:0\n"
    "s_waitcnt lgkmcnt(5)\n"
    "v_mfma_f32_16x16x32_bf16 v[0:3], v[72:75], v[40:43], v[0:3]\n"
    "ds_read_b64_tr_b16 v[94:95], v129 offset:1536\n"
    "ds_read_b64_tr_b16 v[96:97], v130 offset:0\n"
    "v_mfma_f32_16x16x32_bf16 v[4:7], v[72:75], v[44:47], v[4:7]\n"
    "ds_read_b64_tr_b16 v[98:99], v130 offset:1536\n"
    "v_mfma_f32_16x16x32_bf16 v[8:11], v[76:79], v[40:43], v[8:11]\n"
    "ds_read_b64_tr_b16 v[100:101], v131 offset:0\n"
    "v_mfma_f32_16x16x32_bf16 v[12:15], v[76:79], v[44:47], v[12:15]\n"
    "ds_read_b64_tr_b16 v[102:103], v131 offset:1536\n"
    "v_mfma_f32_16x16x32_bf16 v[16:19], v[80:83], v[40:43], v[16:19]\n"
    "ds_read_b128 v[32:35], v133 offset:896\n"
    "ds_read_b128 v[36:39], v133 offset:19328\n"
    "v_mfma_f32_16x16x32_bf16 v[20:23], v[80:83], v[44:47], v[20:23]\n"
    "ds_read_b64_tr_b16 v[56:57], v128 offset:12288\n"
    "v_mfma_f32_16x16x32_bf16 v[24:27], v[84:87], v[40:43], v[24:27]\n"
    "ds_read_b64_tr_b16 v[58:59], v128 offset:13824\n"
    "v_mfma_f32_16x16x32_bf16 v[28:31], v[84:87], v[44:47], v[28:31]\n"
    "ds_read_b64_tr_b16 v[60:61], v129 offset:12288\n"
    "s_waitcnt lgkmcnt(5)\n"
    "v_mfma_f32_16x16x32_bf16 v[0:3], v[88:91], v[48:51], v[0:3]\n"
    "ds_read_b64_tr_b16 v[62:63], v129 offset:13824\n"
    "ds_read_b64_tr_b16 v[64:65], v130 offset:12288\n"
    "v_mfma_f32_16x16x32_bf16 v[4:7], v[88:91], v[52:55], v[4:7]\n"
    "ds_read_b64_tr_b16 v[66:67], v130 offset:13824\n"
    "v_mfma_f32_16x16x32_bf16 v[8:11], v[92:95], v[48:51], v[8:11]\n"
    "s_barrier\n"
    "ds_read_b64_tr_b16 v[68:69], v131 offset:12288\n"
    "v_mfma_f32_16x16x32_bf16 v[12:15], v[92:95], v[52:55], v[12:15]\n"
    "ds_read_b64_tr_b16 v[70:71], v131 offset:13824\n"
    "v_mfma_f32_16x16x32_bf16 v[16:19], v[96:99], v[48:51], v[16:19]\n"
    "ds_read_b128 v[40:43], v132 offset:1024\n"
    "ds_read_b128 v[44:47], v132 offset:19456\n"
    "v_mfma_f32_16x16x32_bf16 v[20:23], v[96:99], v[52:55], v[20:23]\n"
    "ds_read_b64_tr_b16 v[72:73], v128 offset:24576\n"
    "v_mfma_f32_16x16x32_bf16 v[24:27], v[100:103], v[48:51], v[24:27]\n"
    "ds_read_b64_tr_b16 v[74:75], v128 offset:26112\n"
    "v_mfma_f32_16x16x32_bf16 v[28:31], v[100:103], v[52:55], v[28:31]\n"
    "ds_read_b64_tr_b16 v[76:77], v129 offset:24576\n"
    "s_waitcnt lgkmcnt(5)\n"
    "v_mfma_f32_16x16x32_bf16 v[0:3], v[56:59], v[32:35], v[0:3]\n"
    "ds_read_b64_tr_b16 v[78:79], v129 offset:26112\n"
    "ds_read_b64_tr_b16 v[80:81], v130 offset:24576\n"
    "v_mfma_f32_16x16x32_bf16 v[4:7], v[56:59], v[36:39], v[4:7]\n"
    "ds_read_b64_tr_b16 v[82:83], v130 offset:26112\n"
    "v_mfma_f32_16x16x32_bf16 v[8:11], v[60:63], v[32:35], v[8:11]\n"
    "ds_read_b64_tr_b16 v[84:85], v131 offset:24576\n"
    "v_mfma_f32_16x16x32_bf16 v[12:15], v[60:63], v[36:39], v[12:15]\n"
    "ds_read_b64_tr_b16 v[86:87], v131 offset:26112\n"
    "v_mfma_f32_16x16x32_bf16 v[16:19], v[64:67], v[32:35], v[16:19]\n"
    "ds_read_b128 v[48:51], v133 offset:1024\n"
    "ds_read_b128 v[52:55], v133 offset:19456\n"
    "v_mfma_f32_16x16x32_bf16 v[20:23], v[64:67], v[36:39], v[20:23]\n"
    "ds_read_b64_tr_b16 v[88:89], v128 offset:36864\n"
    "v_mfma_f32_16x16x32_bf16 v[24:27], v[68:71], v[32:35], v[24:27]\n"
    "ds_read_b64_tr_b16 v[90:91], v128 offset:38400\n"
    "v_mfma_f32_16x16x32_bf16 v[28:31], v[68:71], v[36:39], v[28:31]\n"
    "ds_read_b64_tr_b16 v[92:93], v129 offset:36864\n"
    "s_waitcnt lgkmcnt(5)\n"
    "v_mfma_f32_16x16x32_bf16 v[0:3], v[72:75], v[40:43], v[0:3]\n"
    "ds_read_b64_tr_b16 v[94:95], v129 offset:38400\n"
    "ds_read_b64_tr_b16 v[96:97], v130 offset:36864\n"
    "v_mfma_f32_16x16x32_bf16 v[4:7], v[72:75], v[44:47], v[4:7]\n"
    "ds_read_b64_tr_b16 v[98:99], v130 offset:38400\n"
    "v_mfma_f32_16x16x32_bf16 v[8:11], v[76:79], v[40:43], v[8:11]\n"
    "ds_read_b64_tr_b16 v[100:101], v131 offset:36864\n"
    "v_mfma_f32_16x16x32_bf16 v[12:15], v[76:79], v[44:47], v[12:15]\n"
    "ds_read_b64_tr_b16 v[102:103], v131 offset:38400\n"
    "v_mfma_f32_16x16x32_bf16 v[16:19], v[80:83], v[40:43], v[16:19]\n"
    "v_mfma_f32_16x16x32_bf16 v[20:23], v[80:83], v[44:47], v[20:23]\n"
    "v_mfma_f32_16x16x32_bf16 v[24:27], v[84:87], v[40:43], v[24:27]\n"
    "v_mfma_f32_16x16x32_bf16 v[28:31], v[84:87], v[44:47], v[28:31]\n"
    "s_waitcnt lgkmcnt(0)\n"
    "v_mfma_f32_16x16x32_bf16 v[0:3], v[88:91], v[48:51], v[0:3]\n"
    "v_mfma_f32_16x16x32_bf16 v[4:7], v[88:91], v[52:55], v[4:7]\n"
    "v_mfma_f32_16x16x32_bf16 v[8:11], v[92:95], v[48:51], v[8:11]\n"
    "v_mfma_f32_16x16x32_bf16 v[12:15], v[92:95], v[52:55], v[12:15]\n"
    "v_mfma_f32_16x16x32_bf16 v[16:19], v[96:99], v[48:51], v[16:19]\n"
    "v_mfma_f32_16x16x32_bf16 v[20:23], v[96:99], v[52:55], v[20:23]\n"
    "v_mfma_f32_16x16x32_bf16 v[24:27], v[100:103], v[48:51], v[24:27]\n"
    "v_mfma_f32_16x16x32_bf16 v[28:31], v[100:103], v[52:55], v[28:31]\n"
    "s_nop 7\n"
    : "=&{v[0:15]}"(q0), "=&{v[16:31]}"(q1), "+{v[128:131]}"(qad)
    : "{v[132:133]}"(xad)
    : "v32","v33","v34","v35","v36","v37","v38","v39","v40","v41","v42","v43","v44","v45","v46","v47","v48","v49","v50","v51","v52","v53","v54","v55","v56","v57","v58","v59","v60","v61","v62","v63","v64","v65","v66","v67","v68","v69","v70","v71","v72","v73","v74","v75","v76","v77","v78","v79","v80","v81","v82","v83","v84","v85","v86","v87","v88","v89","v90","v91","v92","v93","v94","v95","v96","v97","v98","v99","v100","v101","v102","v103","memory");
    const float ri[2] = {*(const LDSP float*)(uintptr_t)(lds + K1_RI + tok * 4) * QSCALE, *(const LDSP float*)(uintptr_t)(lds + K1_RI + 64 + tok * 4) * QSCALE};
    const rsrc_t rQ = mkr(ws + OFF_Q + (size_t)rt * 36864, 36864);
#pragma unroll
    for (int k = 0; k < 4; ++k) { const int m = k & 1; const f32x16 q = ((k >> 1) ? q1 : q0) * ri[m];
      const auto s0 = __builtin_amdgcn_permlane16_swap(cvtpk(q[4 * m], q[4 * m + 1]), cvtpk(q[8 + 4 * m], q[9 + 4 * m]), false, false);
      const auto s1 = __builtin_amdgcn_permlane16_swap(cvtpk(q[4 * m + 2], q[4 * m + 3]), cvtpk(q[10 + 4 * m], q[11 + 4 * m]), false, false);
      ST16((u32x4{s0[0], s1[0], s0[1], s1[1]}), rQ, tok * 1152 + (g & 1) * 32 + (g >> 1) * 16, m * 16 * 1152 + role * 384 + cw * 128 + (k >> 1) * 64); }
  } else {
    BAR();
    { const int w = cw;
      const int cp = (w == 2) ? ((p4 == 1) ? 2 : 0) : 2 * w, co = (w == 2) ? 32 : 8 * p4;
      const u32 a0 = lds + K1_W + (cp + (q4 & 1)) * K1_CP - 4 * (q4 & 1) + (8 * g + q4) * 36 + co;
      const u32x4 kad = {xa0, xa1, a0, 0u}; f32x8 a8;
      asm volatile(
    "ds_read_b128 v[48:51], v40 offset:0\n"
    "ds_read_b128 v[52:55], v40 offset:18432\n"
    "ds_read_b64_tr_b16 v[56:57], v42 offset:0\n"
    "ds_read_b64_tr_b16 v[58:59], v42 offset:144\n"
    "ds_read_b128 v[60:63], v41 offset:0\n"
    "ds_read_b128 v[64:67], v41 offset:18432\n"
    "ds_read_b64_tr_b16 v[68:69], v42 offset:1152\n"
    "ds_read_b64_tr_b16 v[70:71], v42 offset:1296\n"
    "ds_read_b128 v[72:75], v40 offset:128\n"
    "ds_read_b128 v[76:79], v40 offset:18560\n"
    "ds_read_b64_tr_b16 v[80:81], v42 offset:2304\n"
    "ds_read_b64_tr_b16 v[82:83], v42 offset:2448\n"
    "s_waitcnt lgkmcnt(8)\n"
    "v_mfma_f32_16x16x32_bf16 v[0:3], v[56:59], v[48:51], 0\n"
    "ds_read_b128 v[84:87], v41 offset:128\n"
    "ds_read_b128 v[88:91], v41 offset:18560\n"
    "v_mfma_f32_16x16x32_bf16 v[4:7], v[56:59], v[52:55], 0\n"
    "ds_read_b64_tr_b16 v[92:93], v42 offset:3456\n"
    "ds_read_b64_tr_b16 v[94:95], v42 offset:3600\n"
    "s_waitcnt lgkmcnt(8)\n"
    "v_mfma_f32_16x16x32_bf16 v[0:3], v[68:71], v[60:63], v[0:3]\n"
    "ds_read_b128 v[48:51], v40 offset:256\n"
    "ds_read_b128 v[52:55], v40 offset:18688\n"
    "v_mfma_f32_16x16x32_bf16 v[4:7], v[68:71], v[64:67], v[4:7]\n"
    "ds_read_b64_tr_b16 v[56:57], v42 offset:4608\n"
    "ds_read_b64_tr_b16 v[58:59], v42 offset:4752\n"
    "s_waitcnt lgkmcnt(8)\n"
    "v_mfma_f32_16x16x32_bf16 v[0:3], v[80:83], v[72:75], v[0:3]\n"
    "ds_read_b128 v[60:63], v41 offset:256\n"
    "ds_read_b128 v[64:67], v41 offset:18688\n"
    "v_mfma_f32_16x16x32_bf16 v[4:7], v[80:83], v[76:79], v[4:7]\n"
    "ds_read_b64_tr_b16 v[68:69], v42 offset:5760\n"
    "ds_read_b64_tr_b16 v[70:71], v42 offset:5904\n"
    "s_waitcnt lgkmcnt(8)\n"
    "v_mfma_f32_16x16x32_bf16 v[0:3], v[92:95], v[84:87], v[0:3]\n"
    "ds_read_b128 v[72:75], v40 offset:384\n"
    "ds_read_b128 v[76:79], v40 offset:18816\n"
    "v_mfma_f32_16x16x32_bf16 v[4:7], v[92:95], v[88:91], v[4:7]\n"
    "ds_read_b64_tr_b16 v[80:81], v42 offset:6912\n"
    "ds_read_b64_tr_b16 v[82:83], v42 offset:7056\n"
    "s_waitcnt lgkmcnt(8)\n"
    "v_mfma_f32_16x16x32_bf16 v[0:3], v[56:59], v[48:51], v[0:3]\n"
    "ds_read_b128 v[84:87], v41 offset:384\n"
    "ds_read_b128 v[88:91], v41 offset:18816\n"
    "v_mfma_f32_16x16x32_bf16 v[4:7], v[56:59], v[52:55], v[4:7]\n"
    "ds_read_b64_tr_b16 v[92:93], v42 offset:8064\n"
    "ds_read_b64_tr_b16 v[94:95], v42 offset:8208\n"
    "s_waitcnt lgkmcnt(8)\n"
    "v_mfma_f32_16x16x32_bf16 v[0:3], v[68:71], v[60:63], v[0:3]\n"
    "ds_read_b128 v[48:51], v40 offset:512\n"
    "ds_read_b128 v[52:55], v40 offset:18944\n"
    "v_mfma_f32_16x16x32_bf16 v[4:7], v[68:71], v[64:67], v[4:7]\n"
    "ds_read_b64_tr_b16 v[56:57], v42 offset:9216\n"
    "ds_read_b64_tr_b16 v[58:59], v42 offset:9360\n"
    "s_waitcnt lgkmcnt(8)\n"
    "v_mfma_f32_16x16x32_bf16 v[0:3], v[80:83], v[72:75], v[0:3]\n"
    "ds_read_b128 v[60:63], v41 offset:512\n"
    "ds_read_b128 v[64:67], v41 offset:18944\n"
    "v_mfma_f32_16x16x32_bf16 v[4:7], v[80:83], v[76:79], v[4:7]\n"
    "ds_read_b64_tr_b16 v[68:69], v42 offset:10368\n"
    "ds_read_b64_tr_b16 v[70:71], v42 offset:10512\n"
    "s_waitcnt lgkmcnt(8)\n"
    "v_mfma_f32_16x16x32_bf16 v[0:3], v[92:95], v[84:87], v[0:3]\n"
    "ds_read_b128 v[72:75], v40 offset:640\n"
    "ds_read_b128 v[76:79], v40 offset:19072\n"
    "v_mfma_f32_16x16x32_bf16 v[4:7], v[92:95], v[88:91], v[4:7]\n"
    "ds_read_b64_tr_b16 v[80:81], v42 offset:11520\n"
    "ds_read_b64_tr_b16 v[82:83], v42 offset:11664\n"
    "s_waitcnt lgkmcnt(8)\n"
    "v_mfma_f32_16x16x32_bf16 v[0:3], v[56:59], v[48:51], v[0:3]\n"
    "ds_read_b128 v[84:87], v41 offset:640\n"
    "ds_read_b128 v[88:91], v41 offset:19072\n"
    "v_mfma_f32_16x16x32_bf16 v[4:7], v[56:59], v[52:55], v[4:7]\n"
    "ds_read_b64_tr_b16 v[92:93], v42 offset:12672\n"
    "ds_read_b64_tr_b16 v[94:95], v42 offset:12816\n"
    "s_barrier\n"
    "s_waitcnt lgkmcnt(8)\n"
    "v_mfma_f32_16x16x32_bf16 v[0:3], v[68:71], v[60:63], v[0:3]\n"
    "ds_read_b128 v[48:51], v40 offset:768\n"
    "ds_read_b128 v[52:55], v40 offset:19200\n"
    "v_mfma_f32_16x16x32_bf16 v[4:7], v[68:71], v[64:67], v[4:7]\n"
    "ds_read_b64_tr_b16 v[56:57], v42 offset:13824\n"
    "ds_read_b64_tr_b16 v[58:59], v42 offset:13968\n"
    "s_waitcnt lgkmcnt(8)\n"
    "v_mfma_f32_16x16x32_bf16 v[0:3], v[80:83], v[72:75], v[0:3]\n"
    "ds_read_b128 v[60:63], v41 offset:768\n"
    "ds_read_b128 v[64:67], v41 offset:19200\n"
    "v_mfma_f32_16x16x32_bf16 v[4:7], v[80:83], v[76:79], v[4:7]\n"
    "ds_read_b64_tr_b16 v[68:69], v42 offset:14976\n"
    "ds_read_b64_tr_b16 v[70:71], v42 offset:15120\n"
    "s_waitcnt lgkmcnt(8)\n"
    "v_mfma_f32_16x16x32_bf16 v[0:3], v[92:95], v[84:87], v[0:3]\n"
    "ds_read_b128 v[72:75], v40 offset:896\n"
    "ds_read_b128 v[76:79], v40 offset:19328\n"
    "v_mfma_f32_16x16x32_bf16 v[4:7], v[92:95], v[88:91], v[4:7]\n"
    "ds_read_b64_tr_b16 v[80:81], v42 offset:16128\n"
    "ds_read_b64_tr_b16 v[82:83], v42 offset:16272\n"
    "s_waitcnt lgkmcnt(8)\n"
    "v_mfma_f32_16x16x32_bf16 v[0:3], v[56:59], v[48:51], v[0:3]\n"
    "ds_read_b128 v[84:87], v41 offset:896\n"
    "ds_read_b128 v[88:91], v41 offset:19328\n"
    "v_mfma_f32_16x16x32_bf16 v[4:7], v[56:59], v[52:55], v[4:7]\n"
    "ds_read_b64_tr_b16 v[92:93], v42 offset:17280\n"
    "ds_read_b64_tr_b16 v[94:95], v42 offset:17424\n"
    "s_waitcnt lgkmcnt(8)\n"
    "v_mfma_f32_16x16x32_bf16 v[0:3], v[68:71], v[60:63], v[0:3]\n"
    "ds_read_b128 v[48:51], v40 offset:1024\n"
    "ds_read_b128 v[52:55], v40 offset:19456\n"
    "v_mfma_f32_16x16x32_bf16 v[4:7], v[68:71], v[64:67], v[4:7]\n"
    "ds_read_b64_tr_b16 v[56:57], v42 offset:18432\n"
    "ds_read_b64_tr_b16 v[58:59], v42 offset:18576\n"
    "s_waitcnt lgkmcnt(8)\n"
    "v_mfma_f32_16x16x32_bf16 v[0:3], v[80:83], v[72:75], v[0:3]\n"
    "ds_read_b128 v[60:63], v41 offset:1024\n"
    "ds_read_b128 v[64:67], v41 offset:19456\n"
    "v_mfma_f32_16x16x32_bf16 v[4:7], v[80:83], v[76:79], v[4:7]\n"
    "ds_read_b64_tr_b16 v[68:69], v42 offset:19584\n"
    "ds_read_b64_tr_b16 v[70:71], v42 offset:19728\n"
    "s_waitcnt lgkmcnt(8)\n"
    "v_mfma_f32_16x16x32_bf16 v[0:3], v[92:95], v[84:87], v[0:3]\n"
    "v_mfma_f32_16x16x32_bf16 v[4:7], v[92:95], v[88:91], v[4:7]\n"
    "s_waitcnt lgkmcnt(4)\n"
    "v_mfma_f32_16x16x32_bf16 v[0:3], v[56:59], v[48:51], v[0:3]\n"
    "v_mfma_f32_16x16x32_bf16 v[4:7], v[56:59], v[52:55], v[4:7]\n"
    "s_waitcnt lgkmcnt(0)\n"
    "v_mfma_f32_16x16x32_bf16 v[0:3], v[68:71], v[60:63], v[0:3]\n"
    "v_mfma_f32_16x16x32_bf16 v[4:7], v[68:71], v[64:67], v[4:7]\n"
    "s_nop 7\n"
    : "=&{v[0:7]}"(a8)
    : "{v[40:43]}"(kad)
    : "v48","v49","v50","v51","v52","v53","v54","v55","v56","v57","v58","v59","v60","v61","v62","v63","v64","v65","v66","v67","v68","v69","v70","v71","v72","v73","v74","v75","v76","v77","v78","v79","v80","v81","v82","v83","v84","v85","v86","v87","v88","v89","v90","v91","v92","v93","v94","v95","memory");
      f32x4 acc[2] = {f32x4{a8[0], a8[1], a8[2], a8[3]}, f32x4{a8[4], a8[5], a8[6], a8[7]}};
#pragma unroll
      for (int m = 0; m < 2; ++m) acc[m] *= *(const LDSP float*)(uintptr_t)(lds + K1_RI + (16 * m + tok) * 4);
      char* KV = ws + OFF_KV + (rt >> 3) * CH; const int tt = rt & 7;
      float mx0 = 0.f, mx1 = 0.f;
#pragma unroll
      for (int m = 0; m < 2; ++m) { const int rho = 16 * m + tok; const u32 p0 = cvtpk(acc[m][0], acc[m][1]), p1 = cvtpk(acc[m][2], acc[m][3]);
        char* vb = KV + 9216 + (2 * tt + m) * 640 + (tok >> 3) * 320 + (tok & 7) * 2;
        float n2 = 0.f, av = 0.f;
        if (w == 0) { stw(KV + tt * 1024 + (g >> 1) * 512 + rho * 16 + 8 * (g & 1), (uint64_t)p0 | ((uint64_t)p1 << 32));
          n2 = swapadd32(swapadd16(bflo(p0) * bflo(p0) + bfhi(p0) * bfhi(p0) + bflo(p1) * bflo(p1) + bfhi(p1) * bfhi(p1))); }
        else if (w == 1) { stw(vb + (4 * g) * 16, (u16)p0); stw(vb + (4 * g + 1) * 16, (u16)(p0 >> 16)); stw(vb + (4 * g + 2) * 16, (u16)p1); stw(vb + (4 * g + 3) * 16, (u16)(p1 >> 16));
          av = fabsf(bflo(p0)) + fabsf(bfhi(p0)) + fabsf(bflo(p1)) + fabsf(bfhi(p1));
          if (g < 2 && (tok & 7) == 0) { const uint64_t c = g ? 0ull : 0x3f803f803f803f80ull; stw(vb + (18 + g) * 16, c); stw(vb + (18 + g) * 16 + 8, c); } }
        else { if (g == 0) { stw(KV + 8192 + ((tt >> 2) * 32 + rho) * 16 + 4 * (tt & 3), p0); n2 = bflo(p0) * bflo(p0) + bfhi(p0) * bfhi(p0); }
          if (g == 1) { stw(vb + 16 * 16, (u16)p0); stw(vb + 17 * 16, (u16)(p0 >> 16)); av = fabsf(bflo(p0)) + fabsf(bfhi(p0)); } }
        mx0 = fmaxf(mx0, (n2 <= 3e38f) ? n2 : INFINITY); mx1 = fmaxf(mx1, (av <= 3e38f) ? av : INFINITY); }
      mx0 = wavemax(mx0); mx1 = wavemax(mx1);
      float* km = (float*)(ws + OFF_KVMAX) + rt * 4;
      if (lane == 0) { if (w == 0) stw(km, __float_as_uint(mx0)); else if (w == 1) stw(km + 1, __float_as_uint(mx1)); else { stw(km + 2, __float_as_uint(mx0)); stw(km + 3, __float_as_uint(mx1)); } }
    }
  }
#endif
}

DI int w1_base(int t) { return (t + 0 + (t >= 6) * 102 + (t >= 10) * 68 + (t >= 14) * 68) * 1024; }
DI int w1_step(int t) { return (6 + (t >= 6) * (-2)) * 1024; }
DI int w2_kpos(int ks, int nq) { const int u = ks % 9, d = (ks / 9 - nq) & 3, B = u + 9 + (u >= 3) * 6 + (u >= 5) * 4 + (u >= 7) * 4, H = 3 + (u >= 3) * (-1); return d == 0 ? u : B + (d - 1) * H; }
DI u32 sreg(u32 v) { asm volatile("" : "+s"(v)); return v; }
#define SU(e) sreg(rfl(e))
typedef u32 u32x32 __attribute__((ext_vector_type(32)));
constexpr int KVL = 77824, KVS = 20480, L_MU = KVL + 36864, L_WS = KVL + 2 * KVS, L_SL = 156 * 1024, K2_LDS = L_SL + 4096;

extern "C" __global__ __launch_bounds__(256, 1) void k2_rest(const u16* x, const u16* w1, const u16* w2, char* ws, u16* out) {
#if defined(__HIP_DEVICE_COMPILE__)
  extern __shared__ __attribute__((aligned(16))) char lds_[];
  const u32 lds = (u32)(uintptr_t)(LDSP char*)lds_;
  const int b = blockIdx.x, xc = b & 7, i_ = b >> 3, panel = (xc & 1) * 8 + (i_ >> 2), kq = xc >> 1, nq = i_ & 3, rt = panel * 4 + kq;
  const int tid = threadIdx.x, lane = tid & 63, w = rfl(tid >> 6), tok = lane & 15, g = lane >> 4, r = lane & 31, h = lane >> 5;
  const int ub = (nq >> 1) * 9 + (nq & 1) * 5, us = (nq >> 1) * 9 + 4, hs_ = nq & 1;
  const u32 l16 = lane * 16;
  const u32x4 rws = mkrsrc(ws, (u32)WS_USED);
  {
    const int lr = nq * 8 + 2 * w, token = rt * 32 + lr;
    u32x4 qa, qb, kvm; u32 ea, eb;
    { const u32 qo = (u32)(token * 1152 + r * 36);
      asm volatile("global_load_dwordx4 %0, %1, %2" : "=v"(qa) : "v"(qo + 16 * h), "s"(ws + OFF_Q) : "memory");
      asm volatile("global_load_dwordx4 %0, %1, %2 offset:1152" : "=v"(qb) : "v"(qo + 16 * h), "s"(ws + OFF_Q) : "memory");
      asm volatile("global_load_dword %0, %1, %2 offset:32" : "=v"(ea) : "v"(qo), "s"(ws + OFF_Q) : "memory");
      asm volatile("global_load_dword %0, %1, %2 offset:1184" : "=v"(eb) : "v"(qo), "s"(ws + OFF_Q) : "memory");
      asm volatile("global_load_dwordx4 %0, %1, %2" : "=v"(kvm) : "v"(l16), "s"(ws + OFF_KVMAX) : "memory"); }
#define KV_DMA(c) do { _Pragma("unroll") for (int i = 0; i < 5; ++i) dma16(lds + KVL + ((c) & 3) * KVS + (w * 5 + i) * 1024, l16, rws, (u32)OFF_KV + (c) * CH + (w * 5 + i) * 1024); } while (0)
    KV_DMA(0); KV_DMA(1);
    const int hid = b * 4 + w, hf = (lane >> 2) & 1, rho16 = (((lane >> 3) & 3) * 4 + (lane & 3)) * 16;
    u32x4 rtl = mkrsrc(ws, 0u); u32 tvo = 0, tso = 0, tst = 0, dk[3] = {0x80000000u, 0x80000000u, 0x80000000u};
    if (hid < 432) { const int c = hid / 6, kg0 = (hid % 6) * 12, U = 2 * c + h, t1 = 2 * (U % 9) + hf, stp = w1_step(t1);
      rtl = mkrsrc(w1, D_ * F_ * 2); tvo = (lane >> 3) * 9216 + (lane & 7) * 16; tso = kg0 * 73728 + c * 128; tst = 73728;
      dk[0] = (u32)OFF_W1T + (U / 9) * 331776 + w1_base(t1) + (kg0 >> 2) * stp + rho16; dk[1] = dk[0] + stp; dk[2] = dk[1] + stp;
    } else if (hid < 864) { const int id = hid - 432, c = id / 48, ks0 = (id % 48) * 3, k2 = ks0 / 36, u = 2 * c + h, u9 = u % 9;
      const int n2 = (u / 9) * 2 + (u9 == 4 ? hf : (u9 > 4)), tl = (u9 == 4) ? 8 : 2 * (u9 > 4 ? u9 - 5 : u9) + hf;
      const int pos = (tl == 8) ? 6 : ((tl >> 1) == k2) ? 7 + (tl & 1) : ((tl - 2 * k2 - 2) & 7), stp = pos < 7 ? 7168 : 2048;
      const u32 dl = (u32)OFF_W2T + (k2 * 4 + n2) * 331776 + (pos < 7 ? pos : 252 + pos - 7) * 1024 + rho16;
      rtl = mkrsrc(w2, D_ * F_ * 2); tvo = (lane >> 3) * 1152 + (lane & 7) * 16; tso = ks0 * 36864 + c * 128; tst = 9216;
#pragma unroll
      for (int i = 0; i < 3; ++i) dk[i] = dl + w2_kpos(ks0 % 36 + i, n2) * stp; }
    const u32 slot = lds + L_SL + w * 1024, ta = slot + ((lane & 15) >> 2) * 128 + g * 32 + 8 * (lane & 3);
    const u32 ka = lds + KVL + l16, va = lds + KVL + 9216 + h * 320 + min(r, 19) * 16;
    u32x16 vka = {l16, l16 + w * 5120, tvo, dk[0], dk[1], dk[2], slot + l16, ta, (u32)(r * 72 + h * 16), 0u, 1u, (u32)((lane & 7) * 128), ka, ka + 3 * KVS, va, va + 3 * KVS};
    const u32 owp = (u32)OFF_WOT + (ub + w) * 36864, ows = (u32)OFF_WOT + (us * 2 + hs_) * 18432, ow1 = (u32)OFF_W1T + (kq * 4 + nq) * 331776;
    tso = rfl(tso); tst = rfl(tst); rtl = u32x4{rfl(rtl[0]), rfl(rtl[1]), rfl(rtl[2]), 0x00020000u};
    const u32x8 ska = {SU((u32)OFF_KV), SU(tso), SU(tst), SU(owp), SU(ows), SU((u32)OFF_CT + xc * 128), SU((u32)OFF_CT), SU((u32)OFF_W1T + kq * 1327104 + (i_ * 4 + w) * 1024)};
    const u32x4 skb = {SU(lds + KVL + w * 5120), SU(slot), SU((u32)w), SU(0u)};
    asm volatile("s_waitcnt vmcnt(10)" : "+v"(qa), "+v"(qb), "+v"(ea), "+v"(eb), "+v"(kvm) :: "memory");
    { float na = 0.f, nb = 0.f;
#pragma unroll
      for (int e = 0; e < 4; ++e) { na += bflo(qa[e]) * bflo(qa[e]) + bfhi(qa[e]) * bfhi(qa[e]); nb += bflo(qb[e]) * bflo(qb[e]) + bfhi(qb[e]) * bfhi(qb[e]); }
      na = swapadd32(na) + bflo(ea) * bflo(ea) + bfhi(ea) * bfhi(ea); nb = swapadd32(nb) + bflo(eb) * bflo(eb) + bfhi(eb) * bfhi(eb);
      const float qn = wavemax((na <= 3e38f && nb <= 3e38f) ? fmaxf(na, nb) : INFINITY);
      const bool ok = (__uint_as_float(kvm[1]) <= 1e12f) && (__uint_as_float(kvm[3]) <= 1e12f) && (qn * (__uint_as_float(kvm[0]) + __uint_as_float(kvm[2])) <= 3600.f) && (qn <= 1e30f);
      *(LDSP u32*)(uintptr_t)(slot + lane * 4) = __all(ok) ? 1u : 0u; }
    u32x8 qm2 = {qa[0], qa[1], qa[2], qa[3], qb[0], qb[1], qb[2], qb[3]};
    u32x4 qx2 = {h ? eb : ea, 0u, 0u, h ? eb : ea}; u32x2 xka = {lds + KVL + r * 16, lds + KVL + 3 * KVS + r * 16};
    asm volatile("" : "+{v[128:135]}"(qm2), "+{v[136:139]}"(qx2), "+{v[160:175]}"(vka));
    const u32x4 rx = mkrsrc((const char*)x + (size_t)rt * 36864, 36864); const u32 oxp = (ub + w) * 64;
    u32x4 vx = {(u32)(tok * 1152 + g * 16), (u32)((16 + tok) * 1152 + g * 16), (u32)(((w & 1) * 16 + tok) * 1152 + g * 16 + us * 64 + hs_ * 8), 0u};
    u32x4 rcode; CODE_RSRC(rcode, k2_rest, k2_code_end); const u32 ocode = 24576 + (i_ & 3) * 8192;
    asm volatile("" : "+{v[248:251]}"(vx));
    asm volatile("s_waitcnt vmcnt(%0) lgkmcnt(0)\n\ts_barrier" :: "i"(5) : "memory");
    const bool fast = __all(*(const LDSP u32*)(uintptr_t)(lds + L_SL + (lane & 3) * 1024) == 1u);
    f32x8 rm01; u32x4 rm2x; u32x8 xr; f32x2 m1;
    f32x16 oA, oB; u32x32 wa0, wa1, wa2, wa3; u32x16 wa4;
    if (__builtin_expect(fast, 1)) {
      asm volatile(
    "ds_read_b128 v[176:179], v141 offset:8192\n"
    "ds_read_b128 v[144:147], v172 offset:0\n"
    "ds_read_b128 v[148:151], v172 offset:1024\n"
    "ds_read_b128 v[184:187], v174 offset:0\n"
    "ds_read_b128 v[192:195], v174 offset:640\n"
    "ds_read_b128 v[180:183], v141 offset:8704\n"
    "s_waitcnt lgkmcnt(5)\n"
    "v_mfma_f32_32x32x4_2b_bf16 v[32:63], v[176:177], v[136:137], 0\n"
    "v_mov_b32 v0, 0\n"
    "v_mov_b32 v1, 0\n"
    "v_mov_b32 v2, 0\n"
    "v_mov_b32 v3, 0\n"
    "v_mov_b32 v4, 0\n"
    "v_mov_b32 v5, 0\n"
    "v_mov_b32 v6, 0\n"
    "v_mov_b32 v7, 0\n"
    "v_mov_b32 v8, 0\n"
    "v_mov_b32 v9, 0\n"
    "v_mov_b32 v10, 0\n"
    "v_mov_b32 v11, 0\n"
    "v_mov_b32 v12, 0\n"
    "v_mov_b32 v13, 0\n"
    "v_mov_b32 v14, 0\n"
    "v_mov_b32 v15, 0\n"
    "v_mov_b32 v16, 0\n"
    "s_waitcnt lgkmcnt(4)\n"
    "v_mfma_f32_32x32x4_2b_bf16 v[64:95], v[176:177], v[138:139], 0\n"
    "v_mov_b32 v17, 0\n"
    "v_mov_b32 v18, 0\n"
    "v_mov_b32 v19, 0\n"
    "v_mov_b32 v20, 0\n"
    "v_mov_b32 v21, 0\n"
    "v_mov_b32 v22, 0\n"
    "v_mov_b32 v23, 0\n"
    "v_mov_b32 v24, 0\n"
    "v_mov_b32 v25, 0\n"
    "v_mov_b32 v26, 0\n"
    "v_mov_b32 v27, 0\n"
    "v_mov_b32 v28, 0\n"
    "v_mov_b32 v29, 0\n"
    "v_mov_b32 v30, 0\n"
    "v_mov_b32 v31, 0\n"
    "v_mov_b32 v143, 0xdddddddd\n"
    "v_mfma_f32_32x32x16_bf16 v[32:47], v[144:147], v[128:131], v[32:47]\n"
    "v_mfma_f32_32x32x16_bf16 v[48:63], v[144:147], v[132:135], v[48:63]\n"
    "ds_read_b128 v[144:147], v172 offset:2048\n"
    "s_nop 11\n"
    "v_exp_f32 v32, v32\n"
    "v_exp_f32 v33, v33\n"
    "s_waitcnt lgkmcnt(2)\n"
    "v_exp_f32 v34, v34\n"
    "v_exp_f32 v35, v35\n"
    "v_exp_f32 v36, v36\n"
    "v_exp_f32 v37, v37\n"
    "v_mfma_f32_32x32x16_bf16 v[64:79], v[148:151], v[128:131], v[64:79]\n"
    "s_add_u32 m0, s32, 40960\n"
    "s_add_u32 s40, s24, 38912\n"
    "buffer_load_dwordx4 v161, %[rs], s40 offen offset:0 lds\n"
    "v_exp_f32 v38, v38\n"
    "v_exp_f32 v39, v39\n"
    "v_exp_f32 v40, v40\n"
    "v_exp_f32 v41, v41\n"
    "v_smfmac_f32_32x32x32_bf16 v[0:15], v[184:187], v[32:39], v143\n"
    "ds_read_b128 v[188:191], v174 offset:1280\n"
    "v_exp_f32 v42, v42\n"
    "v_exp_f32 v43, v43\n"
    "v_exp_f32 v44, v44\n"
    "v_exp_f32 v45, v45\n"
    "v_mfma_f32_32x32x16_bf16 v[80:95], v[148:151], v[132:135], v[80:95]\n"
    "ds_read_b128 v[148:151], v172 offset:3072\n"
    "v_exp_f32 v46, v46\n"
    "v_exp_f32 v47, v47\n"
    "v_exp_f32 v48, v48\n"
    "v_exp_f32 v49, v49\n"
    "ds_read_b128 v[196:199], v174 offset:1920\n"
    "v_exp_f32 v50, v50\n"
    "v_exp_f32 v51, v51\n"
    "v_exp_f32 v52, v52\n"
    "v_exp_f32 v53, v53\n"
    "v_mfma_f32_32x32x4_2b_bf16 v[96:127], v[178:179], v[136:137], 0\n"
    "v_exp_f32 v54, v54\n"
    "v_exp_f32 v55, v55\n"
    "v_exp_f32 v56, v56\n"
    "v_exp_f32 v57, v57\n"
    "v_exp_f32 v58, v58\n"
    "v_exp_f32 v59, v59\n"
    "v_exp_f32 v60, v60\n"
    "v_exp_f32 v61, v61\n"
    "v_smfmac_f32_32x32x32_bf16 v[16:31], v[184:187], v[48:55], v143\n"
    "v_exp_f32 v62, v62\n"
    "v_exp_f32 v63, v63\n"
    "v_exp_f32 v64, v64\n"
    "v_exp_f32 v65, v65\n"
    "v_smfmac_f32_32x32x32_bf16 v[0:15], v[192:195], v[40:47], v143\n"
    "s_waitcnt lgkmcnt(0)\n"
    "v_exp_f32 v66, v66\n"
    "v_exp_f32 v67, v67\n"
    "v_exp_f32 v68, v68\n"
    "v_exp_f32 v69, v69\n"
    "v_mfma_f32_32x32x16_bf16 v[96:111], v[144:147], v[128:131], v[96:111]\n"
    "buffer_load_dwordx4 v161, %[rs], s40 offen offset:1024 lds\n"
    "v_exp_f32 v70, v70\n"
    "v_exp_f32 v71, v71\n"
    "v_exp_f32 v72, v72\n"
    "v_exp_f32 v73, v73\n"
    "v_smfmac_f32_32x32x32_bf16 v[0:15], v[188:191], v[64:71], v143\n"
    "ds_read_b128 v[184:187], v174 offset:2560\n"
    "v_exp_f32 v74, v74\n"
    "v_exp_f32 v75, v75\n"
    "v_exp_f32 v76, v76\n"
    "v_exp_f32 v77, v77\n"
    "v_mfma_f32_32x32x16_bf16 v[112:127], v[144:147], v[132:135], v[112:127]\n"
    "ds_read_b128 v[144:147], v172 offset:4096\n"
    "v_exp_f32 v78, v78\n"
    "v_exp_f32 v79, v79\n"
    "v_exp_f32 v80, v80\n"
    "v_exp_f32 v81, v81\n"
    "v_smfmac_f32_32x32x32_bf16 v[16:31], v[192:195], v[56:63], v143\n"
    "ds_read_b128 v[192:195], v174 offset:3200\n"
    "v_exp_f32 v82, v82\n"
    "v_exp_f32 v83, v83\n"
    "v_exp_f32 v84, v84\n"
    "v_exp_f32 v85, v85\n"
    "v_mfma_f32_32x32x4_2b_bf16 v[32:63], v[178:179], v[138:139], 0\n"
    "s_waitcnt vmcnt(2)\n"
    "s_barrier\n"
    "v_exp_f32 v86, v86\n"
    "v_exp_f32 v87, v87\n"
    "v_exp_f32 v88, v88\n"
    "v_exp_f32 v89, v89\n"
    "v_exp_f32 v90, v90\n"
    "v_exp_f32 v91, v91\n"
    "v_exp_f32 v92, v92\n"
    "v_exp_f32 v93, v93\n"
    "v_smfmac_f32_32x32x32_bf16 v[16:31], v[188:191], v[80:87], v143\n"
    "v_exp_f32 v94, v94\n"
    "v_exp_f32 v95, v95\n"
    "v_exp_f32 v96, v96\n"
    "v_exp_f32 v97, v97\n"
    "v_smfmac_f32_32x32x32_bf16 v[0:15], v[196:199], v[72:79], v143\n"
    "s_waitcnt lgkmcnt(0)\n"
    "v_exp_f32 v98, v98\n"
    "v_exp_f32 v99, v99\n"
    "v_exp_f32 v100, v100\n"
    "v_exp_f32 v101, v101\n"
    "v_mfma_f32_32x32x16_bf16 v[32:47], v[148:151], v[128:131], v[32:47]\n"
    "buffer_load_dwordx4 v161, %[rs], s40 offen offset:2048 lds\n"
    "v_exp_f32 v102, v102\n"
    "v_exp_f32 v103, v103\n"
    "v_exp_f32 v104, v104\n"
    "v_exp_f32 v105, v105\n"
    "v_smfmac_f32_32x32x32_bf16 v[0:15], v[184:187], v[96:103], v143\n"
    "ds_read_b128 v[188:191], v174 offset:3840\n"
    "v_exp_f32 v106, v106\n"
    "v_exp_f32 v107, v107\n"
    "v_exp_f32 v108, v108\n"
    "v_exp_f32 v109, v109\n"
    "v_mfma_f32_32x32x16_bf16 v[48:63], v[148:151], v[132:135], v[48:63]\n"
    "ds_read_b128 v[148:151], v172 offset:5120\n"
    "v_exp_f32 v110, v110\n"
    "v_exp_f32 v111, v111\n"
    "v_exp_f32 v112, v112\n"
    "v_exp_f32 v113, v113\n"
    "v_smfmac_f32_32x32x32_bf16 v[16:31], v[196:199], v[88:95], v143\n"
    "ds_read_b128 v[196:199], v174 offset:4480\n"
    "v_exp_f32 v114, v114\n"
    "v_exp_f32 v115, v115\n"
    "v_exp_f32 v116, v116\n"
    "v_exp_f32 v117, v117\n"
    "v_mfma_f32_32x32x4_2b_bf16 v[64:95], v[180:181], v[136:137], 0\n"
    "ds_read_b128 v[176:179], v141 offset:28672\n"
    "v_exp_f32 v118, v118\n"
    "v_exp_f32 v119, v119\n"
    "v_exp_f32 v120, v120\n"
    "v_exp_f32 v121, v121\n"
    "v_exp_f32 v122, v122\n"
    "v_exp_f32 v123, v123\n"
    "v_exp_f32 v124, v124\n"
    "v_exp_f32 v125, v125\n"
    "v_smfmac_f32_32x32x32_bf16 v[16:31], v[184:187], v[112:119], v143\n"
    "v_exp_f32 v126, v126\n"
    "v_exp_f32 v127, v127\n"
    "v_exp_f32 v32, v32\n"
    "v_exp_f32 v33, v33\n"
    "v_smfmac_f32_32x32x32_bf16 v[0:15], v[192:195], v[104:111], v143\n"
    "s_waitcnt lgkmcnt(1)\n"
    "v_exp_f32 v34, v34\n"
    "v_exp_f32 v35, v35\n"
    "v_exp_f32 v36, v36\n"
    "v_exp_f32 v37, v37\n"
    "v_mfma_f32_32x32x16_bf16 v[64:79], v[144:147], v[128:131], v[64:79]\n"
    "buffer_load_dwordx4 v161, %[rs], s40 offen offset:3072 lds\n"
    "v_exp_f32 v38, v38\n"
    "v_exp_f32 v39, v39\n"
    "v_exp_f32 v40, v40\n"
    "v_exp_f32 v41, v41\n"
    "v_smfmac_f32_32x32x32_bf16 v[0:15], v[188:191], v[32:39], v143\n"
    "ds_read_b128 v[184:187], v174 offset:5120\n"
    "v_exp_f32 v42, v42\n"
    "v_exp_f32 v43, v43\n"
    "v_exp_f32 v44, v44\n"
    "v_exp_f32 v45, v45\n"
    "v_mfma_f32_32x32x16_bf16 v[80:95], v[144:147], v[132:135], v[80:95]\n"
    "ds_read_b128 v[144:147], v172 offset:6144\n"
    "v_exp_f32 v46, v46\n"
    "v_exp_f32 v47, v47\n"
    "v_exp_f32 v48, v48\n"
    "v_exp_f32 v49, v49\n"
    "v_smfmac_f32_32x32x32_bf16 v[16:31], v[192:195], v[120:127], v143\n"
    "ds_read_b128 v[192:195], v174 offset:5760\n"
    "v_exp_f32 v50, v50\n"
    "v_exp_f32 v51, v51\n"
    "v_exp_f32 v52, v52\n"
    "v_exp_f32 v53, v53\n"
    "v_mfma_f32_32x32x4_2b_bf16 v[96:127], v[180:181], v[138:139], 0\n"
    "s_mov_b32 s41, s25\n"
    "buffer_load_dwordx4 v[200:203], v162, %[rt], s41 offen\n"
    "v_exp_f32 v54, v54\n"
    "v_exp_f32 v55, v55\n"
    "v_exp_f32 v56, v56\n"
    "v_exp_f32 v57, v57\n"
    "v_exp_f32 v58, v58\n"
    "v_exp_f32 v59, v59\n"
    "v_exp_f32 v60, v60\n"
    "v_exp_f32 v61, v61\n"
    "v_smfmac_f32_32x32x32_bf16 v[16:31], v[188:191], v[48:55], v143\n"
    "v_exp_f32 v62, v62\n"
    "v_exp_f32 v63, v63\n"
    "v_exp_f32 v64, v64\n"
    "v_exp_f32 v65, v65\n"
    "v_smfmac_f32_32x32x32_bf16 v[0:15], v[196:199], v[40:47], v143\n"
    "s_waitcnt lgkmcnt(0)\n"
    "v_exp_f32 v66, v66\n"
    "v_exp_f32 v67, v67\n"
    "v_exp_f32 v68, v68\n"
    "v_exp_f32 v69, v69\n"
    "v_mfma_f32_32x32x16_bf16 v[96:111], v[148:151], v[128:131], v[96:111]\n"
    "s_add_u32 m0, s32, 45056\n"
    "s_add_u32 s40, s24, 43008\n"
    "buffer_load_dwordx4 v161, %[rs], s40 offen offset:0 lds\n"
    "v_exp_f32 v70, v70\n"
    "v_exp_f32 v71, v71\n"
    "v_exp_f32 v72, v72\n"
    "v_exp_f32 v73, v73\n"
    "v_smfmac_f32_32x32x32_bf16 v[0:15], v[184:187], v[64:71], v143\n"
    "ds_read_b128 v[188:191], v174 offset:6400\n"
    "v_exp_f32 v74, v74\n"
    "v_exp_f32 v75, v75\n"
    "v_exp_f32 v76, v76\n"
    "v_exp_f32 v77, v77\n"
    "v_mfma_f32_32x32x16_bf16 v[112:127], v[148:151], v[132:135], v[112:127]\n"
    "ds_read_b128 v[148:151], v172 offset:7168\n"
    "v_exp_f32 v78, v78\n"
    "v_exp_f32 v79, v79\n"
    "v_exp_f32 v80, v80\n"
    "v_exp_f32 v81, v81\n"
    "v_smfmac_f32_32x32x32_bf16 v[16:31], v[196:199], v[56:63], v143\n"
    "ds_read_b128 v[196:199], v174 offset:7040\n"
    "v_exp_f32 v82, v82\n"
    "v_exp_f32 v83, v83\n"
    "v_exp_f32 v84, v84\n"
    "v_exp_f32 v85, v85\n"
    "v_mfma_f32_32x32x4_2b_bf16 v[32:63], v[182:183], v[136:137], 0\n"
    "v_exp_f32 v86, v86\n"
    "v_exp_f32 v87, v87\n"
    "v_exp_f32 v88, v88\n"
    "v_exp_f32 v89, v89\n"
    "v_exp_f32 v90, v90\n"
    "v_exp_f32 v91, v91\n"
    "v_exp_f32 v92, v92\n"
    "v_exp_f32 v93, v93\n"
    "v_smfmac_f32_32x32x32_bf16 v[16:31], v[184:187], v[80:87], v143\n"
    "v_exp_f32 v94, v94\n"
    "v_exp_f32 v95, v95\n"
    "v_exp_f32 v96, v96\n"
    "v_exp_f32 v97, v97\n"
    "v_smfmac_f32_32x32x32_bf16 v[0:15], v[192:195], v[72:79], v143\n"
    "s_waitcnt lgkmcnt(0)\n"
    "v_exp_f32 v98, v98\n"
    "v_exp_f32 v99, v99\n"
    "v_exp_f32 v100, v100\n"
    "v_exp_f32 v101, v101\n"
    "v_mfma_f32_32x32x16_bf16 v[32:47], v[144:147], v[128:131], v[32:47]\n"
    "s_add_u32 m0, s32, 61440\n"
    "s_add_u32 s40, s24, 58368\n"
    "buffer_load_dwordx4 v161, %[rs], s40 offen offset:0 lds\n"
    "v_exp_f32 v102, v102\n"
    "v_exp_f32 v103, v103\n"
    "v_exp_f32 v104, v104\n"
    "v_exp_f32 v105, v105\n"
    "v_smfmac_f32_32x32x32_bf16 v[0:15], v[188:191], v[96:103], v143\n"
    "ds_read_b128 v[184:187], v174 offset:7680\n"
    "v_exp_f32 v106, v106\n"
    "v_exp_f32 v107, v107\n"
    "v_exp_f32 v108, v108\n"
    "v_exp_f32 v109, v109\n"
    "v_mfma_f32_32x32x16_bf16 v[48:63], v[144:147], v[132:135], v[48:63]\n"
    "ds_read_b128 v[144:147], v172 offset:20480\n"
    "v_exp_f32 v110, v110\n"
    "v_exp_f32 v111, v111\n"
    "v_exp_f32 v112, v112\n"
    "v_exp_f32 v113, v113\n"
    "v_smfmac_f32_32x32x32_bf16 v[16:31], v[192:195], v[88:95], v143\n"
    "ds_read_b128 v[192:195], v174 offset:8320\n"
    "v_exp_f32 v114, v114\n"
    "v_exp_f32 v115, v115\n"
    "v_exp_f32 v116, v116\n"
    "v_exp_f32 v117, v117\n"
    "v_mfma_f32_32x32x4_2b_bf16 v[64:95], v[182:183], v[138:139], 0\n"
    "s_add_u32 s41, s41, s26\n"
    "buffer_load_dwordx4 v[204:207], v162, %[rt], s41 offen\n"
    "v_exp_f32 v118, v118\n"
    "v_exp_f32 v119, v119\n"
    "v_exp_f32 v120, v120\n"
    "v_exp_f32 v121, v121\n"
    "v_exp_f32 v122, v122\n"
    "v_exp_f32 v123, v123\n"
    "v_exp_f32 v124, v124\n"
    "v_exp_f32 v125, v125\n"
    "v_smfmac_f32_32x32x32_bf16 v[16:31], v[188:191], v[112:119], v143\n"
    "v_exp_f32 v126, v126\n"
    "v_exp_f32 v127, v127\n"
    "v_exp_f32 v32, v32\n"
    "v_exp_f32 v33, v33\n"
    "v_smfmac_f32_32x32x32_bf16 v[0:15], v[196:199], v[104:111], v143\n"
    "s_waitcnt lgkmcnt(0)\n"
    "v_exp_f32 v34, v34\n"
    "v_exp_f32 v35, v35\n"
    "v_exp_f32 v36, v36\n"
    "v_exp_f32 v37, v37\n"
    "v_mfma_f32_32x32x16_bf16 v[64:79], v[148:151], v[128:131], v[64:79]\n"
    "buffer_load_dwordx4 v161, %[rs], s40 offen offset:1024 lds\n"
    "v_exp_f32 v38, v38\n"
    "v_exp_f32 v39, v39\n"
    "v_exp_f32 v40, v40\n"
    "v_exp_f32 v41, v41\n"
    "v_smfmac_f32_32x32x32_bf16 v[0:15], v[184:187], v[32:39], v143\n"
    "ds_read_b128 v[188:191], v174 offset:8960\n"
    "v_exp_f32 v42, v42\n"
    "v_exp_f32 v43, v43\n"
    "v_exp_f32 v44, v44\n"
    "v_exp_f32 v45, v45\n"
    "v_mfma_f32_32x32x16_bf16 v[80:95], v[148:151], v[132:135], v[80:95]\n"
    "ds_read_b128 v[148:151], v172 offset:21504\n"
    "v_exp_f32 v46, v46\n"
    "v_exp_f32 v47, v47\n"
    "v_exp_f32 v48, v48\n"
    "v_exp_f32 v49, v49\n"
    "v_smfmac_f32_32x32x32_bf16 v[16:31], v[196:199], v[120:127], v143\n"
    "ds_read_b128 v[196:199], v174 offset:9600\n"
    "v_exp_f32 v50, v50\n"
    "v_exp_f32 v51, v51\n"
    "v_exp_f32 v52, v52\n"
    "v_exp_f32 v53, v53\n"
    "v_mfma_f32_32x32x4_2b_bf16 v[96:127], v[176:177], v[136:137], 0\n"
    "ds_read_b128 v[180:183], v141 offset:29184\n"
    "v_exp_f32 v54, v54\n"
    "v_exp_f32 v55, v55\n"
    "v_exp_f32 v56, v56\n"
    "v_exp_f32 v57, v57\n"
    "v_exp_f32 v58, v58\n"
    "v_exp_f32 v59, v59\n"
    "v_exp_f32 v60, v60\n"
    "v_exp_f32 v61, v61\n"
    "v_smfmac_f32_32x32x32_bf16 v[16:31], v[184:187], v[48:55], v143\n"
    "v_exp_f32 v62, v62\n"
    "v_exp_f32 v63, v63\n"
    "v_exp_f32 v64, v64\n"
    "v_exp_f32 v65, v65\n"
    "v_smfmac_f32_32x32x32_bf16 v[0:15], v[192:195], v[40:47], v143\n"
    "s_waitcnt lgkmcnt(1)\n"
    "v_exp_f32 v66, v66\n"
    "v_exp_f32 v67, v67\n"
    "v_exp_f32 v68, v68\n"
    "v_exp_f32 v69, v69\n"
    "v_mfma_f32_32x32x16_bf16 v[96:111], v[144:147], v[128:131], v[96:111]\n"
    "buffer_load_dwordx4 v161, %[rs], s40 offen offset:2048 lds\n"
    "v_exp_f32 v70, v70\n"
    "v_exp_f32 v71, v71\n"
    "v_exp_f32 v72, v72\n"
    "v_exp_f32 v73, v73\n"
    "v_smfmac_f32_32x32x32_bf16 v[0:15], v[188:191], v[64:71], v143\n"
    "ds_read_b128 v[184:187], v174 offset:20480\n"
    "v_exp_f32 v74, v74\n"
    "v_exp_f32 v75, v75\n"
    "v_exp_f32 v76, v76\n"
    "v_exp_f32 v77, v77\n"
    "v_mfma_f32_32x32x16_bf16 v[112:127], v[144:147], v[132:135], v[112:127]\n"
    "ds_read_b128 v[144:147], v172 offset:22528\n"
    "v_exp_f32 v78, v78\n"
    "v_exp_f32 v79, v79\n"
    "v_exp_f32 v80, v80\n"
    "v_exp_f32 v81, v81\n"
    "v_smfmac_f32_32x32x32_bf16 v[16:31], v[192:195], v[56:63], v143\n"
    "ds_read_b128 v[192:195], v174 offset:21120\n"
    "v_exp_f32 v82, v82\n"
    "v_exp_f32 v83, v83\n"
    "v_exp_f32 v84, v84\n"
    "v_exp_f32 v85, v85\n"
    "v_mfma_f32_32x32x4_2b_bf16 v[32:63], v[176:177], v[138:139], 0\n"
    "s_add_u32 s41, s41, s26\n"
    "buffer_load_dwordx4 v[208:211], v162, %[rt], s41 offen\n"
    "v_exp_f32 v86, v86\n"
    "v_exp_f32 v87, v87\n"
    "v_exp_f32 v88, v88\n"
    "v_exp_f32 v89, v89\n"
    "v_exp_f32 v90, v90\n"
    "v_exp_f32 v91, v91\n"
    "v_exp_f32 v92, v92\n"
    "v_exp_f32 v93, v93\n"
    "v_smfmac_f32_32x32x32_bf16 v[16:31], v[188:191], v[80:87], v143\n"
    "v_exp_f32 v94, v94\n"
    "v_exp_f32 v95, v95\n"
    "v_exp_f32 v96, v96\n"
    "v_exp_f32 v97, v97\n"
    "v_smfmac_f32_32x32x32_bf16 v[0:15], v[196:199], v[72:79], v143\n"
    "s_waitcnt lgkmcnt(0)\n"
    "v_exp_f32 v98, v98\n"
    "v_exp_f32 v99, v99\n"
    "v_exp_f32 v100, v100\n"
    "v_exp_f32 v101, v101\n"
    "v_mfma_f32_32x32x16_bf16 v[32:47], v[148:151], v[128:131], v[32:47]\n"
    "buffer_load_dwordx4 v161, %[rs], s40 offen offset:3072 lds\n"
    "v_exp_f32 v102, v102\n"
    "v_exp_f32 v103, v103\n"
    "v_exp_f32 v104, v104\n"
    "v_exp_f32 v105, v105\n"
    "v_smfmac_f32_32x32x32_bf16 v[0:15], v[184:187], v[96:103], v143\n"
    "ds_read_b128 v[188:191], v174 offset:21760\n"
    "v_exp_f32 v106, v106\n"
    "v_exp_f32 v107, v107\n"
    "v_exp_f32 v108, v108\n"
    "v_exp_f32 v109, v109\n"
    "v_mfma_f32_32x32x16_bf16 v[48:63], v[148:151], v[132:135], v[48:63]\n"
    "ds_read_b128 v[148:151], v172 offset:23552\n"
    "v_exp_f32 v110, v110\n"
    "v_exp_f32 v111, v111\n"
    "v_exp_f32 v112, v112\n"
    "v_exp_f32 v113, v113\n"
    "v_smfmac_f32_32x32x32_bf16 v[16:31], v[196:199], v[88:95], v143\n"
    "ds_read_b128 v[196:199], v174 offset:22400\n"
    "v_exp_f32 v114, v114\n"
    "v_exp_f32 v115, v115\n"
    "v_exp_f32 v116, v116\n"
    "v_exp_f32 v117, v117\n"
    "v_mfma_f32_32x32x4_2b_bf16 v[64:95], v[178:179], v[136:137], 0\n"
    "v_exp_f32 v118, v118\n"
    "v_exp_f32 v119, v119\n"
    "v_exp_f32 v120, v120\n"
    "v_exp_f32 v121, v121\n"
    "v_exp_f32 v122, v122\n"
    "v_exp_f32 v123, v123\n"
    "v_exp_f32 v124, v124\n"
    "v_exp_f32 v125, v125\n"
    "v_smfmac_f32_32x32x32_bf16 v[16:31], v[184:187], v[112:119], v143\n"
    "v_exp_f32 v126, v126\n"
    "v_exp_f32 v127, v127\n"
    "v_exp_f32 v32, v32\n"
    "v_exp_f32 v33, v33\n"
    "v_smfmac_f32_32x32x32_bf16 v[0:15], v[192:195], v[104:111], v143\n"
    "s_waitcnt lgkmcnt(0)\n"
    "v_exp_f32 v34, v34\n"
    "v_exp_f32 v35, v35\n"
    "v_exp_f32 v36, v36\n"
    "v_exp_f32 v37, v37\n"
    "v_mfma_f32_32x32x16_bf16 v[64:79], v[144:147], v[128:131], v[64:79]\n"
    "s_add_u32 m0, s32, 65536\n"
    "s_add_u32 s40, s24, 62464\n"
    "buffer_load_dwordx4 v161, %[rs], s40 offen offset:0 lds\n"
    "v_exp_f32 v38, v38\n"
    "v_exp_f32 v39, v39\n"
    "v_exp_f32 v40, v40\n"
    "v_exp_f32 v41, v41\n"
    "v_smfmac_f32_32x32x32_bf16 v[0:15], v[188:191], v[32:39], v143\n"
    "ds_read_b128 v[184:187], v174 offset:23040\n"
    "v_exp_f32 v42, v42\n"
    "v_exp_f32 v43, v43\n"
    "v_exp_f32 v44, v44\n"
    "v_exp_f32 v45, v45\n"
    "v_mfma_f32_32x32x16_bf16 v[80:95], v[144:147], v[132:135], v[80:95]\n"
    "ds_read_b128 v[144:147], v172 offset:24576\n"
    "v_exp_f32 v46, v46\n"
    "v_exp_f32 v47, v47\n"
    "v_exp_f32 v48, v48\n"
    "v_exp_f32 v49, v49\n"
    "v_smfmac_f32_32x32x32_bf16 v[16:31], v[192:195], v[120:127], v143\n"
    "ds_read_b128 v[192:195], v174 offset:23680\n"
    "v_exp_f32 v50, v50\n"
    "v_exp_f32 v51, v51\n"
    "v_exp_f32 v52, v52\n"
    "v_exp_f32 v53, v53\n"
    "v_mfma_f32_32x32x4_2b_bf16 v[96:127], v[178:179], v[138:139], 0\n"
    "s_waitcnt vmcnt(7)\n"
    "s_barrier\n"
    "v_exp_f32 v54, v54\n"
    "v_exp_f32 v55, v55\n"
    "v_exp_f32 v56, v56\n"
    "v_exp_f32 v57, v57\n"
    "v_exp_f32 v58, v58\n"
    "v_exp_f32 v59, v59\n"
    "v_exp_f32 v60, v60\n"
    "v_exp_f32 v61, v61\n"
    "v_smfmac_f32_32x32x32_bf16 v[16:31], v[188:191], v[48:55], v143\n"
    "s_add_u32 m0, s32, 0\n"
    "s_add_u32 s40, s24, 77824\n"
    "buffer_load_dwordx4 v161, %[rs], s40 offen offset:0 lds\n"
    "v_exp_f32 v62, v62\n"
    "v_exp_f32 v63, v63\n"
    "v_exp_f32 v64, v64\n"
    "v_exp_f32 v65, v65\n"
    "v_smfmac_f32_32x32x32_bf16 v[0:15], v[196:199], v[40:47], v143\n"
    "s_waitcnt lgkmcnt(0)\n"
    "v_exp_f32 v66, v66\n"
    "v_exp_f32 v67, v67\n"
    "v_exp_f32 v68, v68\n"
    "v_exp_f32 v69, v69\n"
    "v_mfma_f32_32x32x16_bf16 v[96:111], v[148:151], v[128:131], v[96:111]\n"
    "buffer_load_dwordx4 v161, %[rs], s40 offen offset:1024 lds\n"
    "v_exp_f32 v70, v70\n"
    "v_exp_f32 v71, v71\n"
    "v_exp_f32 v72, v72\n"
    "v_exp_f32 v73, v73\n"
    "v_smfmac_f32_32x32x32_bf16 v[0:15], v[184:187], v[64:71], v143\n"
    "ds_read_b128 v[188:191], v174 offset:24320\n"
    "v_exp_f32 v74, v74\n"
    "v_exp_f32 v75, v75\n"
    "v_exp_f32 v76, v76\n"
    "v_exp_f32 v77, v77\n"
    "v_mfma_f32_32x32x16_bf16 v[112:127], v[148:151], v[132:135], v[112:127]\n"
    "ds_read_b128 v[148:151], v172 offset:25600\n"
    "v_exp_f32 v78, v78\n"
    "v_exp_f32 v79, v79\n"
    "v_exp_f32 v80, v80\n"
    "v_exp_f32 v81, v81\n"
    "v_smfmac_f32_32x32x32_bf16 v[16:31], v[196:199], v[56:63], v143\n"
    "ds_read_b128 v[196:199], v174 offset:24960\n"
    "v_exp_f32 v82, v82\n"
    "v_exp_f32 v83, v83\n"
    "v_exp_f32 v84, v84\n"
    "v_exp_f32 v85, v85\n"
    "v_mfma_f32_32x32x4_2b_bf16 v[32:63], v[180:181], v[136:137], 0\n"
    "ds_read_b128 v[176:179], v141 offset:49152\n"
    "v_exp_f32 v86, v86\n"
    "v_exp_f32 v87, v87\n"
    "v_exp_f32 v88, v88\n"
    "v_exp_f32 v89, v89\n"
    "v_exp_f32 v90, v90\n"
    "v_exp_f32 v91, v91\n"
    "v_exp_f32 v92, v92\n"
    "v_exp_f32 v93, v93\n"
    "v_smfmac_f32_32x32x32_bf16 v[16:31], v[184:187], v[80:87], v143\n"
    "s_add_u32 s41, s41, s26\n"
    "buffer_load_dwordx4 v[212:215], v162, %[rt], s41 offen\n"
    "v_exp_f32 v94, v94\n"
    "v_exp_f32 v95, v95\n"
    "v_exp_f32 v96, v96\n"
    "v_exp_f32 v97, v97\n"
    "v_smfmac_f32_32x32x32_bf16 v[0:15], v[192:195], v[72:79], v143\n"
    "s_waitcnt lgkmcnt(1)\n"
    "v_exp_f32 v98, v98\n"
    "v_exp_f32 v99, v99\n"
    "v_exp_f32 v100, v100\n"
    "v_exp_f32 v101, v101\n"
    "v_mfma_f32_32x32x16_bf16 v[32:47], v[144:147], v[128:131], v[32:47]\n"
    "buffer_load_dwordx4 v161, %[rs], s40 offen offset:2048 lds\n"
    "v_exp_f32 v102, v102\n"
    "v_exp_f32 v103, v103\n"
    "v_exp_f32 v104, v104\n"
    "v_exp_f32 v105, v105\n"
    "v_smfmac_f32_32x32x32_bf16 v[0:15], v[188:191], v[96:103], v143\n"
    "ds_read_b128 v[184:187], v174 offset:25600\n"
    "v_exp_f32 v106, v106\n"
    "v_exp_f32 v107, v107\n"
    "v_exp_f32 v108, v108\n"
    "v_exp_f32 v109, v109\n"
    "v_mfma_f32_32x32x16_bf16 v[48:63], v[144:147], v[132:135], v[48:63]\n"
    "ds_read_b128 v[144:147], v172 offset:26624\n"
    "v_exp_f32 v110, v110\n"
    "v_exp_f32 v111, v111\n"
    "v_exp_f32 v112, v112\n"
    "v_exp_f32 v113, v113\n"
    "v_smfmac_f32_32x32x32_bf16 v[16:31], v[192:195], v[88:95], v143\n"
    "ds_read_b128 v[192:195], v174 offset:26240\n"
    "v_exp_f32 v114, v114\n"
    "v_exp_f32 v115, v115\n"
    "v_exp_f32 v116, v116\n"
    "v_exp_f32 v117, v117\n"
    "v_mfma_f32_32x32x4_2b_bf16 v[64:95], v[180:181], v[138:139], 0\n"
    "s_add_u32 s41, s41, s26\n"
    "buffer_load_dwordx4 v[216:219], v162, %[rt], s41 offen\n"
    "v_exp_f32 v118, v118\n"
    "v_exp_f32 v119, v119\n"
    "v_exp_f32 v120, v120\n"
    "v_exp_f32 v121, v121\n"
    "v_exp_f32 v122, v122\n"
    "v_exp_f32 v123, v123\n"
    "v_exp_f32 v124, v124\n"
    "v_exp_f32 v125, v125\n"
    "v_smfmac_f32_32x32x32_bf16 v[16:31], v[188:191], v[112:119], v143\n"
    "buffer_load_dwordx4 a[144:147], v168, %[rs], %[orm] offen\n"
    "v_exp_f32 v126, v126\n"
    "v_exp_f32 v127, v127\n"
    "v_exp_f32 v32, v32\n"
    "v_exp_f32 v33, v33\n"
    "v_smfmac_f32_32x32x32_bf16 v[0:15], v[196:199], v[104:111], v143\n"
    "s_waitcnt lgkmcnt(0)\n"
    "v_exp_f32 v34, v34\n"
    "v_exp_f32 v35, v35\n"
    "v_exp_f32 v36, v36\n"
    "v_exp_f32 v37, v37\n"
    "v_mfma_f32_32x32x16_bf16 v[64:79], v[148:151], v[128:131], v[64:79]\n"
    "buffer_load_dwordx4 v161, %[rs], s40 offen offset:3072 lds\n"
    "v_exp_f32 v38, v38\n"
    "v_exp_f32 v39, v39\n"
    "v_exp_f32 v40, v40\n"
    "v_exp_f32 v41, v41\n"
    "v_smfmac_f32_32x32x32_bf16 v[0:15], v[184:187], v[32:39], v143\n"
    "ds_read_b128 v[188:191], v174 offset:26880\n"
    "v_exp_f32 v42, v42\n"
    "v_exp_f32 v43, v43\n"
    "v_exp_f32 v44, v44\n"
    "v_exp_f32 v45, v45\n"
    "v_mfma_f32_32x32x16_bf16 v[80:95], v[148:151], v[132:135], v[80:95]\n"
    "ds_read_b128 v[148:151], v172 offset:27648\n"
    "v_exp_f32 v46, v46\n"
    "v_exp_f32 v47, v47\n"
    "v_exp_f32 v48, v48\n"
    "v_exp_f32 v49, v49\n"
    "v_smfmac_f32_32x32x32_bf16 v[16:31], v[196:199], v[120:127], v143\n"
    "ds_read_b128 v[196:199], v174 offset:27520\n"
    "v_exp_f32 v50, v50\n"
    "v_exp_f32 v51, v51\n"
    "v_exp_f32 v52, v52\n"
    "v_exp_f32 v53, v53\n"
    "v_mfma_f32_32x32x4_2b_bf16 v[96:127], v[182:183], v[136:137], 0\n"
    "buffer_load_dwordx4 a[148:151], v168, %[rs], %[orm] offen offset:32\n"
    "v_exp_f32 v54, v54\n"
    "v_exp_f32 v55, v55\n"
    "v_exp_f32 v56, v56\n"
    "v_exp_f32 v57, v57\n"
    "v_exp_f32 v58, v58\n"
    "v_exp_f32 v59, v59\n"
    "v_exp_f32 v60, v60\n"
    "v_exp_f32 v61, v61\n"
    "v_smfmac_f32_32x32x32_bf16 v[16:31], v[184:187], v[48:55], v143\n"
    "buffer_load_dwordx2 a[152:153], v168, %[rs], %[orm] offen offset:64\n"
    "v_exp_f32 v62, v62\n"
    "v_exp_f32 v63, v63\n"
    "v_exp_f32 v64, v64\n"
    "v_exp_f32 v65, v65\n"
    "v_smfmac_f32_32x32x32_bf16 v[0:15], v[192:195], v[40:47], v143\n"
    "s_waitcnt lgkmcnt(0)\n"
    "v_exp_f32 v66, v66\n"
    "v_exp_f32 v67, v67\n"
    "v_exp_f32 v68, v68\n"
    "v_exp_f32 v69, v69\n"
    "v_mfma_f32_32x32x16_bf16 v[96:111], v[144:147], v[128:131], v[96:111]\n"
    "s_add_u32 m0, s32, 4096\n"
    "s_add_u32 s40, s24, 81920\n"
    "buffer_load_dwordx4 v161, %[rs], s40 offen offset:0 lds\n"
    "v_exp_f32 v70, v70\n"
    "v_exp_f32 v71, v71\n"
    "v_exp_f32 v72, v72\n"
    "v_exp_f32 v73, v73\n"
    "v_smfmac_f32_32x32x32_bf16 v[0:15], v[188:191], v[64:71], v143\n"
    "ds_read_b128 v[184:187], v174 offset:28160\n"
    "v_exp_f32 v74, v74\n"
    "v_exp_f32 v75, v75\n"
    "v_exp_f32 v76, v76\n"
    "v_exp_f32 v77, v77\n"
    "v_mfma_f32_32x32x16_bf16 v[112:127], v[144:147], v[132:135], v[112:127]\n"
    "ds_read_b128 v[144:147], v172 offset:40960\n"
    "v_exp_f32 v78, v78\n"
    "v_exp_f32 v79, v79\n"
    "v_exp_f32 v80, v80\n"
    "v_exp_f32 v81, v81\n"
    "v_smfmac_f32_32x32x32_bf16 v[16:31], v[192:195], v[56:63], v143\n"
    "ds_read_b128 v[192:195], v174 offset:28800\n"
    "v_exp_f32 v82, v82\n"
    "v_exp_f32 v83, v83\n"
    "v_exp_f32 v84, v84\n"
    "v_exp_f32 v85, v85\n"
    "v_mfma_f32_32x32x4_2b_bf16 v[32:63], v[182:183], v[138:139], 0\n"
    "s_add_u32 s41, s41, s26\n"
    "buffer_load_dwordx4 v[220:223], v162, %[rt], s41 offen\n"
    "v_exp_f32 v86, v86\n"
    "v_exp_f32 v87, v87\n"
    "v_exp_f32 v88, v88\n"
    "v_exp_f32 v89, v89\n"
    "v_exp_f32 v90, v90\n"
    "v_exp_f32 v91, v91\n"
    "v_exp_f32 v92, v92\n"
    "v_exp_f32 v93, v93\n"
    "v_smfmac_f32_32x32x32_bf16 v[16:31], v[188:191], v[80:87], v143\n"
    "buffer_load_dwordx4 a[156:159], v248, %[rx], %[ox] offen\n"
    "v_exp_f32 v94, v94\n"
    "v_exp_f32 v95, v95\n"
    "v_exp_f32 v96, v96\n"
    "v_exp_f32 v97, v97\n"
    "v_smfmac_f32_32x32x32_bf16 v[0:15], v[196:199], v[72:79], v143\n"
    "s_waitcnt lgkmcnt(0)\n"
    "v_exp_f32 v98, v98\n"
    "v_exp_f32 v99, v99\n"
    "v_exp_f32 v100, v100\n"
    "v_exp_f32 v101, v101\n"
    "v_mfma_f32_32x32x16_bf16 v[32:47], v[148:151], v[128:131], v[32:47]\n"
    "buffer_load_dwordx4 a[160:163], v249, %[rx], %[ox] offen\n"
    "v_exp_f32 v102, v102\n"
    "v_exp_f32 v103, v103\n"
    "v_exp_f32 v104, v104\n"
    "v_exp_f32 v105, v105\n"
    "v_smfmac_f32_32x32x32_bf16 v[0:15], v[184:187], v[96:103], v143\n"
    "ds_read_b128 v[188:191], v174 offset:29440\n"
    "v_exp_f32 v106, v106\n"
    "v_exp_f32 v107, v107\n"
    "v_exp_f32 v108, v108\n"
    "v_exp_f32 v109, v109\n"
    "v_mfma_f32_32x32x16_bf16 v[48:63], v[148:151], v[132:135], v[48:63]\n"
    "ds_read_b128 v[148:151], v172 offset:41984\n"
    "v_exp_f32 v110, v110\n"
    "v_exp_f32 v111, v111\n"
    "v_exp_f32 v112, v112\n"
    "v_exp_f32 v113, v113\n"
    "v_smfmac_f32_32x32x32_bf16 v[16:31], v[196:199], v[88:95], v143\n"
    "ds_read_b128 v[196:199], v174 offset:30080\n"
    "v_exp_f32 v114, v114\n"
    "v_exp_f32 v115, v115\n"
    "v_exp_f32 v116, v116\n"
    "v_exp_f32 v117, v117\n"
    "v_mfma_f32_32x32x4_2b_bf16 v[64:95], v[176:177], v[136:137], 0\n"
    "ds_read_b128 v[180:183], v141 offset:49664\n"
    "v_exp_f32 v118, v118\n"
    "v_exp_f32 v119, v119\n"
    "v_exp_f32 v120, v120\n"
    "v_exp_f32 v121, v121\n"
    "v_exp_f32 v122, v122\n"
    "v_exp_f32 v123, v123\n"
    "v_exp_f32 v124, v124\n"
    "v_exp_f32 v125, v125\n"
    "v_smfmac_f32_32x32x32_bf16 v[16:31], v[184:187], v[112:119], v143\n"
    "v_exp_f32 v126, v126\n"
    "v_exp_f32 v127, v127\n"
    "v_exp_f32 v32, v32\n"
    "v_exp_f32 v33, v33\n"
    "v_smfmac_f32_32x32x32_bf16 v[0:15], v[192:195], v[104:111], v143\n"
    "s_waitcnt lgkmcnt(1)\n"
    "v_exp_f32 v34, v34\n"
    "v_exp_f32 v35, v35\n"
    "v_exp_f32 v36, v36\n"
    "v_exp_f32 v37, v37\n"
    "v_mfma_f32_32x32x16_bf16 v[64:79], v[144:147], v[128:131], v[64:79]\n"
    "s_add_u32 s41, s41, s26\n"
    "buffer_load_dwordx4 v[224:227], v162, %[rt], s41 offen\n"
    "v_exp_f32 v38, v38\n"
    "v_exp_f32 v39, v39\n"
    "v_exp_f32 v40, v40\n"
    "v_exp_f32 v41, v41\n"
    "v_smfmac_f32_32x32x32_bf16 v[0:15], v[188:191], v[32:39], v143\n"
    "ds_read_b128 v[184:187], v174 offset:40960\n"
    "v_exp_f32 v42, v42\n"
    "v_exp_f32 v43, v43\n"
    "v_exp_f32 v44, v44\n"
    "v_exp_f32 v45, v45\n"
    "v_mfma_f32_32x32x16_bf16 v[80:95], v[144:147], v[132:135], v[80:95]\n"
    "ds_read_b128 v[144:147], v172 offset:43008\n"
    "v_exp_f32 v46, v46\n"
    "v_exp_f32 v47, v47\n"
    "v_exp_f32 v48, v48\n"
    "v_exp_f32 v49, v49\n"
    "v_smfmac_f32_32x32x32_bf16 v[16:31], v[192:195], v[120:127], v143\n"
    "ds_read_b128 v[192:195], v174 offset:41600\n"
    "v_exp_f32 v50, v50\n"
    "v_exp_f32 v51, v51\n"
    "v_exp_f32 v52, v52\n"
    "v_exp_f32 v53, v53\n"
    "v_mfma_f32_32x32x4_2b_bf16 v[96:127], v[176:177], v[138:139], 0\n"
    "buffer_load_dwordx2 a[154:155], v250, %[rx], 0 offen\n"
    "v_exp_f32 v54, v54\n"
    "v_exp_f32 v55, v55\n"
    "v_exp_f32 v56, v56\n"
    "v_exp_f32 v57, v57\n"
    "v_exp_f32 v58, v58\n"
    "v_exp_f32 v59, v59\n"
    "v_exp_f32 v60, v60\n"
    "v_exp_f32 v61, v61\n"
    "v_smfmac_f32_32x32x32_bf16 v[16:31], v[188:191], v[48:55], v143\n"
    "v_exp_f32 v62, v62\n"
    "v_exp_f32 v63, v63\n"
    "v_exp_f32 v64, v64\n"
    "v_exp_f32 v65, v65\n"
    "v_smfmac_f32_32x32x32_bf16 v[0:15], v[196:199], v[40:47], v143\n"
    "s_waitcnt lgkmcnt(0)\n"
    "v_exp_f32 v66, v66\n"
    "v_exp_f32 v67, v67\n"
    "v_exp_f32 v68, v68\n"
    "v_exp_f32 v69, v69\n"
    "v_mfma_f32_32x32x16_bf16 v[96:111], v[148:151], v[128:131], v[96:111]\n"
    "buffer_load_dwordx2 a[164:165], v169, %[rs], %[om] offen\n"
    "v_exp_f32 v70, v70\n"
    "v_exp_f32 v71, v71\n"
    "v_exp_f32 v72, v72\n"
    "v_exp_f32 v73, v73\n"
    "v_smfmac_f32_32x32x32_bf16 v[0:15], v[184:187], v[64:71], v143\n"
    "ds_read_b128 v[188:191], v174 offset:42240\n"
    "v_exp_f32 v74, v74\n"
    "v_exp_f32 v75, v75\n"
    "v_exp_f32 v76, v76\n"
    "v_exp_f32 v77, v77\n"
    "v_mfma_f32_32x32x16_bf16 v[112:127], v[148:151], v[132:135], v[112:127]\n"
    "ds_read_b128 v[148:151], v172 offset:44032\n"
    "v_exp_f32 v78, v78\n"
    "v_exp_f32 v79, v79\n"
    "v_exp_f32 v80, v80\n"
    "v_exp_f32 v81, v81\n"
    "v_smfmac_f32_32x32x32_bf16 v[16:31], v[196:199], v[56:63], v143\n"
    "ds_read_b128 v[196:199], v174 offset:42880\n"
    "v_exp_f32 v82, v82\n"
    "v_exp_f32 v83, v83\n"
    "v_exp_f32 v84, v84\n"
    "v_exp_f32 v85, v85\n"
    "v_mfma_f32_32x32x4_2b_bf16 v[32:63], v[178:179], v[136:137], 0\n"
    "v_exp_f32 v86, v86\n"
    "v_exp_f32 v87, v87\n"
    "v_exp_f32 v88, v88\n"
    "v_exp_f32 v89, v89\n"
    "v_exp_f32 v90, v90\n"
    "v_exp_f32 v91, v91\n"
    "v_exp_f32 v92, v92\n"
    "v_exp_f32 v93, v93\n"
    "v_smfmac_f32_32x32x32_bf16 v[16:31], v[184:187], v[80:87], v143\n"
    "v_exp_f32 v94, v94\n"
    "v_exp_f32 v95, v95\n"
    "v_exp_f32 v96, v96\n"
    "v_exp_f32 v97, v97\n"
    "v_smfmac_f32_32x32x32_bf16 v[0:15], v[192:195], v[72:79], v143\n"
    "s_waitcnt lgkmcnt(0)\n"
    "v_exp_f32 v98, v98\n"
    "v_exp_f32 v99, v99\n"
    "v_exp_f32 v100, v100\n"
    "v_exp_f32 v101, v101\n"
    "v_mfma_f32_32x32x16_bf16 v[32:47], v[144:147], v[128:131], v[32:47]\n"
    "ds_write_b128 v166, v[200:203]\n"
    "v_exp_f32 v102, v102\n"
    "v_exp_f32 v103, v103\n"
    "v_exp_f32 v104, v104\n"
    "v_exp_f32 v105, v105\n"
    "v_smfmac_f32_32x32x32_bf16 v[0:15], v[188:191], v[96:103], v143\n"
    "ds_read_b128 v[184:187], v174 offset:43520\n"
    "v_exp_f32 v106, v106\n"
    "v_exp_f32 v107, v107\n"
    "v_exp_f32 v108, v108\n"
    "v_exp_f32 v109, v109\n"
    "v_mfma_f32_32x32x16_bf16 v[48:63], v[144:147], v[132:135], v[48:63]\n"
    "ds_read_b128 v[144:147], v172 offset:45056\n"
    "v_exp_f32 v110, v110\n"
    "v_exp_f32 v111, v111\n"
    "v_exp_f32 v112, v112\n"
    "v_exp_f32 v113, v113\n"
    "v_smfmac_f32_32x32x32_bf16 v[16:31], v[192:195], v[88:95], v143\n"
    "ds_read_b128 v[192:195], v174 offset:44160\n"
    "v_exp_f32 v114, v114\n"
    "v_exp_f32 v115, v115\n"
    "v_exp_f32 v116, v116\n"
    "v_exp_f32 v117, v117\n"
    "v_mfma_f32_32x32x4_2b_bf16 v[64:95], v[178:179], v[138:139], 0\n"
    "s_waitcnt vmcnt(16)\n"
    "s_barrier\n"
    "v_exp_f32 v118, v118\n"
    "v_exp_f32 v119, v119\n"
    "v_exp_f32 v120, v120\n"
    "v_exp_f32 v121, v121\n"
    "v_exp_f32 v122, v122\n"
    "v_exp_f32 v123, v123\n"
    "v_exp_f32 v124, v124\n"
    "v_exp_f32 v125, v125\n"
    "v_smfmac_f32_32x32x32_bf16 v[16:31], v[188:191], v[112:119], v143\n"
    "ds_read_b64_tr_b16 v[152:153], v167\n"
    "ds_read_b64_tr_b16 v[154:155], v167 offset:512\n"
    "v_exp_f32 v126, v126\n"
    "v_exp_f32 v127, v127\n"
    "v_exp_f32 v32, v32\n"
    "v_exp_f32 v33, v33\n"
    "v_smfmac_f32_32x32x32_bf16 v[0:15], v[196:199], v[104:111], v143\n"
    "s_waitcnt lgkmcnt(2)\n"
    "v_exp_f32 v34, v34\n"
    "v_exp_f32 v35, v35\n"
    "v_exp_f32 v36, v36\n"
    "v_exp_f32 v37, v37\n"
    "v_mfma_f32_32x32x16_bf16 v[64:79], v[148:151], v[128:131], v[64:79]\n"
    "s_add_u32 m0, s32, 20480\n"
    "s_add_u32 s40, s24, 97280\n"
    "buffer_load_dwordx4 v161, %[rs], s40 offen offset:0 lds\n"
    "v_exp_f32 v38, v38\n"
    "v_exp_f32 v39, v39\n"
    "v_exp_f32 v40, v40\n"
    "v_exp_f32 v41, v41\n"
    "v_smfmac_f32_32x32x32_bf16 v[0:15], v[184:187], v[32:39], v143\n"
    "ds_read_b128 v[188:191], v174 offset:44800\n"
    "v_exp_f32 v42, v42\n"
    "v_exp_f32 v43, v43\n"
    "v_exp_f32 v44, v44\n"
    "v_exp_f32 v45, v45\n"
    "v_mfma_f32_32x32x16_bf16 v[80:95], v[148:151], v[132:135], v[80:95]\n"
    "ds_read_b128 v[148:151], v172 offset:46080\n"
    "v_exp_f32 v46, v46\n"
    "v_exp_f32 v47, v47\n"
    "v_exp_f32 v48, v48\n"
    "v_exp_f32 v49, v49\n"
    "v_smfmac_f32_32x32x32_bf16 v[16:31], v[196:199], v[120:127], v143\n"
    "ds_read_b128 v[196:199], v174 offset:45440\n"
    "v_exp_f32 v50, v50\n"
    "v_exp_f32 v51, v51\n"
    "v_exp_f32 v52, v52\n"
    "v_exp_f32 v53, v53\n"
    "v_mfma_f32_32x32x4_2b_bf16 v[96:127], v[180:181], v[136:137], 0\n"
    "ds_read_b128 v[176:179], v142 offset:8192\n"
    "v_exp_f32 v54, v54\n"
    "v_exp_f32 v55, v55\n"
    "v_exp_f32 v56, v56\n"
    "v_exp_f32 v57, v57\n"
    "v_exp_f32 v58, v58\n"
    "v_exp_f32 v59, v59\n"
    "v_exp_f32 v60, v60\n"
    "v_exp_f32 v61, v61\n"
    "v_smfmac_f32_32x32x32_bf16 v[16:31], v[184:187], v[48:55], v143\n"
    "s_waitcnt lgkmcnt(4)\n"
    "buffer_store_dwordx4 v[152:155], v163, %[rs], 0 offen offset:0 sc1\n"
    "v_exp_f32 v62, v62\n"
    "v_exp_f32 v63, v63\n"
    "v_exp_f32 v64, v64\n"
    "v_exp_f32 v65, v65\n"
    "v_smfmac_f32_32x32x32_bf16 v[0:15], v[192:195], v[40:47], v143\n"
    "s_waitcnt lgkmcnt(1)\n"
    "v_exp_f32 v66, v66\n"
    "v_exp_f32 v67, v67\n"
    "v_exp_f32 v68, v68\n"
    "v_exp_f32 v69, v69\n"
    "v_mfma_f32_32x32x16_bf16 v[96:111], v[144:147], v[128:131], v[96:111]\n"
    "ds_write_b128 v166, v[204:207]\n"
    "v_exp_f32 v70, v70\n"
    "v_exp_f32 v71, v71\n"
    "v_exp_f32 v72, v72\n"
    "v_exp_f32 v73, v73\n"
    "v_smfmac_f32_32x32x32_bf16 v[0:15], v[188:191], v[64:71], v143\n"
    "ds_read_b128 v[184:187], v174 offset:46080\n"
    "v_exp_f32 v74, v74\n"
    "v_exp_f32 v75, v75\n"
    "v_exp_f32 v76, v76\n"
    "v_exp_f32 v77, v77\n"
    "v_mfma_f32_32x32x16_bf16 v[112:127], v[144:147], v[132:135], v[112:127]\n"
    "ds_read_b128 v[144:147], v172 offset:47104\n"
    "v_exp_f32 v78, v78\n"
    "v_exp_f32 v79, v79\n"
    "v_exp_f32 v80, v80\n"
    "v_exp_f32 v81, v81\n"
    "v_smfmac_f32_32x32x32_bf16 v[16:31], v[192:195], v[56:63], v143\n"
    "ds_read_b128 v[192:195], v174 offset:46720\n"
    "v_exp_f32 v82, v82\n"
    "v_exp_f32 v83, v83\n"
    "v_exp_f32 v84, v84\n"
    "v_exp_f32 v85, v85\n"
    "v_mfma_f32_32x32x4_2b_bf16 v[32:63], v[180:181], v[138:139], 0\n"
    "buffer_load_dwordx4 v161, %[rs], s40 offen offset:1024 lds\n"
    "v_exp_f32 v86, v86\n"
    "v_exp_f32 v87, v87\n"
    "v_exp_f32 v88, v88\n"
    "v_exp_f32 v89, v89\n"
    "v_exp_f32 v90, v90\n"
    "v_exp_f32 v91, v91\n"
    "v_exp_f32 v92, v92\n"
    "v_exp_f32 v93, v93\n"
    "v_smfmac_f32_32x32x32_bf16 v[16:31], v[188:191], v[80:87], v143\n"
    "ds_read_b64_tr_b16 v[156:157], v167\n"
    "ds_read_b64_tr_b16 v[158:159], v167 offset:512\n"
    "v_exp_f32 v94, v94\n"
    "v_exp_f32 v95, v95\n"
    "v_exp_f32 v96, v96\n"
    "v_exp_f32 v97, v97\n"
    "v_smfmac_f32_32x32x32_bf16 v[0:15], v[196:199], v[72:79], v143\n"
    "s_waitcnt lgkmcnt(2)\n"
    "v_exp_f32 v98, v98\n"
    "v_exp_f32 v99, v99\n"
    "v_exp_f32 v100, v100\n"
    "v_exp_f32 v101, v101\n"
    "v_mfma_f32_32x32x16_bf16 v[32:47], v[148:151], v[128:131], v[32:47]\n"
    "buffer_load_dwordx4 v161, %[rs], s40 offen offset:2048 lds\n"
    "v_exp_f32 v102, v102\n"
    "v_exp_f32 v103, v103\n"
    "v_exp_f32 v104, v104\n"
    "v_exp_f32 v105, v105\n"
    "v_smfmac_f32_32x32x32_bf16 v[0:15], v[184:187], v[96:103], v143\n"
    "ds_read_b128 v[188:191], v174 offset:47360\n"
    "v_exp_f32 v106, v106\n"
    "v_exp_f32 v107, v107\n"
    "v_exp_f32 v108, v108\n"
    "v_exp_f32 v109, v109\n"
    "v_mfma_f32_32x32x16_bf16 v[48:63], v[148:151], v[132:135], v[48:63]\n"
    "ds_read_b128 v[148:151], v172 offset:48128\n"
    "v_exp_f32 v110, v110\n"
    "v_exp_f32 v111, v111\n"
    "v_exp_f32 v112, v112\n"
    "v_exp_f32 v113, v113\n"
    "v_smfmac_f32_32x32x32_bf16 v[16:31], v[196:199], v[88:95], v143\n"
    "ds_read_b128 v[196:199], v174 offset:48000\n"
    "v_exp_f32 v114, v114\n"
    "v_exp_f32 v115, v115\n"
    "v_exp_f32 v116, v116\n"
    "v_exp_f32 v117, v117\n"
    "v_mfma_f32_32x32x4_2b_bf16 v[64:95], v[182:183], v[136:137], 0\n"
    "buffer_load_dwordx4 v161, %[rs], s40 offen offset:3072 lds\n"
    "v_exp_f32 v118, v118\n"
    "v_exp_f32 v119, v119\n"
    "v_exp_f32 v120, v120\n"
    "v_exp_f32 v121, v121\n"
    "v_exp_f32 v122, v122\n"
    "v_exp_f32 v123, v123\n"
    "v_exp_f32 v124, v124\n"
    "v_exp_f32 v125, v125\n"
    "v_smfmac_f32_32x32x32_bf16 v[16:31], v[184:187], v[112:119], v143\n"
    "s_waitcnt lgkmcnt(3)\n"
    "buffer_store_dwordx4 v[156:159], v163, %[rs], 0 offen offset:256 sc1\n"
    "v_exp_f32 v126, v126\n"
    "v_exp_f32 v127, v127\n"
    "v_exp_f32 v32, v32\n"
    "v_exp_f32 v33, v33\n"
    "v_smfmac_f32_32x32x32_bf16 v[0:15], v[192:195], v[104:111], v143\n"
    "s_waitcnt lgkmcnt(0)\n"
    "v_exp_f32 v34, v34\n"
    "v_exp_f32 v35, v35\n"
    "v_exp_f32 v36, v36\n"
    "v_exp_f32 v37, v37\n"
    "v_mfma_f32_32x32x16_bf16 v[64:79], v[144:147], v[128:131], v[64:79]\n"
    "ds_write_b128 v166, v[208:211]\n"
    "v_exp_f32 v38, v38\n"
    "v_exp_f32 v39, v39\n"
    "v_exp_f32 v40, v40\n"
    "v_exp_f32 v41, v41\n"
    "v_smfmac_f32_32x32x32_bf16 v[0:15], v[188:191], v[32:39], v143\n"
    "ds_read_b128 v[184:187], v174 offset:48640\n"
    "v_exp_f32 v42, v42\n"
    "v_exp_f32 v43, v43\n"
    "v_exp_f32 v44, v44\n"
    "v_exp_f32 v45, v45\n"
    "v_mfma_f32_32x32x16_bf16 v[80:95], v[144:147], v[132:135], v[80:95]\n"
    "ds_read_b128 v[144:147], v173 offset:0\n"
    "v_exp_f32 v46, v46\n"
    "v_exp_f32 v47, v47\n"
    "v_exp_f32 v48, v48\n"
    "v_exp_f32 v49, v49\n"
    "v_smfmac_f32_32x32x32_bf16 v[16:31], v[192:195], v[120:127], v143\n"
    "ds_read_b128 v[192:195], v174 offset:49280\n"
    "v_exp_f32 v50, v50\n"
    "v_exp_f32 v51, v51\n"
    "v_exp_f32 v52, v52\n"
    "v_exp_f32 v53, v53\n"
    "v_mfma_f32_32x32x4_2b_bf16 v[96:127], v[182:183], v[138:139], 0\n"
    "s_add_u32 m0, s32, 24576\n"
    "s_add_u32 s40, s24, 101376\n"
    "buffer_load_dwordx4 v161, %[rs], s40 offen offset:0 lds\n"
    "v_exp_f32 v54, v54\n"
    "v_exp_f32 v55, v55\n"
    "v_exp_f32 v56, v56\n"
    "v_exp_f32 v57, v57\n"
    "v_exp_f32 v58, v58\n"
    "v_exp_f32 v59, v59\n"
    "v_exp_f32 v60, v60\n"
    "v_exp_f32 v61, v61\n"
    "v_smfmac_f32_32x32x32_bf16 v[16:31], v[188:191], v[48:55], v143\n"
    "ds_read_b64_tr_b16 v[152:153], v167\n"
    "ds_read_b64_tr_b16 v[154:155], v167 offset:512\n"
    "v_exp_f32 v62, v62\n"
    "v_exp_f32 v63, v63\n"
    "v_exp_f32 v64, v64\n"
    "v_exp_f32 v65, v65\n"
    "v_smfmac_f32_32x32x32_bf16 v[0:15], v[196:199], v[40:47], v143\n"
    "s_waitcnt lgkmcnt(2)\n"
    "v_exp_f32 v66, v66\n"
    "v_exp_f32 v67, v67\n"
    "v_exp_f32 v68, v68\n"
    "v_exp_f32 v69, v69\n"
    "v_mfma_f32_32x32x16_bf16 v[96:111], v[148:151], v[128:131], v[96:111]\n"
    "s_add_u32 s41, s41, s26\n"
    "buffer_load_dwordx4 v[228:231], v162, %[rt], s41 offen\n"
    "v_exp_f32 v70, v70\n"
    "v_exp_f32 v71, v71\n"
    "v_exp_f32 v72, v72\n"
    "v_exp_f32 v73, v73\n"
    "v_smfmac_f32_32x32x32_bf16 v[0:15], v[184:187], v[64:71], v143\n"
    "ds_read_b128 v[188:191], v174 offset:49920\n"
    "v_exp_f32 v74, v74\n"
    "v_exp_f32 v75, v75\n"
    "v_exp_f32 v76, v76\n"
    "v_exp_f32 v77, v77\n"
    "v_mfma_f32_32x32x16_bf16 v[112:127], v[148:151], v[132:135], v[112:127]\n"
    "ds_read_b128 v[148:151], v173 offset:1024\n"
    "v_exp_f32 v78, v78\n"
    "v_exp_f32 v79, v79\n"
    "v_exp_f32 v80, v80\n"
    "v_exp_f32 v81, v81\n"
    "v_smfmac_f32_32x32x32_bf16 v[16:31], v[196:199], v[56:63], v143\n"
    "ds_read_b128 v[196:199], v174 offset:50560\n"
    "v_exp_f32 v82, v82\n"
    "v_exp_f32 v83, v83\n"
    "v_exp_f32 v84, v84\n"
    "v_exp_f32 v85, v85\n"
    "v_mfma_f32_32x32x4_2b_bf16 v[32:63], v[176:177], v[136:137], 0\n"
    "ds_read_b128 v[180:183], v142 offset:8704\n"
    "v_exp_f32 v86, v86\n"
    "v_exp_f32 v87, v87\n"
    "v_exp_f32 v88, v88\n"
    "v_exp_f32 v89, v89\n"
    "v_exp_f32 v90, v90\n"
    "v_exp_f32 v91, v91\n"
    "v_exp_f32 v92, v92\n"
    "v_exp_f32 v93, v93\n"
    "v_smfmac_f32_32x32x32_bf16 v[16:31], v[184:187], v[80:87], v143\n"
    "s_waitcnt lgkmcnt(4)\n"
    "buffer_store_dwordx4 v[152:155], v163, %[rs], 0 offen offset:512 sc1\n"
    "v_exp_f32 v94, v94\n"
    "v_exp_f32 v95, v95\n"
    "v_exp_f32 v96, v96\n"
    "v_exp_f32 v97, v97\n"
    "v_smfmac_f32_32x32x32_bf16 v[0:15], v[192:195], v[72:79], v143\n"
    "s_waitcnt lgkmcnt(1)\n"
    "v_exp_f32 v98, v98\n"
    "v_exp_f32 v99, v99\n"
    "v_exp_f32 v100, v100\n"
    "v_exp_f32 v101, v101\n"
    "v_mfma_f32_32x32x16_bf16 v[32:47], v[144:147], v[128:131], v[32:47]\n"
    "s_waitcnt vmcnt(22)\n"
    "ds_write_b128 v166, v[212:215]\n"
    "v_exp_f32 v102, v102\n"
    "v_exp_f32 v103, v103\n"
    "v_exp_f32 v104, v104\n"
    "v_exp_f32 v105, v105\n"
    "v_smfmac_f32_32x32x32_bf16 v[0:15], v[188:191], v[96:103], v143\n"
    "ds_read_b128 v[184:187], v175 offset:0\n"
    "v_exp_f32 v106, v106\n"
    "v_exp_f32 v107, v107\n"
    "v_exp_f32 v108, v108\n"
    "v_exp_f32 v109, v109\n"
    "v_mfma_f32_32x32x16_bf16 v[48:63], v[144:147], v[132:135], v[48:63]\n"
    "ds_read_b128 v[144:147], v173 offset:2048\n"
    "v_exp_f32 v110, v110\n"
    "v_exp_f32 v111, v111\n"
    "v_exp_f32 v112, v112\n"
    "v_exp_f32 v113, v113\n"
    "v_smfmac_f32_32x32x32_bf16 v[16:31], v[192:195], v[88:95], v143\n"
    "ds_read_b128 v[192:195], v175 offset:640\n"
    "v_exp_f32 v114, v114\n"
    "v_exp_f32 v115, v115\n"
    "v_exp_f32 v116, v116\n"
    "v_exp_f32 v117, v117\n"
    "v_mfma_f32_32x32x4_2b_bf16 v[64:95], v[176:177], v[138:139], 0\n"
    "s_add_u32 s41, s41, s26\n"
    "buffer_load_dwordx4 v[232:235], v162, %[rt], s41 offen\n"
    "v_exp_f32 v118, v118\n"
    "v_exp_f32 v119, v119\n"
    "v_exp_f32 v120, v120\n"
    "v_exp_f32 v121, v121\n"
    "v_exp_f32 v122, v122\n"
    "v_exp_f32 v123, v123\n"
    "v_exp_f32 v124, v124\n"
    "v_exp_f32 v125, v125\n"
    "v_smfmac_f32_32x32x32_bf16 v[16:31], v[188:191], v[112:119], v143\n"
    "ds_read_b64_tr_b16 v[156:157], v167\n"
    "ds_read_b64_tr_b16 v[158:159], v167 offset:512\n"
    "v_exp_f32 v126, v126\n"
    "v_exp_f32 v127, v127\n"
    "v_exp_f32 v32, v32\n"
    "v_exp_f32 v33, v33\n"
    "v_smfmac_f32_32x32x32_bf16 v[0:15], v[196:199], v[104:111], v143\n"
    "s_waitcnt lgkmcnt(2)\n"
    "v_exp_f32 v34, v34\n"
    "v_exp_f32 v35, v35\n"
    "v_exp_f32 v36, v36\n"
    "v_exp_f32 v37, v37\n"
    "v_mfma_f32_32x32x16_bf16 v[64:79], v[148:151], v[128:131], v[64:79]\n"
    "s_add_u32 s41, s41, s26\n"
    "buffer_load_dwordx4 v[236:239], v162, %[rt], s41 offen\n"
    "v_exp_f32 v38, v38\n"
    "v_exp_f32 v39, v39\n"
    "v_exp_f32 v40, v40\n"
    "v_exp_f32 v41, v41\n"
    "v_smfmac_f32_32x32x32_bf16 v[0:15], v[184:187], v[32:39], v143\n"
    "ds_read_b128 v[188:191], v175 offset:1280\n"
    "v_exp_f32 v42, v42\n"
    "v_exp_f32 v43, v43\n"
    "v_exp_f32 v44, v44\n"
    "v_exp_f32 v45, v45\n"
    "v_mfma_f32_32x32x16_bf16 v[80:95], v[148:151], v[132:135], v[80:95]\n"
    "ds_read_b128 v[148:151], v173 offset:3072\n"
    "v_exp_f32 v46, v46\n"
    "v_exp_f32 v47, v47\n"
    "v_exp_f32 v48, v48\n"
    "v_exp_f32 v49, v49\n"
    "v_smfmac_f32_32x32x32_bf16 v[16:31], v[196:199], v[120:127], v143\n"
    "ds_read_b128 v[196:199], v175 offset:1920\n"
    "v_exp_f32 v50, v50\n"
    "v_exp_f32 v51, v51\n"
    "v_exp_f32 v52, v52\n"
    "v_exp_f32 v53, v53\n"
    "v_mfma_f32_32x32x4_2b_bf16 v[96:127], v[178:179], v[136:137], 0\n"
    "s_add_u32 s41, s41, s26\n"
    "buffer_load_dwordx4 v[240:243], v162, %[rt], s41 offen\n"
    "v_exp_f32 v54, v54\n"
    "v_exp_f32 v55, v55\n"
    "v_exp_f32 v56, v56\n"
    "v_exp_f32 v57, v57\n"
    "v_exp_f32 v58, v58\n"
    "v_exp_f32 v59, v59\n"
    "v_exp_f32 v60, v60\n"
    "v_exp_f32 v61, v61\n"
    "v_smfmac_f32_32x32x32_bf16 v[16:31], v[184:187], v[48:55], v143\n"
    "s_waitcnt lgkmcnt(3)\n"
    "buffer_store_dwordx4 v[156:159], v163, %[rs], 0 offen offset:768 sc1\n"
    "v_exp_f32 v62, v62\n"
    "v_exp_f32 v63, v63\n"
    "v_exp_f32 v64, v64\n"
    "v_exp_f32 v65, v65\n"
    "v_smfmac_f32_32x32x32_bf16 v[0:15], v[192:195], v[40:47], v143\n"
    "s_waitcnt lgkmcnt(0)\n"
    "v_exp_f32 v66, v66\n"
    "v_exp_f32 v67, v67\n"
    "v_exp_f32 v68, v68\n"
    "v_exp_f32 v69, v69\n"
    "v_mfma_f32_32x32x16_bf16 v[96:111], v[144:147], v[128:131], v[96:111]\n"
    "s_waitcnt vmcnt(24)\n"
    "ds_write_b128 v166, v[216:219]\n"
    "v_exp_f32 v70, v70\n"
    "v_exp_f32 v71, v71\n"
    "v_exp_f32 v72, v72\n"
    "v_exp_f32 v73, v73\n"
    "v_smfmac_f32_32x32x32_bf16 v[0:15], v[188:191], v[64:71], v143\n"
    "ds_read_b128 v[184:187], v175 offset:2560\n"
    "v_exp_f32 v74, v74\n"
    "v_exp_f32 v75, v75\n"
    "v_exp_f32 v76, v76\n"
    "v_exp_f32 v77, v77\n"
    "v_mfma_f32_32x32x16_bf16 v[112:127], v[144:147], v[132:135], v[112:127]\n"
    "ds_read_b128 v[144:147], v173 offset:4096\n"
    "v_exp_f32 v78, v78\n"
    "v_exp_f32 v79, v79\n"
    "v_exp_f32 v80, v80\n"
    "v_exp_f32 v81, v81\n"
    "v_smfmac_f32_32x32x32_bf16 v[16:31], v[192:195], v[56:63], v143\n"
    "ds_read_b128 v[192:195], v175 offset:3200\n"
    "v_exp_f32 v82, v82\n"
    "v_exp_f32 v83, v83\n"
    "v_exp_f32 v84, v84\n"
    "v_exp_f32 v85, v85\n"
    "v_mfma_f32_32x32x4_2b_bf16 v[32:63], v[178:179], v[138:139], 0\n"
    "s_waitcnt vmcnt(19)\n"
    "s_barrier\n"
    "v_exp_f32 v86, v86\n"
    "v_exp_f32 v87, v87\n"
    "v_exp_f32 v88, v88\n"
    "v_exp_f32 v89, v89\n"
    "v_exp_f32 v90, v90\n"
    "v_exp_f32 v91, v91\n"
    "v_exp_f32 v92, v92\n"
    "v_exp_f32 v93, v93\n"
    "v_smfmac_f32_32x32x32_bf16 v[16:31], v[188:191], v[80:87], v143\n"
    "ds_read_b64_tr_b16 v[152:153], v167\n"
    "ds_read_b64_tr_b16 v[154:155], v167 offset:512\n"
    "v_exp_f32 v94, v94\n"
    "v_exp_f32 v95, v95\n"
    "v_exp_f32 v96, v96\n"
    "v_exp_f32 v97, v97\n"
    "v_smfmac_f32_32x32x32_bf16 v[0:15], v[196:199], v[72:79], v143\n"
    "s_waitcnt lgkmcnt(2)\n"
    "v_exp_f32 v98, v98\n"
    "v_exp_f32 v99, v99\n"
    "v_exp_f32 v100, v100\n"
    "v_exp_f32 v101, v101\n"
    "v_mfma_f32_32x32x16_bf16 v[32:47], v[148:151], v[128:131], v[32:47]\n"
    "s_add_u32 m0, s32, 40960\n"
    "s_add_u32 s40, s24, 116736\n"
    "buffer_load_dwordx4 v161, %[rs], s40 offen offset:0 lds\n"
    "v_exp_f32 v102, v102\n"
    "v_exp_f32 v103, v103\n"
    "v_exp_f32 v104, v104\n"
    "v_exp_f32 v105, v105\n"
    "v_smfmac_f32_32x32x32_bf16 v[0:15], v[184:187], v[96:103], v143\n"
    "ds_read_b128 v[188:191], v175 offset:3840\n"
    "v_exp_f32 v106, v106\n"
    "v_exp_f32 v107, v107\n"
    "v_exp_f32 v108, v108\n"
    "v_exp_f32 v109, v109\n"
    "v_mfma_f32_32x32x16_bf16 v[48:63], v[148:151], v[132:135], v[48:63]\n"
    "ds_read_b128 v[148:151], v173 offset:5120\n"
    "v_exp_f32 v110, v110\n"
    "v_exp_f32 v111, v111\n"
    "v_exp_f32 v112, v112\n"
    "v_exp_f32 v113, v113\n"
    "v_smfmac_f32_32x32x32_bf16 v[16:31], v[196:199], v[88:95], v143\n"
    "ds_read_b128 v[196:199], v175 offset:4480\n"
    "v_exp_f32 v114, v114\n"
    "v_exp_f32 v115, v115\n"
    "v_exp_f32 v116, v116\n"
    "v_exp_f32 v117, v117\n"
    "v_mfma_f32_32x32x4_2b_bf16 v[64:95], v[180:181], v[136:137], 0\n"
    "ds_read_b128 v[176:179], v141 offset:8192\n"
    "v_exp_f32 v118, v118\n"
    "v_exp_f32 v119, v119\n"
    "v_exp_f32 v120, v120\n"
    "v_exp_f32 v121, v121\n"
    "v_exp_f32 v122, v122\n"
    "v_exp_f32 v123, v123\n"
    "v_exp_f32 v124, v124\n"
    "v_exp_f32 v125, v125\n"
    "v_smfmac_f32_32x32x32_bf16 v[16:31], v[184:187], v[112:119], v143\n"
    "s_waitcnt lgkmcnt(4)\n"
    "buffer_store_dwordx4 v[152:155], v164, %[rs], 0 offen offset:0 sc1\n"
    "v_exp_f32 v126, v126\n"
    "v_exp_f32 v127, v127\n"
    "v_exp_f32 v32, v32\n"
    "v_exp_f32 v33, v33\n"
    "v_smfmac_f32_32x32x32_bf16 v[0:15], v[192:195], v[104:111], v143\n"
    "s_waitcnt lgkmcnt(1)\n"
    "v_exp_f32 v34, v34\n"
    "v_exp_f32 v35, v35\n"
    "v_exp_f32 v36, v36\n"
    "v_exp_f32 v37, v37\n"
    "v_mfma_f32_32x32x16_bf16 v[64:79], v[144:147], v[128:131], v[64:79]\n"
    "s_waitcnt vmcnt(20)\n"
    "ds_write_b128 v166, v[220:223]\n"
    "v_exp_f32 v38, v38\n"
    "v_exp_f32 v39, v39\n"
    "v_exp_f32 v40, v40\n"
    "v_exp_f32 v41, v41\n"
    "v_smfmac_f32_32x32x32_bf16 v[0:15], v[188:191], v[32:39], v143\n"
    "ds_read_b128 v[184:187], v175 offset:5120\n"
    "v_exp_f32 v42, v42\n"
    "v_exp_f32 v43, v43\n"
    "v_exp_f32 v44, v44\n"
    "v_exp_f32 v45, v45\n"
    "v_mfma_f32_32x32x16_bf16 v[80:95], v[144:147], v[132:135], v[80:95]\n"
    "ds_read_b128 v[144:147], v173 offset:6144\n"
    "v_exp_f32 v46, v46\n"
    "v_exp_f32 v47, v47\n"
    "v_exp_f32 v48, v48\n"
    "v_exp_f32 v49, v49\n"
    "v_smfmac_f32_32x32x32_bf16 v[16:31], v[192:195], v[120:127], v143\n"
    "ds_read_b128 v[192:195], v175 offset:5760\n"
    "v_exp_f32 v50, v50\n"
    "v_exp_f32 v51, v51\n"
    "v_exp_f32 v52, v52\n"
    "v_exp_f32 v53, v53\n"
    "v_mfma_f32_32x32x4_2b_bf16 v[96:127], v[180:181], v[138:139], 0\n"
    "buffer_load_dwordx4 v161, %[rs], s40 offen offset:1024 lds\n"
    "v_exp_f32 v54, v54\n"
    "v_exp_f32 v55, v55\n"
    "v_exp_f32 v56, v56\n"
    "v_exp_f32 v57, v57\n"
    "v_exp_f32 v58, v58\n"
    "v_exp_f32 v59, v59\n"
    "v_exp_f32 v60, v60\n"
    "v_exp_f32 v61, v61\n"
    "v_smfmac_f32_32x32x32_bf16 v[16:31], v[188:191], v[48:55], v143\n"
    "ds_read_b64_tr_b16 v[156:157], v167\n"
    "ds_read_b64_tr_b16 v[158:159], v167 offset:512\n"
    "v_exp_f32 v62, v62\n"
    "v_exp_f32 v63, v63\n"
    "v_exp_f32 v64, v64\n"
    "v_exp_f32 v65, v65\n"
    "v_smfmac_f32_32x32x32_bf16 v[0:15], v[196:199], v[40:47], v143\n"
    "s_waitcnt lgkmcnt(2)\n"
    "v_exp_f32 v66, v66\n"
    "v_exp_f32 v67, v67\n"
    "v_exp_f32 v68, v68\n"
    "v_exp_f32 v69, v69\n"
    "v_mfma_f32_32x32x16_bf16 v[96:111], v[148:151], v[128:131], v[96:111]\n"
    "buffer_load_dwordx4 v161, %[rs], s40 offen offset:2048 lds\n"
    "v_exp_f32 v70, v70\n"
    "v_exp_f32 v71, v71\n"
    "v_exp_f32 v72, v72\n"
    "v_exp_f32 v73, v73\n"
    "v_smfmac_f32_32x32x32_bf16 v[0:15], v[184:187], v[64:71], v143\n"
    "ds_read_b128 v[188:191], v175 offset:6400\n"
    "v_exp_f32 v74, v74\n"
    "v_exp_f32 v75, v75\n"
    "v_exp_f32 v76, v76\n"
    "v_exp_f32 v77, v77\n"
    "v_mfma_f32_32x32x16_bf16 v[112:127], v[148:151], v[132:135], v[112:127]\n"
    "ds_read_b128 v[148:151], v173 offset:7168\n"
    "v_exp_f32 v78, v78\n"
    "v_exp_f32 v79, v79\n"
    "v_exp_f32 v80, v80\n"
    "v_exp_f32 v81, v81\n"
    "v_smfmac_f32_32x32x32_bf16 v[16:31], v[196:199], v[56:63], v143\n"
    "ds_read_b128 v[196:199], v175 offset:7040\n"
    "v_exp_f32 v82, v82\n"
    "v_exp_f32 v83, v83\n"
    "v_exp_f32 v84, v84\n"
    "v_exp_f32 v85, v85\n"
    "v_mfma_f32_32x32x4_2b_bf16 v[32:63], v[182:183], v[136:137], 0\n"
    "buffer_load_dwordx4 v161, %[rs], s40 offen offset:3072 lds\n"
    "v_exp_f32 v86, v86\n"
    "v_exp_f32 v87, v87\n"
    "v_exp_f32 v88, v88\n"
    "v_exp_f32 v89, v89\n"
    "v_exp_f32 v90, v90\n"
    "v_exp_f32 v91, v91\n"
    "v_exp_f32 v92, v92\n"
    "v_exp_f32 v93, v93\n"
    "v_smfmac_f32_32x32x32_bf16 v[16:31], v[184:187], v[80:87], v143\n"
    "s_waitcnt lgkmcnt(3)\n"
    "buffer_store_dwordx4 v[156:159], v164, %[rs], 0 offen offset:256 sc1\n"
    "v_exp_f32 v94, v94\n"
    "v_exp_f32 v95, v95\n"
    "v_exp_f32 v96, v96\n"
    "v_exp_f32 v97, v97\n"
    "v_smfmac_f32_32x32x32_bf16 v[0:15], v[192:195], v[72:79], v143\n"
    "s_waitcnt lgkmcnt(0)\n"
    "v_exp_f32 v98, v98\n"
    "v_exp_f32 v99, v99\n"
    "v_exp_f32 v100, v100\n"
    "v_exp_f32 v101, v101\n"
    "v_mfma_f32_32x32x16_bf16 v[32:47], v[144:147], v[128:131], v[32:47]\n"
    "s_waitcnt vmcnt(21)\n"
    "ds_write_b128 v166, v[224:227]\n"
    "v_exp_f32 v102, v102\n"
    "v_exp_f32 v103, v103\n"
    "v_exp_f32 v104, v104\n"
    "v_exp_f32 v105, v105\n"
    "v_smfmac_f32_32x32x32_bf16 v[0:15], v[188:191], v[96:103], v143\n"
    "ds_read_b128 v[184:187], v175 offset:7680\n"
    "v_exp_f32 v106, v106\n"
    "v_exp_f32 v107, v107\n"
    "v_exp_f32 v108, v108\n"
    "v_exp_f32 v109, v109\n"
    "v_mfma_f32_32x32x16_bf16 v[48:63], v[144:147], v[132:135], v[48:63]\n"
    "ds_read_b128 v[144:147], v172 offset:0\n"
    "v_exp_f32 v110, v110\n"
    "v_exp_f32 v111, v111\n"
    "v_exp_f32 v112, v112\n"
    "v_exp_f32 v113, v113\n"
    "v_smfmac_f32_32x32x32_bf16 v[16:31], v[192:195], v[88:95], v143\n"
    "ds_read_b128 v[192:195], v175 offset:8320\n"
    "v_exp_f32 v114, v114\n"
    "v_exp_f32 v115, v115\n"
    "v_exp_f32 v116, v116\n"
    "v_exp_f32 v117, v117\n"
    "v_mfma_f32_32x32x4_2b_bf16 v[64:95], v[182:183], v[138:139], 0\n"
    "s_add_u32 m0, s32, 45056\n"
    "s_add_u32 s40, s24, 120832\n"
    "buffer_load_dwordx4 v161, %[rs], s40 offen offset:0 lds\n"
    "v_exp_f32 v118, v118\n"
    "v_exp_f32 v119, v119\n"
    "v_exp_f32 v120, v120\n"
    "v_exp_f32 v121, v121\n"
    "v_exp_f32 v122, v122\n"
    "v_exp_f32 v123, v123\n"
    "v_exp_f32 v124, v124\n"
    "v_exp_f32 v125, v125\n"
    "v_smfmac_f32_32x32x32_bf16 v[16:31], v[188:191], v[112:119], v143\n"
    "ds_read_b64_tr_b16 v[152:153], v167\n"
    "ds_read_b64_tr_b16 v[154:155], v167 offset:512\n"
    "v_exp_f32 v126, v126\n"
    "v_exp_f32 v127, v127\n"
    "v_exp_f32 v32, v32\n"
    "v_exp_f32 v33, v33\n"
    "v_smfmac_f32_32x32x32_bf16 v[0:15], v[196:199], v[104:111], v143\n"
    "s_waitcnt lgkmcnt(2)\n"
    "v_exp_f32 v34, v34\n"
    "v_exp_f32 v35, v35\n"
    "v_exp_f32 v36, v36\n"
    "v_exp_f32 v37, v37\n"
    "v_mfma_f32_32x32x16_bf16 v[64:79], v[148:151], v[128:131], v[64:79]\n"
    "s_add_u32 s41, s41, s26\n"
    "buffer_load_dwordx4 v[244:247], v162, %[rt], s41 offen\n"
    "v_exp_f32 v38, v38\n"
    "v_exp_f32 v39, v39\n"
    "v_exp_f32 v40, v40\n"
    "v_exp_f32 v41, v41\n"
    "v_smfmac_f32_32x32x32_bf16 v[0:15], v[184:187], v[32:39], v143\n"
    "ds_read_b128 v[188:191], v175 offset:8960\n"
    "v_exp_f32 v42, v42\n"
    "v_exp_f32 v43, v43\n"
    "v_exp_f32 v44, v44\n"
    "v_exp_f32 v45, v45\n"
    "v_mfma_f32_32x32x16_bf16 v[80:95], v[148:151], v[132:135], v[80:95]\n"
    "ds_read_b128 v[148:151], v172 offset:1024\n"
    "v_exp_f32 v46, v46\n"
    "v_exp_f32 v47, v47\n"
    "v_exp_f32 v48, v48\n"
    "v_exp_f32 v49, v49\n"
    "v_smfmac_f32_32x32x32_bf16 v[16:31], v[196:199], v[120:127], v143\n"
    "ds_read_b128 v[196:199], v175 offset:9600\n"
    "v_exp_f32 v50, v50\n"
    "v_exp_f32 v51, v51\n"
    "v_exp_f32 v52, v52\n"
    "v_exp_f32 v53, v53\n"
    "v_mfma_f32_32x32x4_2b_bf16 v[96:127], v[176:177], v[136:137], 0\n"
    "ds_read_b128 v[180:183], v141 offset:8704\n"
    "v_exp_f32 v54, v54\n"
    "v_exp_f32 v55, v55\n"
    "v_exp_f32 v56, v56\n"
    "v_exp_f32 v57, v57\n"
    "v_exp_f32 v58, v58\n"
    "v_exp_f32 v59, v59\n"
    "v_exp_f32 v60, v60\n"
    "v_exp_f32 v61, v61\n"
    "v_smfmac_f32_32x32x32_bf16 v[16:31], v[184:187], v[48:55], v143\n"
    "s_waitcnt lgkmcnt(4)\n"
    "buffer_store_dwordx4 v[152:155], v164, %[rs], 0 offen offset:512 sc1\n"
    "v_exp_f32 v62, v62\n"
    "v_exp_f32 v63, v63\n"
    "v_exp_f32 v64, v64\n"
    "v_exp_f32 v65, v65\n"
    "v_smfmac_f32_32x32x32_bf16 v[0:15], v[192:195], v[40:47], v143\n"
    "s_waitcnt lgkmcnt(1)\n"
    "v_exp_f32 v66, v66\n"
    "v_exp_f32 v67, v67\n"
    "v_exp_f32 v68, v68\n"
    "v_exp_f32 v69, v69\n"
    "v_mfma_f32_32x32x16_bf16 v[96:111], v[144:147], v[128:131], v[96:111]\n"
    "s_waitcnt vmcnt(14)\n"
    "ds_write_b128 v166, v[228:231]\n"
    "v_exp_f32 v70, v70\n"
    "v_exp_f32 v71, v71\n"
    "v_exp_f32 v72, v72\n"
    "v_exp_f32 v73, v73\n"
    "v_smfmac_f32_32x32x32_bf16 v[0:15], v[188:191], v[64:71], v143\n"
    "ds_read_b128 v[184:187], v174 offset:0\n"
    "v_exp_f32 v74, v74\n"
    "v_exp_f32 v75, v75\n"
    "v_exp_f32 v76, v76\n"
    "v_exp_f32 v77, v77\n"
    "v_mfma_f32_32x32x16_bf16 v[112:127], v[144:147], v[132:135], v[112:127]\n"
    "ds_read_b128 v[144:147], v172 offset:2048\n"
    "v_exp_f32 v78, v78\n"
    "v_exp_f32 v79, v79\n"
    "v_exp_f32 v80, v80\n"
    "v_exp_f32 v81, v81\n"
    "v_smfmac_f32_32x32x32_bf16 v[16:31], v[192:195], v[56:63], v143\n"
    "ds_read_b128 v[192:195], v174 offset:640\n"
    "v_exp_f32 v82, v82\n"
    "v_exp_f32 v83, v83\n"
    "v_exp_f32 v84, v84\n"
    "v_exp_f32 v85, v85\n"
    "v_mfma_f32_32x32x4_2b_bf16 v[32:63], v[176:177], v[138:139], 0\n"
    "s_add_u32 s42, s27, 0\n"
    "buffer_load_dwordx4 a[0:3], v160, %[rs], s42 offen offset:0\n"
    "v_exp_f32 v86, v86\n"
    "v_exp_f32 v87, v87\n"
    "v_exp_f32 v88, v88\n"
    "v_exp_f32 v89, v89\n"
    "v_exp_f32 v90, v90\n"
    "v_exp_f32 v91, v91\n"
    "v_exp_f32 v92, v92\n"
    "v_exp_f32 v93, v93\n"
    "v_smfmac_f32_32x32x32_bf16 v[16:31], v[188:191], v[80:87], v143\n"
    "ds_read_b64_tr_b16 v[156:157], v167\n"
    "ds_read_b64_tr_b16 v[158:159], v167 offset:512\n"
    "v_exp_f32 v94, v94\n"
    "v_exp_f32 v95, v95\n"
    "v_exp_f32 v96, v96\n"
    "v_exp_f32 v97, v97\n"
    "v_smfmac_f32_32x32x32_bf16 v[0:15], v[196:199], v[72:79], v143\n"
    "s_waitcnt lgkmcnt(2)\n"
    "v_exp_f32 v98, v98\n"
    "v_exp_f32 v99, v99\n"
    "v_exp_f32 v100, v100\n"
    "v_exp_f32 v101, v101\n"
    "v_mfma_f32_32x32x16_bf16 v[32:47], v[148:151], v[128:131], v[32:47]\n"
    "s_add_u32 s43, s27, 18432\n"
    "buffer_load_dwordx4 a[4:7], v160, %[rs], s43 offen offset:0\n"
    "v_exp_f32 v102, v102\n"
    "v_exp_f32 v103, v103\n"
    "v_exp_f32 v104, v104\n"
    "v_exp_f32 v105, v105\n"
    "v_smfmac_f32_32x32x32_bf16 v[0:15], v[184:187], v[96:103], v143\n"
    "ds_read_b128 v[188:191], v174 offset:1280\n"
    "v_exp_f32 v106, v106\n"
    "v_exp_f32 v107, v107\n"
    "v_exp_f32 v108, v108\n"
    "v_exp_f32 v109, v109\n"
    "v_mfma_f32_32x32x16_bf16 v[48:63], v[148:151], v[132:135], v[48:63]\n"
    "ds_read_b128 v[148:151], v172 offset:3072\n"
    "v_exp_f32 v110, v110\n"
    "v_exp_f32 v111, v111\n"
    "v_exp_f32 v112, v112\n"
    "v_exp_f32 v113, v113\n"
    "v_smfmac_f32_32x32x32_bf16 v[16:31], v[196:199], v[88:95], v143\n"
    "ds_read_b128 v[196:199], v174 offset:1920\n"
    "v_exp_f32 v114, v114\n"
    "v_exp_f32 v115, v115\n"
    "v_exp_f32 v116, v116\n"
    "v_exp_f32 v117, v117\n"
    "v_mfma_f32_32x32x4_2b_bf16 v[64:95], v[178:179], v[136:137], 0\n"
    "buffer_load_dwordx4 a[8:11], v160, %[rs], s42 offen offset:1024\n"
    "v_exp_f32 v118, v118\n"
    "v_exp_f32 v119, v119\n"
    "v_exp_f32 v120, v120\n"
    "v_exp_f32 v121, v121\n"
    "v_exp_f32 v122, v122\n"
    "v_exp_f32 v123, v123\n"
    "v_exp_f32 v124, v124\n"
    "v_exp_f32 v125, v125\n"
    "v_smfmac_f32_32x32x32_bf16 v[16:31], v[184:187], v[112:119], v143\n"
    "s_waitcnt lgkmcnt(3)\n"
    "buffer_store_dwordx4 v[156:159], v164, %[rs], 0 offen offset:768 sc1\n"
    "v_exp_f32 v126, v126\n"
    "v_exp_f32 v127, v127\n"
    "v_exp_f32 v32, v32\n"
    "v_exp_f32 v33, v33\n"
    "v_smfmac_f32_32x32x32_bf16 v[0:15], v[192:195], v[104:111], v143\n"
    "s_waitcnt lgkmcnt(0)\n"
    "v_exp_f32 v34, v34\n"
    "v_exp_f32 v35, v35\n"
    "v_exp_f32 v36, v36\n"
    "v_exp_f32 v37, v37\n"
    "v_mfma_f32_32x32x16_bf16 v[64:79], v[144:147], v[128:131], v[64:79]\n"
    "s_waitcnt vmcnt(16)\n"
    "ds_write_b128 v166, v[232:235]\n"
    "v_exp_f32 v38, v38\n"
    "v_exp_f32 v39, v39\n"
    "v_exp_f32 v40, v40\n"
    "v_exp_f32 v41, v41\n"
    "v_smfmac_f32_32x32x32_bf16 v[0:15], v[188:191], v[32:39], v143\n"
    "ds_read_b128 v[184:187], v174 offset:2560\n"
    "v_exp_f32 v42, v42\n"
    "v_exp_f32 v43, v43\n"
    "v_exp_f32 v44, v44\n"
    "v_exp_f32 v45, v45\n"
    "v_mfma_f32_32x32x16_bf16 v[80:95], v[144:147], v[132:135], v[80:95]\n"
    "ds_read_b128 v[144:147], v172 offset:4096\n"
    "v_exp_f32 v46, v46\n"
    "v_exp_f32 v47, v47\n"
    "v_exp_f32 v48, v48\n"
    "v_exp_f32 v49, v49\n"
    "v_smfmac_f32_32x32x32_bf16 v[16:31], v[192:195], v[120:127], v143\n"
    "ds_read_b128 v[192:195], v174 offset:3200\n"
    "v_exp_f32 v50, v50\n"
    "v_exp_f32 v51, v51\n"
    "v_exp_f32 v52, v52\n"
    "v_exp_f32 v53, v53\n"
    "v_mfma_f32_32x32x4_2b_bf16 v[96:127], v[178:179], v[138:139], 0\n"
    "s_barrier\n"
    "v_exp_f32 v54, v54\n"
    "v_exp_f32 v55, v55\n"
    "v_exp_f32 v56, v56\n"
    "v_exp_f32 v57, v57\n"
    "v_exp_f32 v58, v58\n"
    "v_exp_f32 v59, v59\n"
    "v_exp_f32 v60, v60\n"
    "v_exp_f32 v61, v61\n"
    "v_smfmac_f32_32x32x32_bf16 v[16:31], v[188:191], v[48:55], v143\n"
    "ds_read_b64_tr_b16 v[152:153], v167\n"
    "ds_read_b64_tr_b16 v[154:155], v167 offset:512\n"
    "v_exp_f32 v62, v62\n"
    "v_exp_f32 v63, v63\n"
    "v_exp_f32 v64, v64\n"
    "v_exp_f32 v65, v65\n"
    "v_smfmac_f32_32x32x32_bf16 v[0:15], v[196:199], v[40:47], v143\n"
    "s_waitcnt lgkmcnt(2)\n"
    "v_exp_f32 v66, v66\n"
    "v_exp_f32 v67, v67\n"
    "v_exp_f32 v68, v68\n"
    "v_exp_f32 v69, v69\n"
    "v_mfma_f32_32x32x16_bf16 v[96:111], v[148:151], v[128:131], v[96:111]\n"
    "s_add_u32 m0, s32, 61440\n"
    "s_add_u32 s40, s24, 136192\n"
    "buffer_load_dwordx4 v161, %[rs], s40 offen offset:0 lds\n"
    "v_exp_f32 v70, v70\n"
    "v_exp_f32 v71, v71\n"
    "v_exp_f32 v72, v72\n"
    "v_exp_f32 v73, v73\n"
    "v_smfmac_f32_32x32x32_bf16 v[0:15], v[184:187], v[64:71], v143\n"
    "ds_read_b128 v[188:191], v174 offset:3840\n"
    "v_exp_f32 v74, v74\n"
    "v_exp_f32 v75, v75\n"
    "v_exp_f32 v76, v76\n"
    "v_exp_f32 v77, v77\n"
    "v_mfma_f32_32x32x16_bf16 v[112:127], v[148:151], v[132:135], v[112:127]\n"
    "ds_read_b128 v[148:151], v172 offset:5120\n"
    "v_exp_f32 v78, v78\n"
    "v_exp_f32 v79, v79\n"
    "v_exp_f32 v80, v80\n"
    "v_exp_f32 v81, v81\n"
    "v_smfmac_f32_32x32x32_bf16 v[16:31], v[196:199], v[56:63], v143\n"
    "ds_read_b128 v[196:199], v174 offset:4480\n"
    "v_exp_f32 v82, v82\n"
    "v_exp_f32 v83, v83\n"
    "v_exp_f32 v84, v84\n"
    "v_exp_f32 v85, v85\n"
    "v_mfma_f32_32x32x4_2b_bf16 v[32:63], v[180:181], v[136:137], 0\n"
    "ds_read_b128 v[176:179], v141 offset:28672\n"
    "v_exp_f32 v86, v86\n"
    "v_exp_f32 v87, v87\n"
    "v_exp_f32 v88, v88\n"
    "v_exp_f32 v89, v89\n"
    "v_exp_f32 v90, v90\n"
    "v_exp_f32 v91, v91\n"
    "v_exp_f32 v92, v92\n"
    "v_exp_f32 v93, v93\n"
    "v_smfmac_f32_32x32x32_bf16 v[16:31], v[184:187], v[80:87], v143\n"
    "s_waitcnt lgkmcnt(4)\n"
    "buffer_store_dwordx4 v[152:155], v165, %[rs], 0 offen offset:0 sc1\n"
    "v_exp_f32 v94, v94\n"
    "v_exp_f32 v95, v95\n"
    "v_exp_f32 v96, v96\n"
    "v_exp_f32 v97, v97\n"
    "v_smfmac_f32_32x32x32_bf16 v[0:15], v[192:195], v[72:79], v143\n"
    "s_waitcnt lgkmcnt(1)\n"
    "v_exp_f32 v98, v98\n"
    "v_exp_f32 v99, v99\n"
    "v_exp_f32 v100, v100\n"
    "v_exp_f32 v101, v101\n"
    "v_mfma_f32_32x32x16_bf16 v[32:47], v[144:147], v[128:131], v[32:47]\n"
    "s_waitcnt vmcnt(17)\n"
    "ds_write_b128 v166, v[236:239]\n"
    "v_exp_f32 v102, v102\n"
    "v_exp_f32 v103, v103\n"
    "v_exp_f32 v104, v104\n"
    "v_exp_f32 v105, v105\n"
    "v_smfmac_f32_32x32x32_bf16 v[0:15], v[188:191], v[96:103], v143\n"
    "ds_read_b128 v[184:187], v174 offset:5120\n"
    "v_exp_f32 v106, v106\n"
    "v_exp_f32 v107, v107\n"
    "v_exp_f32 v108, v108\n"
    "v_exp_f32 v109, v109\n"
    "v_mfma_f32_32x32x16_bf16 v[48:63], v[144:147], v[132:135], v[48:63]\n"
    "ds_read_b128 v[144:147], v172 offset:6144\n"
    "v_exp_f32 v110, v110\n"
    "v_exp_f32 v111, v111\n"
    "v_exp_f32 v112, v112\n"
    "v_exp_f32 v113, v113\n"
    "v_smfmac_f32_32x32x32_bf16 v[16:31], v[192:195], v[88:95], v143\n"
    "ds_read_b128 v[192:195], v174 offset:5760\n"
    "v_exp_f32 v114, v114\n"
    "v_exp_f32 v115, v115\n"
    "v_exp_f32 v116, v116\n"
    "v_exp_f32 v117, v117\n"
    "v_mfma_f32_32x32x4_2b_bf16 v[64:95], v[180:181], v[138:139], 0\n"
    "buffer_load_dwordx4 v161, %[rs], s40 offen offset:1024 lds\n"
    "v_exp_f32 v118, v118\n"
    "v_exp_f32 v119, v119\n"
    "v_exp_f32 v120, v120\n"
    "v_exp_f32 v121, v121\n"
    "v_exp_f32 v122, v122\n"
    "v_exp_f32 v123, v123\n"
    "v_exp_f32 v124, v124\n"
    "v_exp_f32 v125, v125\n"
    "v_smfmac_f32_32x32x32_bf16 v[16:31], v[188:191], v[112:119], v143\n"
    "ds_read_b64_tr_b16 v[156:157], v167\n"
    "ds_read_b64_tr_b16 v[158:159], v167 offset:512\n"
    "v_exp_f32 v126, v126\n"
    "v_exp_f32 v127, v127\n"
    "v_exp_f32 v32, v32\n"
    "v_exp_f32 v33, v33\n"
    "v_smfmac_f32_32x32x32_bf16 v[0:15], v[196:199], v[104:111], v143\n"
    "s_waitcnt lgkmcnt(2)\n"
    "v_exp_f32 v34, v34\n"
    "v_exp_f32 v35, v35\n"
    "v_exp_f32 v36, v36\n"
    "v_exp_f32 v37, v37\n"
    "v_mfma_f32_32x32x16_bf16 v[64:79], v[148:151], v[128:131], v[64:79]\n"
    "buffer_load_dwordx4 v161, %[rs], s40 offen offset:2048 lds\n"
    "v_exp_f32 v38, v38\n"
    "v_exp_f32 v39, v39\n"
    "v_exp_f32 v40, v40\n"
    "v_exp_f32 v41, v41\n"
    "v_smfmac_f32_32x32x32_bf16 v[0:15], v[184:187], v[32:39], v143\n"
    "ds_read_b128 v[188:191], v174 offset:6400\n"
    "v_exp_f32 v42, v42\n"
    "v_exp_f32 v43, v43\n"
    "v_exp_f32 v44, v44\n"
    "v_exp_f32 v45, v45\n"
    "v_mfma_f32_32x32x16_bf16 v[80:95], v[148:151], v[132:135], v[80:95]\n"
    "ds_read_b128 v[148:151], v172 offset:7168\n"
    "v_exp_f32 v46, v46\n"
    "v_exp_f32 v47, v47\n"
    "v_exp_f32 v48, v48\n"
    "v_exp_f32 v49, v49\n"
    "v_smfmac_f32_32x32x32_bf16 v[16:31], v[196:199], v[120:127], v143\n"
    "ds_read_b128 v[196:199], v174 offset:7040\n"
    "v_exp_f32 v50, v50\n"
    "v_exp_f32 v51, v51\n"
    "v_exp_f32 v52, v52\n"
    "v_exp_f32 v53, v53\n"
    "v_mfma_f32_32x32x4_2b_bf16 v[96:127], v[182:183], v[136:137], 0\n"
    "buffer_load_dwordx4 v161, %[rs], s40 offen offset:3072 lds\n"
    "v_exp_f32 v54, v54\n"
    "v_exp_f32 v55, v55\n"
    "v_exp_f32 v56, v56\n"
    "v_exp_f32 v57, v57\n"
    "v_exp_f32 v58, v58\n"
    "v_exp_f32 v59, v59\n"
    "v_exp_f32 v60, v60\n"
    "v_exp_f32 v61, v61\n"
    "v_smfmac_f32_32x32x32_bf16 v[16:31], v[184:187], v[48:55], v143\n"
    "s_waitcnt lgkmcnt(3)\n"
    "buffer_store_dwordx4 v[156:159], v165, %[rs], 0 offen offset:256 sc1\n"
    "v_exp_f32 v62, v62\n"
    "v_exp_f32 v63, v63\n"
    "v_exp_f32 v64, v64\n"
    "v_exp_f32 v65, v65\n"
    "v_smfmac_f32_32x32x32_bf16 v[0:15], v[192:195], v[40:47], v143\n"
    "s_waitcnt lgkmcnt(0)\n"
    "v_exp_f32 v66, v66\n"
    "v_exp_f32 v67, v67\n"
    "v_exp_f32 v68, v68\n"
    "v_exp_f32 v69, v69\n"
    "v_mfma_f32_32x32x16_bf16 v[96:111], v[144:147], v[128:131], v[96:111]\n"
    "s_waitcnt vmcnt(20)\n"
    "ds_write_b128 v166, v[240:243]\n"
    "v_exp_f32 v70, v70\n"
    "v_exp_f32 v71, v71\n"
    "v_exp_f32 v72, v72\n"
    "v_exp_f32 v73, v73\n"
    "v_smfmac_f32_32x32x32_bf16 v[0:15], v[188:191], v[64:71], v143\n"
    "ds_read_b128 v[184:187], v174 offset:7680\n"
    "v_exp_f32 v74, v74\n"
    "v_exp_f32 v75, v75\n"
    "v_exp_f32 v76, v76\n"
    "v_exp_f32 v77, v77\n"
    "v_mfma_f32_32x32x16_bf16 v[112:127], v[144:147], v[132:135], v[112:127]\n"
    "ds_read_b128 v[144:147], v172 offset:20480\n"
    "v_exp_f32 v78, v78\n"
    "v_exp_f32 v79, v79\n"
    "v_exp_f32 v80, v80\n"
    "v_exp_f32 v81, v81\n"
    "v_smfmac_f32_32x32x32_bf16 v[16:31], v[192:195], v[56:63], v143\n"
    "ds_read_b128 v[192:195], v174 offset:8320\n"
    "v_exp_f32 v82, v82\n"
    "v_exp_f32 v83, v83\n"
    "v_exp_f32 v84, v84\n"
    "v_exp_f32 v85, v85\n"
    "v_mfma_f32_32x32x4_2b_bf16 v[32:63], v[182:183], v[138:139], 0\n"
    "s_add_u32 m0, s32, 65536\n"
    "s_add_u32 s40, s24, 140288\n"
    "buffer_load_dwordx4 v161, %[rs], s40 offen offset:0 lds\n"
    "v_exp_f32 v86, v86\n"
    "v_exp_f32 v87, v87\n"
    "v_exp_f32 v88, v88\n"
    "v_exp_f32 v89, v89\n"
    "v_exp_f32 v90, v90\n"
    "v_exp_f32 v91, v91\n"
    "v_exp_f32 v92, v92\n"
    "v_exp_f32 v93, v93\n"
    "v_smfmac_f32_32x32x32_bf16 v[16:31], v[188:191], v[80:87], v143\n"
    "ds_read_b64_tr_b16 v[152:153], v167\n"
    "ds_read_b64_tr_b16 v[154:155], v167 offset:512\n"
    "v_exp_f32 v94, v94\n"
    "v_exp_f32 v95, v95\n"
    "v_exp_f32 v96, v96\n"
    "v_exp_f32 v97, v97\n"
    "v_smfmac_f32_32x32x32_bf16 v[0:15], v[196:199], v[72:79], v143\n"
    "s_waitcnt lgkmcnt(2)\n"
    "v_exp_f32 v98, v98\n"
    "v_exp_f32 v99, v99\n"
    "v_exp_f32 v100, v100\n"
    "v_exp_f32 v101, v101\n"
    "v_mfma_f32_32x32x16_bf16 v[32:47], v[148:151], v[128:131], v[32:47]\n"
    "buffer_load_dwordx4 a[12:15], v160, %[rs], s43 offen offset:1024\n"
    "v_exp_f32 v102, v102\n"
    "v_exp_f32 v103, v103\n"
    "v_exp_f32 v104, v104\n"
    "v_exp_f32 v105, v105\n"
    "v_smfmac_f32_32x32x32_bf16 v[0:15], v[184:187], v[96:103], v143\n"
    "ds_read_b128 v[188:191], v174 offset:8960\n"
    "v_exp_f32 v106, v106\n"
    "v_exp_f32 v107, v107\n"
    "v_exp_f32 v108, v108\n"
    "v_exp_f32 v109, v109\n"
    "v_mfma_f32_32x32x16_bf16 v[48:63], v[148:151], v[132:135], v[48:63]\n"
    "ds_read_b128 v[148:151], v172 offset:21504\n"
    "v_exp_f32 v110, v110\n"
    "v_exp_f32 v111, v111\n"
    "v_exp_f32 v112, v112\n"
    "v_exp_f32 v113, v113\n"
    "v_smfmac_f32_32x32x32_bf16 v[16:31], v[196:199], v[88:95], v143\n"
    "ds_read_b128 v[196:199], v174 offset:9600\n"
    "v_exp_f32 v114, v114\n"
    "v_exp_f32 v115, v115\n"
    "v_exp_f32 v116, v116\n"
    "v_exp_f32 v117, v117\n"
    "v_mfma_f32_32x32x4_2b_bf16 v[64:95], v[176:177], v[136:137], 0\n"
    "ds_read_b128 v[180:183], v141 offset:29184\n"
    "v_exp_f32 v118, v118\n"
    "v_exp_f32 v119, v119\n"
    "v_exp_f32 v120, v120\n"
    "v_exp_f32 v121, v121\n"
    "v_exp_f32 v122, v122\n"
    "v_exp_f32 v123, v123\n"
    "v_exp_f32 v124, v124\n"
    "v_exp_f32 v125, v125\n"
    "v_smfmac_f32_32x32x32_bf16 v[16:31], v[184:187], v[112:119], v143\n"
    "s_waitcnt lgkmcnt(4)\n"
    "buffer_store_dwordx4 v[152:155], v165, %[rs], 0 offen offset:512 sc1\n"
    "v_exp_f32 v126, v126\n"
    "v_exp_f32 v127, v127\n"
    "v_exp_f32 v32, v32\n"
    "v_exp_f32 v33, v33\n"
    "v_smfmac_f32_32x32x32_bf16 v[0:15], v[192:195], v[104:111], v143\n"
    "s_waitcnt lgkmcnt(1)\n"
    "v_exp_f32 v34, v34\n"
    "v_exp_f32 v35, v35\n"
    "v_exp_f32 v36, v36\n"
    "v_exp_f32 v37, v37\n"
    "v_mfma_f32_32x32x16_bf16 v[64:79], v[144:147], v[128:131], v[64:79]\n"
    "s_waitcnt vmcnt(14)\n"
    "ds_write_b128 v166, v[244:247]\n"
    "v_exp_f32 v38, v38\n"
    "v_exp_f32 v39, v39\n"
    "v_exp_f32 v40, v40\n"
    "v_exp_f32 v41, v41\n"
    "v_smfmac_f32_32x32x32_bf16 v[0:15], v[188:191], v[32:39], v143\n"
    "ds_read_b128 v[184:187], v174 offset:20480\n"
    "v_exp_f32 v42, v42\n"
    "v_exp_f32 v43, v43\n"
    "v_exp_f32 v44, v44\n"
    "v_exp_f32 v45, v45\n"
    "v_mfma_f32_32x32x16_bf16 v[80:95], v[144:147], v[132:135], v[80:95]\n"
    "ds_read_b128 v[144:147], v172 offset:22528\n"
    "v_exp_f32 v46, v46\n"
    "v_exp_f32 v47, v47\n"
    "v_exp_f32 v48, v48\n"
    "v_exp_f32 v49, v49\n"
    "v_smfmac_f32_32x32x32_bf16 v[16:31], v[192:195], v[120:127], v143\n"
    "ds_read_b128 v[192:195], v174 offset:21120\n"
    "v_exp_f32 v50, v50\n"
    "v_exp_f32 v51, v51\n"
    "v_exp_f32 v52, v52\n"
    "v_exp_f32 v53, v53\n"
    "v_mfma_f32_32x32x4_2b_bf16 v[96:127], v[176:177], v[138:139], 0\n"
    "buffer_load_dwordx4 a[16:19], v160, %[rs], s42 offen offset:2048\n"
    "v_exp_f32 v54, v54\n"
    "v_exp_f32 v55, v55\n"
    "v_exp_f32 v56, v56\n"
    "v_exp_f32 v57, v57\n"
    "v_exp_f32 v58, v58\n"
    "v_exp_f32 v59, v59\n"
    "v_exp_f32 v60, v60\n"
    "v_exp_f32 v61, v61\n"
    "v_smfmac_f32_32x32x32_bf16 v[16:31], v[188:191], v[48:55], v143\n"
    "ds_read_b64_tr_b16 v[156:157], v167\n"
    "ds_read_b64_tr_b16 v[158:159], v167 offset:512\n"
    "v_exp_f32 v62, v62\n"
    "v_exp_f32 v63, v63\n"
    "v_exp_f32 v64, v64\n"
    "v_exp_f32 v65, v65\n"
    "v_smfmac_f32_32x32x32_bf16 v[0:15], v[196:199], v[40:47], v143\n"
    "s_waitcnt lgkmcnt(2)\n"
    "v_exp_f32 v66, v66\n"
    "v_exp_f32 v67, v67\n"
    "v_exp_f32 v68, v68\n"
    "v_exp_f32 v69, v69\n"
    "v_mfma_f32_32x32x16_bf16 v[96:111], v[148:151], v[128:131], v[96:111]\n"
    "buffer_load_dwordx4 a[20:23], v160, %[rs], s43 offen offset:2048\n"
    "v_exp_f32 v70, v70\n"
    "v_exp_f32 v71, v71\n"
    "v_exp_f32 v72, v72\n"
    "v_exp_f32 v73, v73\n"
    "v_smfmac_f32_32x32x32_bf16 v[0:15], v[184:187], v[64:71], v143\n"
    "ds_read_b128 v[188:191], v174 offset:21760\n"
    "v_exp_f32 v74, v74\n"
    "v_exp_f32 v75, v75\n"
    "v_exp_f32 v76, v76\n"
    "v_exp_f32 v77, v77\n"
    "v_mfma_f32_32x32x16_bf16 v[112:127], v[148:151], v[132:135], v[112:127]\n"
    "ds_read_b128 v[148:151], v172 offset:23552\n"
    "v_exp_f32 v78, v78\n"
    "v_exp_f32 v79, v79\n"
    "v_exp_f32 v80, v80\n"
    "v_exp_f32 v81, v81\n"
    "v_smfmac_f32_32x32x32_bf16 v[16:31], v[196:199], v[56:63], v143\n"
    "ds_read_b128 v[196:199], v174 offset:22400\n"
    "v_exp_f32 v82, v82\n"
    "v_exp_f32 v83, v83\n"
    "v_exp_f32 v84, v84\n"
    "v_exp_f32 v85, v85\n"
    "v_mfma_f32_32x32x4_2b_bf16 v[32:63], v[178:179], v[136:137], 0\n"
    "buffer_load_dwordx4 a[24:27], v160, %[rs], s42 offen offset:3072\n"
    "v_exp_f32 v86, v86\n"
    "v_exp_f32 v87, v87\n"
    "v_exp_f32 v88, v88\n"
    "v_exp_f32 v89, v89\n"
    "v_exp_f32 v90, v90\n"
    "v_exp_f32 v91, v91\n"
    "v_exp_f32 v92, v92\n"
    "v_exp_f32 v93, v93\n"
    "v_smfmac_f32_32x32x32_bf16 v[16:31], v[184:187], v[80:87], v143\n"
    "s_waitcnt lgkmcnt(3)\n"
    "buffer_store_dwordx4 v[156:159], v165, %[rs], 0 offen offset:768 sc1\n"
    "v_exp_f32 v94, v94\n"
    "v_exp_f32 v95, v95\n"
    "v_exp_f32 v96, v96\n"
    "v_exp_f32 v97, v97\n"
    "v_smfmac_f32_32x32x32_bf16 v[0:15], v[192:195], v[72:79], v143\n"
    "s_waitcnt lgkmcnt(0)\n"
    "v_exp_f32 v98, v98\n"
    "v_exp_f32 v99, v99\n"
    "v_exp_f32 v100, v100\n"
    "v_exp_f32 v101, v101\n"
    "v_mfma_f32_32x32x16_bf16 v[32:47], v[144:147], v[128:131], v[32:47]\n"
    "buffer_load_dwordx4 a[28:31], v160, %[rs], s43 offen offset:3072\n"
    "v_exp_f32 v102, v102\n"
    "v_exp_f32 v103, v103\n"
    "v_exp_f32 v104, v104\n"
    "v_exp_f32 v105, v105\n"
    "v_smfmac_f32_32x32x32_bf16 v[0:15], v[188:191], v[96:103], v143\n"
    "ds_read_b128 v[184:187], v174 offset:23040\n"
    "v_exp_f32 v106, v106\n"
    "v_exp_f32 v107, v107\n"
    "v_exp_f32 v108, v108\n"
    "v_exp_f32 v109, v109\n"
    "v_mfma_f32_32x32x16_bf16 v[48:63], v[144:147], v[132:135], v[48:63]\n"
    "ds_read_b128 v[144:147], v172 offset:24576\n"
    "v_exp_f32 v110, v110\n"
    "v_exp_f32 v111, v111\n"
    "v_exp_f32 v112, v112\n"
    "v_exp_f32 v113, v113\n"
    "v_smfmac_f32_32x32x32_bf16 v[16:31], v[192:195], v[88:95], v143\n"
    "ds_read_b128 v[192:195], v174 offset:23680\n"
    "v_exp_f32 v114, v114\n"
    "v_exp_f32 v115, v115\n"
    "v_exp_f32 v116, v116\n"
    "v_exp_f32 v117, v117\n"
    "v_mfma_f32_32x32x4_2b_bf16 v[64:95], v[178:179], v[138:139], 0\n"
    "s_barrier\n"
    "v_exp_f32 v118, v118\n"
    "v_exp_f32 v119, v119\n"
    "v_exp_f32 v120, v120\n"
    "v_exp_f32 v121, v121\n"
    "v_exp_f32 v122, v122\n"
    "v_exp_f32 v123, v123\n"
    "v_exp_f32 v124, v124\n"
    "v_exp_f32 v125, v125\n"
    "v_smfmac_f32_32x32x32_bf16 v[16:31], v[188:191], v[112:119], v143\n"
    "s_add_u32 s42, s27, 4096\n"
    "buffer_load_dwordx4 a[32:35], v160, %[rs], s42 offen offset:0\n"
    "v_exp_f32 v126, v126\n"
    "v_exp_f32 v127, v127\n"
    "v_exp_f32 v32, v32\n"
    "v_exp_f32 v33, v33\n"
    "v_smfmac_f32_32x32x32_bf16 v[0:15], v[196:199], v[104:111], v143\n"
    "s_waitcnt lgkmcnt(0)\n"
    "v_exp_f32 v34, v34\n"
    "v_exp_f32 v35, v35\n"
    "v_exp_f32 v36, v36\n"
    "v_exp_f32 v37, v37\n"
    "v_mfma_f32_32x32x16_bf16 v[64:79], v[148:151], v[128:131], v[64:79]\n"
    "s_add_u32 s43, s27, 22528\n"
    "buffer_load_dwordx4 a[36:39], v160, %[rs], s43 offen offset:0\n"
    "v_exp_f32 v38, v38\n"
    "v_exp_f32 v39, v39\n"
    "v_exp_f32 v40, v40\n"
    "v_exp_f32 v41, v41\n"
    "v_smfmac_f32_32x32x32_bf16 v[0:15], v[184:187], v[32:39], v143\n"
    "ds_read_b128 v[188:191], v174 offset:24320\n"
    "v_exp_f32 v42, v42\n"
    "v_exp_f32 v43, v43\n"
    "v_exp_f32 v44, v44\n"
    "v_exp_f32 v45, v45\n"
    "v_mfma_f32_32x32x16_bf16 v[80:95], v[148:151], v[132:135], v[80:95]\n"
    "ds_read_b128 v[148:151], v172 offset:25600\n"
    "v_exp_f32 v46, v46\n"
    "v_exp_f32 v47, v47\n"
    "v_exp_f32 v48, v48\n"
    "v_exp_f32 v49, v49\n"
    "v_smfmac_f32_32x32x32_bf16 v[16:31], v[196:199], v[120:127], v143\n"
    "ds_read_b128 v[196:199], v174 offset:24960\n"
    "v_exp_f32 v50, v50\n"
    "v_exp_f32 v51, v51\n"
    "v_exp_f32 v52, v52\n"
    "v_exp_f32 v53, v53\n"
    "v_mfma_f32_32x32x4_2b_bf16 v[96:127], v[180:181], v[136:137], 0\n"
    "ds_read_b128 v[176:179], v141 offset:49152\n"
    "v_exp_f32 v54, v54\n"
    "v_exp_f32 v55, v55\n"
    "v_exp_f32 v56, v56\n"
    "v_exp_f32 v57, v57\n"
    "v_exp_f32 v58, v58\n"
    "v_exp_f32 v59, v59\n"
    "v_exp_f32 v60, v60\n"
    "v_exp_f32 v61, v61\n"
    "v_smfmac_f32_32x32x32_bf16 v[16:31], v[184:187], v[48:55], v143\n"
    "buffer_load_dwordx4 a[40:43], v160, %[rs], s42 offen offset:1024\n"
    "v_exp_f32 v62, v62\n"
    "v_exp_f32 v63, v63\n"
    "v_exp_f32 v64, v64\n"
    "v_exp_f32 v65, v65\n"
    "v_smfmac_f32_32x32x32_bf16 v[0:15], v[192:195], v[40:47], v143\n"
    "s_waitcnt lgkmcnt(1)\n"
    "v_exp_f32 v66, v66\n"
    "v_exp_f32 v67, v67\n"
    "v_exp_f32 v68, v68\n"
    "v_exp_f32 v69, v69\n"
    "v_mfma_f32_32x32x16_bf16 v[96:111], v[144:147], v[128:131], v[96:111]\n"
    "buffer_load_dwordx4 a[44:47], v160, %[rs], s43 offen offset:1024\n"
    "v_exp_f32 v70, v70\n"
    "v_exp_f32 v71, v71\n"
    "v_exp_f32 v72, v72\n"
    "v_exp_f32 v73, v73\n"
    "v_smfmac_f32_32x32x32_bf16 v[0:15], v[188:191], v[64:71], v143\n"
    "ds_read_b128 v[184:187], v174 offset:25600\n"
    "v_exp_f32 v74, v74\n"
    "v_exp_f32 v75, v75\n"
    "v_exp_f32 v76, v76\n"
    "v_exp_f32 v77, v77\n"
    "v_mfma_f32_32x32x16_bf16 v[112:127], v[144:147], v[132:135], v[112:127]\n"
    "ds_read_b128 v[144:147], v172 offset:26624\n"
    "v_exp_f32 v78, v78\n"
    "v_exp_f32 v79, v79\n"
    "v_exp_f32 v80, v80\n"
    "v_exp_f32 v81, v81\n"
    "v_smfmac_f32_32x32x32_bf16 v[16:31], v[192:195], v[56:63], v143\n"
    "ds_read_b128 v[192:195], v174 offset:26240\n"
    "v_exp_f32 v82, v82\n"
    "v_exp_f32 v83, v83\n"
    "v_exp_f32 v84, v84\n"
    "v_exp_f32 v85, v85\n"
    "v_mfma_f32_32x32x4_2b_bf16 v[32:63], v[180:181], v[138:139], 0\n"
    "buffer_load_dwordx4 a[48:51], v160, %[rs], s42 offen offset:2048\n"
    "v_exp_f32 v86, v86\n"
    "v_exp_f32 v87, v87\n"
    "v_exp_f32 v88, v88\n"
    "v_exp_f32 v89, v89\n"
    "v_exp_f32 v90, v90\n"
    "v_exp_f32 v91, v91\n"
    "v_exp_f32 v92, v92\n"
    "v_exp_f32 v93, v93\n"
    "v_smfmac_f32_32x32x32_bf16 v[16:31], v[188:191], v[80:87], v143\n"
    "buffer_load_dwordx4 a[52:55], v160, %[rs], s43 offen offset:2048\n"
    "v_exp_f32 v94, v94\n"
    "v_exp_f32 v95, v95\n"
    "v_exp_f32 v96, v96\n"
    "v_exp_f32 v97, v97\n"
    "v_smfmac_f32_32x32x32_bf16 v[0:15], v[196:199], v[72:79], v143\n"
    "s_waitcnt lgkmcnt(0)\n"
    "v_exp_f32 v98, v98\n"
    "v_exp_f32 v99, v99\n"
    "v_exp_f32 v100, v100\n"
    "v_exp_f32 v101, v101\n"
    "v_mfma_f32_32x32x16_bf16 v[32:47], v[148:151], v[128:131], v[32:47]\n"
    "buffer_load_dwordx4 a[56:59], v160, %[rs], s42 offen offset:3072\n"
    "v_exp_f32 v102, v102\n"
    "v_exp_f32 v103, v103\n"
    "v_exp_f32 v104, v104\n"
    "v_exp_f32 v105, v105\n"
    "v_smfmac_f32_32x32x32_bf16 v[0:15], v[184:187], v[96:103], v143\n"
    "ds_read_b128 v[188:191], v174 offset:26880\n"
    "v_exp_f32 v106, v106\n"
    "v_exp_f32 v107, v107\n"
    "v_exp_f32 v108, v108\n"
    "v_exp_f32 v109, v109\n"
    "v_mfma_f32_32x32x16_bf16 v[48:63], v[148:151], v[132:135], v[48:63]\n"
    "ds_read_b128 v[148:151], v172 offset:27648\n"
    "v_exp_f32 v110, v110\n"
    "v_exp_f32 v111, v111\n"
    "v_exp_f32 v112, v112\n"
    "v_exp_f32 v113, v113\n"
    "v_smfmac_f32_32x32x32_bf16 v[16:31], v[196:199], v[88:95], v143\n"
    "ds_read_b128 v[196:199], v174 offset:27520\n"
    "v_exp_f32 v114, v114\n"
    "v_exp_f32 v115, v115\n"
    "v_exp_f32 v116, v116\n"
    "v_exp_f32 v117, v117\n"
    "v_mfma_f32_32x32x4_2b_bf16 v[64:95], v[182:183], v[136:137], 0\n"
    "buffer_load_dwordx4 a[60:63], v160, %[rs], s43 offen offset:3072\n"
    "v_exp_f32 v118, v118\n"
    "v_exp_f32 v119, v119\n"
    "v_exp_f32 v120, v120\n"
    "v_exp_f32 v121, v121\n"
    "v_exp_f32 v122, v122\n"
    "v_exp_f32 v123, v123\n"
    "v_exp_f32 v124, v124\n"
    "v_exp_f32 v125, v125\n"
    "v_smfmac_f32_32x32x32_bf16 v[16:31], v[184:187], v[112:119], v143\n"
    "s_add_u32 s42, s27, 8192\n"
    "buffer_load_dwordx4 a[64:67], v160, %[rs], s42 offen offset:0\n"
    "v_exp_f32 v126, v126\n"
    "v_exp_f32 v127, v127\n"
    "v_exp_f32 v32, v32\n"
    "v_exp_f32 v33, v33\n"
    "v_smfmac_f32_32x32x32_bf16 v[0:15], v[192:195], v[104:111], v143\n"
    "s_waitcnt lgkmcnt(0)\n"
    "v_exp_f32 v34, v34\n"
    "v_exp_f32 v35, v35\n"
    "v_exp_f32 v36, v36\n"
    "v_exp_f32 v37, v37\n"
    "v_mfma_f32_32x32x16_bf16 v[64:79], v[144:147], v[128:131], v[64:79]\n"
    "s_add_u32 s43, s27, 26624\n"
    "buffer_load_dwordx4 a[68:71], v160, %[rs], s43 offen offset:0\n"
    "v_exp_f32 v38, v38\n"
    "v_exp_f32 v39, v39\n"
    "v_exp_f32 v40, v40\n"
    "v_exp_f32 v41, v41\n"
    "v_smfmac_f32_32x32x32_bf16 v[0:15], v[188:191], v[32:39], v143\n"
    "ds_read_b128 v[184:187], v174 offset:28160\n"
    "v_exp_f32 v42, v42\n"
    "v_exp_f32 v43, v43\n"
    "v_exp_f32 v44, v44\n"
    "v_exp_f32 v45, v45\n"
    "v_mfma_f32_32x32x16_bf16 v[80:95], v[144:147], v[132:135], v[80:95]\n"
    "ds_read_b128 v[144:147], v172 offset:40960\n"
    "v_exp_f32 v46, v46\n"
    "v_exp_f32 v47, v47\n"
    "v_exp_f32 v48, v48\n"
    "v_exp_f32 v49, v49\n"
    "v_smfmac_f32_32x32x32_bf16 v[16:31], v[192:195], v[120:127], v143\n"
    "ds_read_b128 v[192:195], v174 offset:28800\n"
    "v_exp_f32 v50, v50\n"
    "v_exp_f32 v51, v51\n"
    "v_exp_f32 v52, v52\n"
    "v_exp_f32 v53, v53\n"
    "v_mfma_f32_32x32x4_2b_bf16 v[96:127], v[182:183], v[138:139], 0\n"
    "buffer_load_dwordx4 a[72:75], v160, %[rs], s42 offen offset:1024\n"
    "v_exp_f32 v54, v54\n"
    "v_exp_f32 v55, v55\n"
    "v_exp_f32 v56, v56\n"
    "v_exp_f32 v57, v57\n"
    "v_exp_f32 v58, v58\n"
    "v_exp_f32 v59, v59\n"
    "v_exp_f32 v60, v60\n"
    "v_exp_f32 v61, v61\n"
    "v_smfmac_f32_32x32x32_bf16 v[16:31], v[188:191], v[48:55], v143\n"
    "buffer_load_dwordx4 a[76:79], v160, %[rs], s43 offen offset:1024\n"
    "v_exp_f32 v62, v62\n"
    "v_exp_f32 v63, v63\n"
    "v_exp_f32 v64, v64\n"
    "v_exp_f32 v65, v65\n"
    "v_smfmac_f32_32x32x32_bf16 v[0:15], v[196:199], v[40:47], v143\n"
    "s_waitcnt lgkmcnt(0)\n"
    "v_exp_f32 v66, v66\n"
    "v_exp_f32 v67, v67\n"
    "v_exp_f32 v68, v68\n"
    "v_exp_f32 v69, v69\n"
    "v_mfma_f32_32x32x16_bf16 v[96:111], v[148:151], v[128:131], v[96:111]\n"
    "buffer_load_dwordx4 a[80:83], v160, %[rs], s42 offen offset:2048\n"
    "v_exp_f32 v70, v70\n"
    "v_exp_f32 v71, v71\n"
    "v_exp_f32 v72, v72\n"
    "v_exp_f32 v73, v73\n"
    "v_smfmac_f32_32x32x32_bf16 v[0:15], v[184:187], v[64:71], v143\n"
    "ds_read_b128 v[188:191], v174 offset:29440\n"
    "v_exp_f32 v74, v74\n"
    "v_exp_f32 v75, v75\n"
    "v_exp_f32 v76, v76\n"
    "v_exp_f32 v77, v77\n"
    "v_mfma_f32_32x32x16_bf16 v[112:127], v[148:151], v[132:135], v[112:127]\n"
    "ds_read_b128 v[148:151], v172 offset:41984\n"
    "v_exp_f32 v78, v78\n"
    "v_exp_f32 v79, v79\n"
    "v_exp_f32 v80, v80\n"
    "v_exp_f32 v81, v81\n"
    "v_smfmac_f32_32x32x32_bf16 v[16:31], v[196:199], v[56:63], v143\n"
    "ds_read_b128 v[196:199], v174 offset:30080\n"
    "v_exp_f32 v82, v82\n"
    "v_exp_f32 v83, v83\n"
    "v_exp_f32 v84, v84\n"
    "v_exp_f32 v85, v85\n"
    "v_mfma_f32_32x32x4_2b_bf16 v[32:63], v[176:177], v[136:137], 0\n"
    "ds_read_b128 v[180:183], v141 offset:49664\n"
    "v_exp_f32 v86, v86\n"
    "v_exp_f32 v87, v87\n"
    "v_exp_f32 v88, v88\n"
    "v_exp_f32 v89, v89\n"
    "v_exp_f32 v90, v90\n"
    "v_exp_f32 v91, v91\n"
    "v_exp_f32 v92, v92\n"
    "v_exp_f32 v93, v93\n"
    "v_smfmac_f32_32x32x32_bf16 v[16:31], v[184:187], v[80:87], v143\n"
    "buffer_load_dwordx4 a[84:87], v160, %[rs], s43 offen offset:2048\n"
    "v_exp_f32 v94, v94\n"
    "v_exp_f32 v95, v95\n"
    "v_exp_f32 v96, v96\n"
    "v_exp_f32 v97, v97\n"
    "v_smfmac_f32_32x32x32_bf16 v[0:15], v[192:195], v[72:79], v143\n"
    "s_waitcnt lgkmcnt(1)\n"
    "v_exp_f32 v98, v98\n"
    "v_exp_f32 v99, v99\n"
    "v_exp_f32 v100, v100\n"
    "v_exp_f32 v101, v101\n"
    "v_mfma_f32_32x32x16_bf16 v[32:47], v[144:147], v[128:131], v[32:47]\n"
    "buffer_load_dwordx4 a[88:91], v160, %[rs], s42 offen offset:3072\n"
    "v_exp_f32 v102, v102\n"
    "v_exp_f32 v103, v103\n"
    "v_exp_f32 v104, v104\n"
    "v_exp_f32 v105, v105\n"
    "v_smfmac_f32_32x32x32_bf16 v[0:15], v[188:191], v[96:103], v143\n"
    "ds_read_b128 v[184:187], v174 offset:40960\n"
    "v_exp_f32 v106, v106\n"
    "v_exp_f32 v107, v107\n"
    "v_exp_f32 v108, v108\n"
    "v_exp_f32 v109, v109\n"
    "v_mfma_f32_32x32x16_bf16 v[48:63], v[144:147], v[132:135], v[48:63]\n"
    "ds_read_b128 v[144:147], v172 offset:43008\n"
    "v_exp_f32 v110, v110\n"
    "v_exp_f32 v111, v111\n"
    "v_exp_f32 v112, v112\n"
    "v_exp_f32 v113, v113\n"
    "v_smfmac_f32_32x32x32_bf16 v[16:31], v[192:195], v[88:95], v143\n"
    "ds_read_b128 v[192:195], v174 offset:41600\n"
    "v_exp_f32 v114, v114\n"
    "v_exp_f32 v115, v115\n"
    "v_exp_f32 v116, v116\n"
    "v_exp_f32 v117, v117\n"
    "v_mfma_f32_32x32x4_2b_bf16 v[64:95], v[176:177], v[138:139], 0\n"
    "buffer_load_dwordx4 a[92:95], v160, %[rs], s43 offen offset:3072\n"
    "v_exp_f32 v118, v118\n"
    "v_exp_f32 v119, v119\n"
    "v_exp_f32 v120, v120\n"
    "v_exp_f32 v121, v121\n"
    "v_exp_f32 v122, v122\n"
    "v_exp_f32 v123, v123\n"
    "v_exp_f32 v124, v124\n"
    "v_exp_f32 v125, v125\n"
    "v_smfmac_f32_32x32x32_bf16 v[16:31], v[188:191], v[112:119], v143\n"
    "s_add_u32 s42, s27, 12288\n"
    "buffer_load_dwordx4 a[96:99], v160, %[rs], s42 offen offset:0\n"
    "v_exp_f32 v126, v126\n"
    "v_exp_f32 v127, v127\n"
    "v_exp_f32 v32, v32\n"
    "v_exp_f32 v33, v33\n"
    "v_smfmac_f32_32x32x32_bf16 v[0:15], v[196:199], v[104:111], v143\n"
    "s_waitcnt lgkmcnt(0)\n"
    "v_exp_f32 v34, v34\n"
    "v_exp_f32 v35, v35\n"
    "v_exp_f32 v36, v36\n"
    "v_exp_f32 v37, v37\n"
    "v_mfma_f32_32x32x16_bf16 v[64:79], v[148:151], v[128:131], v[64:79]\n"
    "s_add_u32 s43, s27, 30720\n"
    "buffer_load_dwordx4 a[100:103], v160, %[rs], s43 offen offset:0\n"
    "v_exp_f32 v38, v38\n"
    "v_exp_f32 v39, v39\n"
    "v_exp_f32 v40, v40\n"
    "v_exp_f32 v41, v41\n"
    "v_smfmac_f32_32x32x32_bf16 v[0:15], v[184:187], v[32:39], v143\n"
    "ds_read_b128 v[188:191], v174 offset:42240\n"
    "v_exp_f32 v42, v42\n"
    "v_exp_f32 v43, v43\n"
    "v_exp_f32 v44, v44\n"
    "v_exp_f32 v45, v45\n"
    "v_mfma_f32_32x32x16_bf16 v[80:95], v[148:151], v[132:135], v[80:95]\n"
    "ds_read_b128 v[148:151], v172 offset:44032\n"
    "v_exp_f32 v46, v46\n"
    "v_exp_f32 v47, v47\n"
    "v_exp_f32 v48, v48\n"
    "v_exp_f32 v49, v49\n"
    "v_smfmac_f32_32x32x32_bf16 v[16:31], v[196:199], v[120:127], v143\n"
    "ds_read_b128 v[196:199], v174 offset:42880\n"
    "v_exp_f32 v50, v50\n"
    "v_exp_f32 v51, v51\n"
    "v_exp_f32 v52, v52\n"
    "v_exp_f32 v53, v53\n"
    "v_mfma_f32_32x32x4_2b_bf16 v[96:127], v[178:179], v[136:137], 0\n"
    "buffer_load_dwordx4 a[104:107], v160, %[rs], s42 offen offset:1024\n"
    "v_exp_f32 v54, v54\n"
    "v_exp_f32 v55, v55\n"
    "v_exp_f32 v56, v56\n"
    "v_exp_f32 v57, v57\n"
    "v_exp_f32 v58, v58\n"
    "v_exp_f32 v59, v59\n"
    "v_exp_f32 v60, v60\n"
    "v_exp_f32 v61, v61\n"
    "v_smfmac_f32_32x32x32_bf16 v[16:31], v[184:187], v[48:55], v143\n"
    "buffer_load_dwordx4 a[108:111], v160, %[rs], s43 offen offset:1024\n"
    "v_exp_f32 v62, v62\n"
    "v_exp_f32 v63, v63\n"
    "v_exp_f32 v64, v64\n"
    "v_exp_f32 v65, v65\n"
    "v_smfmac_f32_32x32x32_bf16 v[0:15], v[192:195], v[40:47], v143\n"
    "s_waitcnt lgkmcnt(0)\n"
    "v_exp_f32 v66, v66\n"
    "v_exp_f32 v67, v67\n"
    "v_exp_f32 v68, v68\n"
    "v_exp_f32 v69, v69\n"
    "v_mfma_f32_32x32x16_bf16 v[96:111], v[144:147], v[128:131], v[96:111]\n"
    "buffer_load_dwordx4 a[112:115], v160, %[rs], s42 offen offset:2048\n"
    "v_exp_f32 v70, v70\n"
    "v_exp_f32 v71, v71\n"
    "v_exp_f32 v72, v72\n"
    "v_exp_f32 v73, v73\n"
    "v_smfmac_f32_32x32x32_bf16 v[0:15], v[188:191], v[64:71], v143\n"
    "ds_read_b128 v[184:187], v174 offset:43520\n"
    "v_exp_f32 v74, v74\n"
    "v_exp_f32 v75, v75\n"
    "v_exp_f32 v76, v76\n"
    "v_exp_f32 v77, v77\n"
    "v_mfma_f32_32x32x16_bf16 v[112:127], v[144:147], v[132:135], v[112:127]\n"
    "ds_read_b128 v[144:147], v172 offset:45056\n"
    "v_exp_f32 v78, v78\n"
    "v_exp_f32 v79, v79\n"
    "v_exp_f32 v80, v80\n"
    "v_exp_f32 v81, v81\n"
    "v_smfmac_f32_32x32x32_bf16 v[16:31], v[192:195], v[56:63], v143\n"
    "ds_read_b128 v[192:195], v174 offset:44160\n"
    "v_exp_f32 v82, v82\n"
    "v_exp_f32 v83, v83\n"
    "v_exp_f32 v84, v84\n"
    "v_exp_f32 v85, v85\n"
    "v_mfma_f32_32x32x4_2b_bf16 v[32:63], v[178:179], v[138:139], 0\n"
    "s_waitcnt vmcnt(22)\n"
    "s_barrier\n"
    "s_cmp_lg_u32 s34, 0\n"
    "s_cbranch_scc1 Lka_s_%=\n"
    "s_mov_b64 s[46:47], exec\n"
    "s_mov_b64 exec, 1\n"
    "buffer_atomic_add v170, v169, %[rs], s29 offen\n"
    "s_mov_b64 exec, s[46:47]\n"
    "Lka_s_%=:\n"
    "v_exp_f32 v86, v86\n"
    "v_exp_f32 v87, v87\n"
    "v_exp_f32 v88, v88\n"
    "v_exp_f32 v89, v89\n"
    "v_exp_f32 v90, v90\n"
    "v_exp_f32 v91, v91\n"
    "v_exp_f32 v92, v92\n"
    "v_exp_f32 v93, v93\n"
    "v_smfmac_f32_32x32x32_bf16 v[16:31], v[188:191], v[80:87], v143\n"
    "buffer_load_dwordx4 a[116:119], v160, %[rs], s43 offen offset:2048\n"
    "v_exp_f32 v94, v94\n"
    "v_exp_f32 v95, v95\n"
    "v_exp_f32 v96, v96\n"
    "v_exp_f32 v97, v97\n"
    "v_smfmac_f32_32x32x32_bf16 v[0:15], v[196:199], v[72:79], v143\n"
    "s_waitcnt lgkmcnt(0)\n"
    "v_exp_f32 v98, v98\n"
    "v_exp_f32 v99, v99\n"
    "v_exp_f32 v100, v100\n"
    "v_exp_f32 v101, v101\n"
    "v_mfma_f32_32x32x16_bf16 v[32:47], v[148:151], v[128:131], v[32:47]\n"
    "buffer_load_dwordx4 a[120:123], v160, %[rs], s42 offen offset:3072\n"
    "v_exp_f32 v102, v102\n"
    "v_exp_f32 v103, v103\n"
    "v_exp_f32 v104, v104\n"
    "v_exp_f32 v105, v105\n"
    "v_smfmac_f32_32x32x32_bf16 v[0:15], v[184:187], v[96:103], v143\n"
    "ds_read_b128 v[188:191], v174 offset:44800\n"
    "v_exp_f32 v106, v106\n"
    "v_exp_f32 v107, v107\n"
    "v_exp_f32 v108, v108\n"
    "v_exp_f32 v109, v109\n"
    "v_mfma_f32_32x32x16_bf16 v[48:63], v[148:151], v[132:135], v[48:63]\n"
    "ds_read_b128 v[148:151], v172 offset:46080\n"
    "v_exp_f32 v110, v110\n"
    "v_exp_f32 v111, v111\n"
    "v_exp_f32 v112, v112\n"
    "v_exp_f32 v113, v113\n"
    "v_smfmac_f32_32x32x32_bf16 v[16:31], v[196:199], v[88:95], v143\n"
    "ds_read_b128 v[196:199], v174 offset:45440\n"
    "v_exp_f32 v114, v114\n"
    "v_exp_f32 v115, v115\n"
    "v_exp_f32 v116, v116\n"
    "v_exp_f32 v117, v117\n"
    "v_mfma_f32_32x32x4_2b_bf16 v[64:95], v[180:181], v[136:137], 0\n"
    "ds_read_b128 v[176:179], v142 offset:8192\n"
    "v_exp_f32 v118, v118\n"
    "v_exp_f32 v119, v119\n"
    "v_exp_f32 v120, v120\n"
    "v_exp_f32 v121, v121\n"
    "v_exp_f32 v122, v122\n"
    "v_exp_f32 v123, v123\n"
    "v_exp_f32 v124, v124\n"
    "v_exp_f32 v125, v125\n"
    "v_smfmac_f32_32x32x32_bf16 v[16:31], v[184:187], v[112:119], v143\n"
    "buffer_load_dwordx4 a[124:127], v160, %[rs], s43 offen offset:3072\n"
    "v_exp_f32 v126, v126\n"
    "v_exp_f32 v127, v127\n"
    "v_exp_f32 v32, v32\n"
    "v_exp_f32 v33, v33\n"
    "v_smfmac_f32_32x32x32_bf16 v[0:15], v[192:195], v[104:111], v143\n"
    "s_waitcnt lgkmcnt(1)\n"
    "v_exp_f32 v34, v34\n"
    "v_exp_f32 v35, v35\n"
    "v_exp_f32 v36, v36\n"
    "v_exp_f32 v37, v37\n"
    "v_mfma_f32_32x32x16_bf16 v[64:79], v[144:147], v[128:131], v[64:79]\n"
    "s_add_u32 s42, s27, 16384\n"
    "buffer_load_dwordx4 a[128:131], v160, %[rs], s42 offen offset:0\n"
    "v_exp_f32 v38, v38\n"
    "v_exp_f32 v39, v39\n"
    "v_exp_f32 v40, v40\n"
    "v_exp_f32 v41, v41\n"
    "v_smfmac_f32_32x32x32_bf16 v[0:15], v[188:191], v[32:39], v143\n"
    "ds_read_b128 v[184:187], v174 offset:46080\n"
    "v_exp_f32 v42, v42\n"
    "v_exp_f32 v43, v43\n"
    "v_exp_f32 v44, v44\n"
    "v_exp_f32 v45, v45\n"
    "v_mfma_f32_32x32x16_bf16 v[80:95], v[144:147], v[132:135], v[80:95]\n"
    "ds_read_b128 v[144:147], v172 offset:47104\n"
    "v_exp_f32 v46, v46\n"
    "v_exp_f32 v47, v47\n"
    "v_exp_f32 v48, v48\n"
    "v_exp_f32 v49, v49\n"
    "v_smfmac_f32_32x32x32_bf16 v[16:31], v[192:195], v[120:127], v143\n"
    "ds_read_b128 v[192:195], v174 offset:46720\n"
    "v_exp_f32 v50, v50\n"
    "v_exp_f32 v51, v51\n"
    "v_exp_f32 v52, v52\n"
    "v_exp_f32 v53, v53\n"
    "v_mfma_f32_32x32x4_2b_bf16 v[96:127], v[180:181], v[138:139], 0\n"
    "s_add_u32 s43, s27, 34816\n"
    "buffer_load_dwordx4 a[132:135], v160, %[rs], s43 offen offset:0\n"
    "v_exp_f32 v54, v54\n"
    "v_exp_f32 v55, v55\n"
    "v_exp_f32 v56, v56\n"
    "v_exp_f32 v57, v57\n"
    "v_exp_f32 v58, v58\n"
    "v_exp_f32 v59, v59\n"
    "v_exp_f32 v60, v60\n"
    "v_exp_f32 v61, v61\n"
    "v_smfmac_f32_32x32x32_bf16 v[16:31], v[188:191], v[48:55], v143\n"
    "buffer_load_dwordx4 a[136:139], v160, %[rs], s42 offen offset:1024\n"
    "v_exp_f32 v62, v62\n"
    "v_exp_f32 v63, v63\n"
    "v_exp_f32 v64, v64\n"
    "v_exp_f32 v65, v65\n"
    "v_smfmac_f32_32x32x32_bf16 v[0:15], v[196:199], v[40:47], v143\n"
    "s_waitcnt lgkmcnt(0)\n"
    "v_exp_f32 v66, v66\n"
    "v_exp_f32 v67, v67\n"
    "v_exp_f32 v68, v68\n"
    "v_exp_f32 v69, v69\n"
    "v_mfma_f32_32x32x16_bf16 v[96:111], v[148:151], v[128:131], v[96:111]\n"
    "v_lshlrev_b32 v140, 3, v160\n"
    "buffer_load_dword v200, v140, %[rc], %[oc] offen\n"
    "v_exp_f32 v70, v70\n"
    "v_exp_f32 v71, v71\n"
    "v_exp_f32 v72, v72\n"
    "v_exp_f32 v73, v73\n"
    "v_smfmac_f32_32x32x32_bf16 v[0:15], v[184:187], v[64:71], v143\n"
    "ds_read_b128 v[188:191], v174 offset:47360\n"
    "v_exp_f32 v74, v74\n"
    "v_exp_f32 v75, v75\n"
    "v_exp_f32 v76, v76\n"
    "v_exp_f32 v77, v77\n"
    "v_mfma_f32_32x32x16_bf16 v[112:127], v[148:151], v[132:135], v[112:127]\n"
    "ds_read_b128 v[148:151], v172 offset:48128\n"
    "v_exp_f32 v78, v78\n"
    "v_exp_f32 v79, v79\n"
    "v_exp_f32 v80, v80\n"
    "v_exp_f32 v81, v81\n"
    "v_smfmac_f32_32x32x32_bf16 v[16:31], v[196:199], v[56:63], v143\n"
    "ds_read_b128 v[196:199], v174 offset:48000\n"
    "v_exp_f32 v82, v82\n"
    "v_exp_f32 v83, v83\n"
    "v_exp_f32 v84, v84\n"
    "v_exp_f32 v85, v85\n"
    "v_mfma_f32_32x32x4_2b_bf16 v[32:63], v[182:183], v[136:137], 0\n"
    "buffer_load_dwordx4 a[140:143], v160, %[rs], s43 offen offset:1024\n"
    "v_exp_f32 v86, v86\n"
    "v_exp_f32 v87, v87\n"
    "v_exp_f32 v88, v88\n"
    "v_exp_f32 v89, v89\n"
    "v_exp_f32 v90, v90\n"
    "v_exp_f32 v91, v91\n"
    "v_exp_f32 v92, v92\n"
    "v_exp_f32 v93, v93\n"
    "v_smfmac_f32_32x32x32_bf16 v[16:31], v[184:187], v[80:87], v143\n"
    "v_exp_f32 v94, v94\n"
    "v_exp_f32 v95, v95\n"
    "v_exp_f32 v96, v96\n"
    "v_exp_f32 v97, v97\n"
    "v_smfmac_f32_32x32x32_bf16 v[0:15], v[192:195], v[72:79], v143\n"
    "s_waitcnt lgkmcnt(0)\n"
    "v_exp_f32 v98, v98\n"
    "v_exp_f32 v99, v99\n"
    "v_exp_f32 v100, v100\n"
    "v_exp_f32 v101, v101\n"
    "v_mfma_f32_32x32x16_bf16 v[32:47], v[144:147], v[128:131], v[32:47]\n"
    "v_exp_f32 v102, v102\n"
    "v_exp_f32 v103, v103\n"
    "v_exp_f32 v104, v104\n"
    "v_exp_f32 v105, v105\n"
    "v_smfmac_f32_32x32x32_bf16 v[0:15], v[188:191], v[96:103], v143\n"
    "ds_read_b128 v[184:187], v174 offset:48640\n"
    "v_exp_f32 v106, v106\n"
    "v_exp_f32 v107, v107\n"
    "v_exp_f32 v108, v108\n"
    "v_exp_f32 v109, v109\n"
    "v_mfma_f32_32x32x16_bf16 v[48:63], v[144:147], v[132:135], v[48:63]\n"
    "ds_read_b128 v[144:147], v173 offset:0\n"
    "v_exp_f32 v110, v110\n"
    "v_exp_f32 v111, v111\n"
    "v_exp_f32 v112, v112\n"
    "v_exp_f32 v113, v113\n"
    "v_smfmac_f32_32x32x32_bf16 v[16:31], v[192:195], v[88:95], v143\n"
    "ds_read_b128 v[192:195], v174 offset:49280\n"
    "v_exp_f32 v114, v114\n"
    "v_exp_f32 v115, v115\n"
    "v_exp_f32 v116, v116\n"
    "v_exp_f32 v117, v117\n"
    "v_mfma_f32_32x32x4_2b_bf16 v[64:95], v[182:183], v[138:139], 0\n"
    "v_exp_f32 v118, v118\n"
    "v_exp_f32 v119, v119\n"
    "v_exp_f32 v120, v120\n"
    "v_exp_f32 v121, v121\n"
    "v_exp_f32 v122, v122\n"
    "v_exp_f32 v123, v123\n"
    "v_exp_f32 v124, v124\n"
    "v_exp_f32 v125, v125\n"
    "v_smfmac_f32_32x32x32_bf16 v[16:31], v[188:191], v[112:119], v143\n"
    "v_exp_f32 v126, v126\n"
    "v_exp_f32 v127, v127\n"
    "v_exp_f32 v32, v32\n"
    "v_exp_f32 v33, v33\n"
    "v_smfmac_f32_32x32x32_bf16 v[0:15], v[196:199], v[104:111], v143\n"
    "s_waitcnt lgkmcnt(0)\n"
    "v_exp_f32 v34, v34\n"
    "v_exp_f32 v35, v35\n"
    "v_exp_f32 v36, v36\n"
    "v_exp_f32 v37, v37\n"
    "v_mfma_f32_32x32x16_bf16 v[64:79], v[148:151], v[128:131], v[64:79]\n"
    "v_exp_f32 v38, v38\n"
    "v_exp_f32 v39, v39\n"
    "v_exp_f32 v40, v40\n"
    "v_exp_f32 v41, v41\n"
    "v_smfmac_f32_32x32x32_bf16 v[0:15], v[184:187], v[32:39], v143\n"
    "ds_read_b128 v[188:191], v174 offset:49920\n"
    "v_exp_f32 v42, v42\n"
    "v_exp_f32 v43, v43\n"
    "v_exp_f32 v44, v44\n"
    "v_exp_f32 v45, v45\n"
    "v_mfma_f32_32x32x16_bf16 v[80:95], v[148:151], v[132:135], v[80:95]\n"
    "ds_read_b128 v[148:151], v173 offset:1024\n"
    "v_exp_f32 v46, v46\n"
    "v_exp_f32 v47, v47\n"
    "v_exp_f32 v48, v48\n"
    "v_exp_f32 v49, v49\n"
    "v_smfmac_f32_32x32x32_bf16 v[16:31], v[196:199], v[120:127], v143\n"
    "ds_read_b128 v[196:199], v174 offset:50560\n"
    "v_exp_f32 v50, v50\n"
    "v_exp_f32 v51, v51\n"
    "v_exp_f32 v52, v52\n"
    "v_exp_f32 v53, v53\n"
    "v_mfma_f32_32x32x4_2b_bf16 v[96:127], v[176:177], v[136:137], 0\n"
    "ds_read_b128 v[180:183], v142 offset:8704\n"
    "v_exp_f32 v54, v54\n"
    "v_exp_f32 v55, v55\n"
    "v_exp_f32 v56, v56\n"
    "v_exp_f32 v57, v57\n"
    "v_exp_f32 v58, v58\n"
    "v_exp_f32 v59, v59\n"
    "v_exp_f32 v60, v60\n"
    "v_exp_f32 v61, v61\n"
    "v_smfmac_f32_32x32x32_bf16 v[16:31], v[184:187], v[48:55], v143\n"
    "v_exp_f32 v62, v62\n"
    "v_exp_f32 v63, v63\n"
    "v_exp_f32 v64, v64\n"
    "v_exp_f32 v65, v65\n"
    "v_smfmac_f32_32x32x32_bf16 v[0:15], v[192:195], v[40:47], v143\n"
    "s_waitcnt lgkmcnt(1)\n"
    "v_exp_f32 v66, v66\n"
    "v_exp_f32 v67, v67\n"
    "v_exp_f32 v68, v68\n"
    "v_exp_f32 v69, v69\n"
    "v_mfma_f32_32x32x16_bf16 v[96:111], v[144:147], v[128:131], v[96:111]\n"
    "s_mov_b32 m0, s33\n"
    "s_add_u32 s40, s31, 0\n"
    "buffer_load_dwordx4 v160, %[rs], s40 offen sc1 lds\n"
    "v_exp_f32 v70, v70\n"
    "v_exp_f32 v71, v71\n"
    "v_exp_f32 v72, v72\n"
    "v_exp_f32 v73, v73\n"
    "v_smfmac_f32_32x32x32_bf16 v[0:15], v[188:191], v[64:71], v143\n"
    "ds_read_b128 v[184:187], v175 offset:0\n"
    "v_exp_f32 v74, v74\n"
    "v_exp_f32 v75, v75\n"
    "v_exp_f32 v76, v76\n"
    "v_exp_f32 v77, v77\n"
    "v_mfma_f32_32x32x16_bf16 v[112:127], v[144:147], v[132:135], v[112:127]\n"
    "ds_read_b128 v[144:147], v173 offset:2048\n"
    "v_exp_f32 v78, v78\n"
    "v_exp_f32 v79, v79\n"
    "v_exp_f32 v80, v80\n"
    "v_exp_f32 v81, v81\n"
    "v_smfmac_f32_32x32x32_bf16 v[16:31], v[192:195], v[56:63], v143\n"
    "ds_read_b128 v[192:195], v175 offset:640\n"
    "v_exp_f32 v82, v82\n"
    "v_exp_f32 v83, v83\n"
    "v_exp_f32 v84, v84\n"
    "v_exp_f32 v85, v85\n"
    "v_mfma_f32_32x32x4_2b_bf16 v[32:63], v[176:177], v[138:139], 0\n"
    "s_add_u32 s40, s31, 131072\n"
    "buffer_load_dwordx4 v160, %[rs], s40 offen sc1 lds\n"
    "v_exp_f32 v86, v86\n"
    "v_exp_f32 v87, v87\n"
    "v_exp_f32 v88, v88\n"
    "v_exp_f32 v89, v89\n"
    "v_exp_f32 v90, v90\n"
    "v_exp_f32 v91, v91\n"
    "v_exp_f32 v92, v92\n"
    "v_exp_f32 v93, v93\n"
    "v_smfmac_f32_32x32x32_bf16 v[16:31], v[188:191], v[80:87], v143\n"
    "v_exp_f32 v94, v94\n"
    "v_exp_f32 v95, v95\n"
    "v_exp_f32 v96, v96\n"
    "v_exp_f32 v97, v97\n"
    "v_smfmac_f32_32x32x32_bf16 v[0:15], v[196:199], v[72:79], v143\n"
    "s_waitcnt lgkmcnt(0)\n"
    "v_exp_f32 v98, v98\n"
    "v_exp_f32 v99, v99\n"
    "v_exp_f32 v100, v100\n"
    "v_exp_f32 v101, v101\n"
    "v_mfma_f32_32x32x16_bf16 v[32:47], v[148:151], v[128:131], v[32:47]\n"
    "s_add_u32 s40, s31, 262144\n"
    "buffer_load_dwordx4 v160, %[rs], s40 offen sc1 lds\n"
    "v_exp_f32 v102, v102\n"
    "v_exp_f32 v103, v103\n"
    "v_exp_f32 v104, v104\n"
    "v_exp_f32 v105, v105\n"
    "v_smfmac_f32_32x32x32_bf16 v[0:15], v[184:187], v[96:103], v143\n"
    "ds_read_b128 v[188:191], v175 offset:1280\n"
    "v_exp_f32 v106, v106\n"
    "v_exp_f32 v107, v107\n"
    "v_exp_f32 v108, v108\n"
    "v_exp_f32 v109, v109\n"
    "v_mfma_f32_32x32x16_bf16 v[48:63], v[148:151], v[132:135], v[48:63]\n"
    "ds_read_b128 v[148:151], v173 offset:3072\n"
    "v_exp_f32 v110, v110\n"
    "v_exp_f32 v111, v111\n"
    "v_exp_f32 v112, v112\n"
    "v_exp_f32 v113, v113\n"
    "v_smfmac_f32_32x32x32_bf16 v[16:31], v[196:199], v[88:95], v143\n"
    "ds_read_b128 v[196:199], v175 offset:1920\n"
    "v_exp_f32 v114, v114\n"
    "v_exp_f32 v115, v115\n"
    "v_exp_f32 v116, v116\n"
    "v_exp_f32 v117, v117\n"
    "v_mfma_f32_32x32x4_2b_bf16 v[64:95], v[178:179], v[136:137], 0\n"
    "s_add_u32 s40, s31, 393216\n"
    "buffer_load_dwordx4 v160, %[rs], s40 offen sc1 lds\n"
    "v_exp_f32 v118, v118\n"
    "v_exp_f32 v119, v119\n"
    "v_exp_f32 v120, v120\n"
    "v_exp_f32 v121, v121\n"
    "v_exp_f32 v122, v122\n"
    "v_exp_f32 v123, v123\n"
    "v_exp_f32 v124, v124\n"
    "v_exp_f32 v125, v125\n"
    "v_smfmac_f32_32x32x32_bf16 v[16:31], v[184:187], v[112:119], v143\n"
    "v_exp_f32 v126, v126\n"
    "v_exp_f32 v127, v127\n"
    "v_exp_f32 v32, v32\n"
    "v_exp_f32 v33, v33\n"
    "v_smfmac_f32_32x32x32_bf16 v[0:15], v[192:195], v[104:111], v143\n"
    "s_waitcnt lgkmcnt(0)\n"
    "v_exp_f32 v34, v34\n"
    "v_exp_f32 v35, v35\n"
    "v_exp_f32 v36, v36\n"
    "v_exp_f32 v37, v37\n"
    "v_mfma_f32_32x32x16_bf16 v[64:79], v[144:147], v[128:131], v[64:79]\n"
    "s_add_u32 s40, s31, 524288\n"
    "buffer_load_dwordx4 v160, %[rs], s40 offen sc1 lds\n"
    "v_exp_f32 v38, v38\n"
    "v_exp_f32 v39, v39\n"
    "v_exp_f32 v40, v40\n"
    "v_exp_f32 v41, v41\n"
    "v_smfmac_f32_32x32x32_bf16 v[0:15], v[188:191], v[32:39], v143\n"
    "ds_read_b128 v[184:187], v175 offset:2560\n"
    "v_exp_f32 v42, v42\n"
    "v_exp_f32 v43, v43\n"
    "v_exp_f32 v44, v44\n"
    "v_exp_f32 v45, v45\n"
    "v_mfma_f32_32x32x16_bf16 v[80:95], v[144:147], v[132:135], v[80:95]\n"
    "ds_read_b128 v[144:147], v173 offset:4096\n"
    "v_exp_f32 v46, v46\n"
    "v_exp_f32 v47, v47\n"
    "v_exp_f32 v48, v48\n"
    "v_exp_f32 v49, v49\n"
    "v_smfmac_f32_32x32x32_bf16 v[16:31], v[192:195], v[120:127], v143\n"
    "ds_read_b128 v[192:195], v175 offset:3200\n"
    "v_exp_f32 v50, v50\n"
    "v_exp_f32 v51, v51\n"
    "v_exp_f32 v52, v52\n"
    "v_exp_f32 v53, v53\n"
    "v_mfma_f32_32x32x4_2b_bf16 v[96:127], v[178:179], v[138:139], 0\n"
    "s_add_u32 s40, s31, 655360\n"
    "buffer_load_dwordx4 v160, %[rs], s40 offen sc1 lds\n"
    "v_exp_f32 v54, v54\n"
    "v_exp_f32 v55, v55\n"
    "v_exp_f32 v56, v56\n"
    "v_exp_f32 v57, v57\n"
    "v_exp_f32 v58, v58\n"
    "v_exp_f32 v59, v59\n"
    "v_exp_f32 v60, v60\n"
    "v_exp_f32 v61, v61\n"
    "v_smfmac_f32_32x32x32_bf16 v[16:31], v[188:191], v[48:55], v143\n"
    "v_exp_f32 v62, v62\n"
    "v_exp_f32 v63, v63\n"
    "v_exp_f32 v64, v64\n"
    "v_exp_f32 v65, v65\n"
    "v_smfmac_f32_32x32x32_bf16 v[0:15], v[196:199], v[40:47], v143\n"
    "s_waitcnt lgkmcnt(0)\n"
    "v_exp_f32 v66, v66\n"
    "v_exp_f32 v67, v67\n"
    "v_exp_f32 v68, v68\n"
    "v_exp_f32 v69, v69\n"
    "v_mfma_f32_32x32x16_bf16 v[96:111], v[148:151], v[128:131], v[96:111]\n"
    "s_add_u32 s40, s31, 786432\n"
    "buffer_load_dwordx4 v160, %[rs], s40 offen sc1 lds\n"
    "v_exp_f32 v70, v70\n"
    "v_exp_f32 v71, v71\n"
    "v_exp_f32 v72, v72\n"
    "v_exp_f32 v73, v73\n"
    "v_smfmac_f32_32x32x32_bf16 v[0:15], v[184:187], v[64:71], v143\n"
    "ds_read_b128 v[188:191], v175 offset:3840\n"
    "v_exp_f32 v74, v74\n"
    "v_exp_f32 v75, v75\n"
    "v_exp_f32 v76, v76\n"
    "v_exp_f32 v77, v77\n"
    "v_mfma_f32_32x32x16_bf16 v[112:127], v[148:151], v[132:135], v[112:127]\n"
    "ds_read_b128 v[148:151], v173 offset:5120\n"
    "v_exp_f32 v78, v78\n"
    "v_exp_f32 v79, v79\n"
    "v_exp_f32 v80, v80\n"
    "v_exp_f32 v81, v81\n"
    "v_smfmac_f32_32x32x32_bf16 v[16:31], v[196:199], v[56:63], v143\n"
    "ds_read_b128 v[196:199], v175 offset:4480\n"
    "v_exp_f32 v82, v82\n"
    "v_exp_f32 v83, v83\n"
    "v_exp_f32 v84, v84\n"
    "v_exp_f32 v85, v85\n"
    "v_mfma_f32_32x32x4_2b_bf16 v[32:63], v[180:181], v[136:137], 0\n"
    "v_exp_f32 v86, v86\n"
    "v_exp_f32 v87, v87\n"
    "v_exp_f32 v88, v88\n"
    "v_exp_f32 v89, v89\n"
    "v_exp_f32 v90, v90\n"
    "v_exp_f32 v91, v91\n"
    "v_exp_f32 v92, v92\n"
    "v_exp_f32 v93, v93\n"
    "v_smfmac_f32_32x32x32_bf16 v[16:31], v[184:187], v[80:87], v143\n"
    "s_add_u32 s40, s31, 917504\n"
    "buffer_load_dwordx4 v160, %[rs], s40 offen sc1 lds\n"
    "v_exp_f32 v94, v94\n"
    "v_exp_f32 v95, v95\n"
    "v_exp_f32 v96, v96\n"
    "v_exp_f32 v97, v97\n"
    "v_smfmac_f32_32x32x32_bf16 v[0:15], v[192:195], v[72:79], v143\n"
    "s_waitcnt lgkmcnt(0)\n"
    "v_exp_f32 v98, v98\n"
    "v_exp_f32 v99, v99\n"
    "v_exp_f32 v100, v100\n"
    "v_exp_f32 v101, v101\n"
    "v_mfma_f32_32x32x16_bf16 v[32:47], v[144:147], v[128:131], v[32:47]\n"
    "s_add_u32 s40, s31, 1048576\n"
    "buffer_load_dwordx4 v160, %[rs], s40 offen sc1 lds\n"
    "v_exp_f32 v102, v102\n"
    "v_exp_f32 v103, v103\n"
    "v_exp_f32 v104, v104\n"
    "v_exp_f32 v105, v105\n"
    "v_smfmac_f32_32x32x32_bf16 v[0:15], v[188:191], v[96:103], v143\n"
    "ds_read_b128 v[184:187], v175 offset:5120\n"
    "v_exp_f32 v106, v106\n"
    "v_exp_f32 v107, v107\n"
    "v_exp_f32 v108, v108\n"
    "v_exp_f32 v109, v109\n"
    "v_mfma_f32_32x32x16_bf16 v[48:63], v[144:147], v[132:135], v[48:63]\n"
    "ds_read_b128 v[144:147], v173 offset:6144\n"
    "v_exp_f32 v110, v110\n"
    "v_exp_f32 v111, v111\n"
    "v_exp_f32 v112, v112\n"
    "v_exp_f32 v113, v113\n"
    "v_smfmac_f32_32x32x32_bf16 v[16:31], v[192:195], v[88:95], v143\n"
    "ds_read_b128 v[192:195], v175 offset:5760\n"
    "v_exp_f32 v114, v114\n"
    "v_exp_f32 v115, v115\n"
    "v_exp_f32 v116, v116\n"
    "v_exp_f32 v117, v117\n"
    "v_mfma_f32_32x32x4_2b_bf16 v[64:95], v[180:181], v[138:139], 0\n"
    "s_add_u32 s40, s31, 1179648\n"
    "buffer_load_dwordx4 v160, %[rs], s40 offen sc1 lds\n"
    "v_exp_f32 v118, v118\n"
    "v_exp_f32 v119, v119\n"
    "v_exp_f32 v120, v120\n"
    "v_exp_f32 v121, v121\n"
    "v_exp_f32 v122, v122\n"
    "v_exp_f32 v123, v123\n"
    "v_exp_f32 v124, v124\n"
    "v_exp_f32 v125, v125\n"
    "v_smfmac_f32_32x32x32_bf16 v[16:31], v[188:191], v[112:119], v143\n"
    "v_exp_f32 v126, v126\n"
    "v_exp_f32 v127, v127\n"
    "v_exp_f32 v32, v32\n"
    "v_exp_f32 v33, v33\n"
    "v_smfmac_f32_32x32x32_bf16 v[0:15], v[196:199], v[104:111], v143\n"
    "s_waitcnt lgkmcnt(0)\n"
    "v_exp_f32 v34, v34\n"
    "v_exp_f32 v35, v35\n"
    "v_exp_f32 v36, v36\n"
    "v_exp_f32 v37, v37\n"
    "v_mfma_f32_32x32x16_bf16 v[64:79], v[148:151], v[128:131], v[64:79]\n"
    "s_add_u32 s40, s31, 1310720\n"
    "buffer_load_dwordx4 v160, %[rs], s40 offen sc1 lds\n"
    "v_exp_f32 v38, v38\n"
    "v_exp_f32 v39, v39\n"
    "v_exp_f32 v40, v40\n"
    "v_exp_f32 v41, v41\n"
    "v_smfmac_f32_32x32x32_bf16 v[0:15], v[184:187], v[32:39], v143\n"
    "ds_read_b128 v[188:191], v175 offset:6400\n"
    "v_exp_f32 v42, v42\n"
    "v_exp_f32 v43, v43\n"
    "v_exp_f32 v44, v44\n"
    "v_exp_f32 v45, v45\n"
    "v_mfma_f32_32x32x16_bf16 v[80:95], v[148:151], v[132:135], v[80:95]\n"
    "ds_read_b128 v[148:151], v173 offset:7168\n"
    "v_exp_f32 v46, v46\n"
    "v_exp_f32 v47, v47\n"
    "v_exp_f32 v48, v48\n"
    "v_exp_f32 v49, v49\n"
    "v_smfmac_f32_32x32x32_bf16 v[16:31], v[196:199], v[120:127], v143\n"
    "ds_read_b128 v[196:199], v175 offset:7040\n"
    "v_exp_f32 v50, v50\n"
    "v_exp_f32 v51, v51\n"
    "v_exp_f32 v52, v52\n"
    "v_exp_f32 v53, v53\n"
    "v_mfma_f32_32x32x4_2b_bf16 v[96:127], v[182:183], v[136:137], 0\n"
    "v_exp_f32 v54, v54\n"
    "v_exp_f32 v55, v55\n"
    "v_exp_f32 v56, v56\n"
    "v_exp_f32 v57, v57\n"
    "v_exp_f32 v58, v58\n"
    "v_exp_f32 v59, v59\n"
    "v_exp_f32 v60, v60\n"
    "v_exp_f32 v61, v61\n"
    "v_smfmac_f32_32x32x32_bf16 v[16:31], v[184:187], v[48:55], v143\n"
    "v_exp_f32 v62, v62\n"
    "v_exp_f32 v63, v63\n"
    "v_exp_f32 v64, v64\n"
    "v_exp_f32 v65, v65\n"
    "v_smfmac_f32_32x32x32_bf16 v[0:15], v[192:195], v[40:47], v143\n"
    "s_waitcnt lgkmcnt(0)\n"
    "v_exp_f32 v66, v66\n"
    "v_exp_f32 v67, v67\n"
    "v_exp_f32 v68, v68\n"
    "v_exp_f32 v69, v69\n"
    "v_mfma_f32_32x32x16_bf16 v[96:111], v[144:147], v[128:131], v[96:111]\n"
    "v_exp_f32 v70, v70\n"
    "v_exp_f32 v71, v71\n"
    "v_exp_f32 v72, v72\n"
    "v_exp_f32 v73, v73\n"
    "v_smfmac_f32_32x32x32_bf16 v[0:15], v[188:191], v[64:71], v143\n"
    "ds_read_b128 v[184:187], v175 offset:7680\n"
    "v_exp_f32 v74, v74\n"
    "v_exp_f32 v75, v75\n"
    "v_exp_f32 v76, v76\n"
    "v_exp_f32 v77, v77\n"
    "v_mfma_f32_32x32x16_bf16 v[112:127], v[144:147], v[132:135], v[112:127]\n"
    "v_exp_f32 v78, v78\n"
    "v_exp_f32 v79, v79\n"
    "v_exp_f32 v80, v80\n"
    "v_exp_f32 v81, v81\n"
    "v_smfmac_f32_32x32x32_bf16 v[16:31], v[192:195], v[56:63], v143\n"
    "ds_read_b128 v[192:195], v175 offset:8320\n"
    "v_exp_f32 v82, v82\n"
    "v_exp_f32 v83, v83\n"
    "v_exp_f32 v84, v84\n"
    "v_exp_f32 v85, v85\n"
    "v_mfma_f32_32x32x4_2b_bf16 v[32:63], v[182:183], v[138:139], 0\n"
    "v_exp_f32 v86, v86\n"
    "v_exp_f32 v87, v87\n"
    "v_exp_f32 v88, v88\n"
    "v_exp_f32 v89, v89\n"
    "v_exp_f32 v90, v90\n"
    "v_exp_f32 v91, v91\n"
    "v_exp_f32 v92, v92\n"
    "v_exp_f32 v93, v93\n"
    "v_smfmac_f32_32x32x32_bf16 v[16:31], v[188:191], v[80:87], v143\n"
    "v_exp_f32 v94, v94\n"
    "v_exp_f32 v95, v95\n"
    "v_exp_f32 v96, v96\n"
    "v_exp_f32 v97, v97\n"
    "v_smfmac_f32_32x32x32_bf16 v[0:15], v[196:199], v[72:79], v143\n"
    "s_waitcnt lgkmcnt(0)\n"
    "v_exp_f32 v98, v98\n"
    "v_exp_f32 v99, v99\n"
    "v_exp_f32 v100, v100\n"
    "v_exp_f32 v101, v101\n"
    "v_mfma_f32_32x32x16_bf16 v[32:47], v[148:151], v[128:131], v[32:47]\n"
    "buffer_load_dword v140, v171, %[rs], s30 offen sc1\n"
    "v_exp_f32 v102, v102\n"
    "v_exp_f32 v103, v103\n"
    "v_exp_f32 v104, v104\n"
    "v_exp_f32 v105, v105\n"
    "v_smfmac_f32_32x32x32_bf16 v[0:15], v[184:187], v[96:103], v143\n"
    "ds_read_b128 v[188:191], v175 offset:8960\n"
    "v_exp_f32 v106, v106\n"
    "v_exp_f32 v107, v107\n"
    "v_exp_f32 v108, v108\n"
    "v_exp_f32 v109, v109\n"
    "v_mfma_f32_32x32x16_bf16 v[48:63], v[148:151], v[132:135], v[48:63]\n"
    "v_exp_f32 v110, v110\n"
    "v_exp_f32 v111, v111\n"
    "v_exp_f32 v112, v112\n"
    "v_exp_f32 v113, v113\n"
    "v_smfmac_f32_32x32x32_bf16 v[16:31], v[196:199], v[88:95], v143\n"
    "ds_read_b128 v[196:199], v175 offset:9600\n"
    "v_exp_f32 v114, v114\n"
    "v_exp_f32 v115, v115\n"
    "v_exp_f32 v116, v116\n"
    "v_exp_f32 v117, v117\n"
    "v_exp_f32 v118, v118\n"
    "v_exp_f32 v119, v119\n"
    "v_exp_f32 v120, v120\n"
    "v_exp_f32 v121, v121\n"
    "v_exp_f32 v122, v122\n"
    "v_exp_f32 v123, v123\n"
    "v_exp_f32 v124, v124\n"
    "v_exp_f32 v125, v125\n"
    "v_smfmac_f32_32x32x32_bf16 v[16:31], v[184:187], v[112:119], v143\n"
    "v_exp_f32 v126, v126\n"
    "v_exp_f32 v127, v127\n"
    "v_exp_f32 v32, v32\n"
    "v_exp_f32 v33, v33\n"
    "v_smfmac_f32_32x32x32_bf16 v[0:15], v[192:195], v[104:111], v143\n"
    "s_waitcnt lgkmcnt(0)\n"
    "v_exp_f32 v34, v34\n"
    "v_exp_f32 v35, v35\n"
    "v_exp_f32 v36, v36\n"
    "v_exp_f32 v37, v37\n"
    "v_exp_f32 v38, v38\n"
    "v_exp_f32 v39, v39\n"
    "v_exp_f32 v40, v40\n"
    "v_exp_f32 v41, v41\n"
    "v_smfmac_f32_32x32x32_bf16 v[0:15], v[188:191], v[32:39], v143\n"
    "v_exp_f32 v42, v42\n"
    "v_exp_f32 v43, v43\n"
    "v_exp_f32 v44, v44\n"
    "v_exp_f32 v45, v45\n"
    "v_exp_f32 v46, v46\n"
    "v_exp_f32 v47, v47\n"
    "v_exp_f32 v48, v48\n"
    "v_exp_f32 v49, v49\n"
    "v_smfmac_f32_32x32x32_bf16 v[16:31], v[192:195], v[120:127], v143\n"
    "v_exp_f32 v50, v50\n"
    "v_exp_f32 v51, v51\n"
    "v_exp_f32 v52, v52\n"
    "v_exp_f32 v53, v53\n"
    "v_exp_f32 v54, v54\n"
    "v_exp_f32 v55, v55\n"
    "v_exp_f32 v56, v56\n"
    "v_exp_f32 v57, v57\n"
    "v_exp_f32 v58, v58\n"
    "v_exp_f32 v59, v59\n"
    "v_exp_f32 v60, v60\n"
    "v_exp_f32 v61, v61\n"
    "v_smfmac_f32_32x32x32_bf16 v[16:31], v[188:191], v[48:55], v143\n"
    "v_exp_f32 v62, v62\n"
    "v_exp_f32 v63, v63\n"
    "s_nop 1\n"
    "v_smfmac_f32_32x32x32_bf16 v[0:15], v[196:199], v[40:47], v143\n"
    "v_smfmac_f32_32x32x32_bf16 v[16:31], v[196:199], v[56:63], v143\n"
    "s_waitcnt vmcnt(0)\n"
    "v_cmp_ne_u32 vcc, 32, v140\n"
    "s_mov_b32 s45, 0x10000\n"
    "s_nop 3\n"
    "s_cbranch_vccz Lka_e_%=\n"
    "Lka_p_%=:\n"
    "buffer_load_dword v140, v171, %[rs], s30 offen sc1\n"
    "s_waitcnt vmcnt(0)\n"
    "v_cmp_ne_u32 vcc, 32, v140\n"
    "s_sub_u32 s45, s45, 1\n"
    "s_cbranch_scc1 Lka_e_%=\n"
    "s_sleep 8\n"
    "s_cbranch_vccnz Lka_p_%=\n"
    "Lka_e_%=:\n"
    "s_waitcnt lgkmcnt(0)\n"
    "s_nop 7\n"
    : "=&{v[0:15]}"(oA), "=&{v[16:31]}"(oB), "=&{a[0:31]}"(wa0), "=&{a[32:63]}"(wa1), "=&{a[64:95]}"(wa2), "=&{a[96:127]}"(wa3), "=&{a[128:143]}"(wa4), "=&{a[144:151]}"(rm01), "=&{a[152:155]}"(rm2x), "=&{a[156:163]}"(xr), "=&{a[164:165]}"(m1)
    : "{v[128:135]}"(qm2), "{v[136:139]}"(qx2), "{v141}"(xka[0]), "{v142}"(xka[1]), "{v[160:175]}"(vka), "{s[24:31]}"(ska), "{s[32:35]}"(skb), [rs]"s"(rws), [rt]"s"(rtl), [rc]"s"(rcode), [oc]"s"(ocode), [rx]"s"(rx), [ox]"s"(oxp), [orm]"s"((u32)OFF_RM), [om]"s"((u32)OFF_MEAN1 + token * 4), "{v[248:251]}"(vx)
    : "v32","v33","v34","v35","v36","v37","v38","v39","v40","v41","v42","v43","v44","v45","v46","v47","v48","v49","v50","v51","v52","v53","v54","v55","v56","v57","v58","v59","v60","v61","v62","v63","v64","v65","v66","v67","v68","v69","v70","v71","v72","v73","v74","v75","v76","v77","v78","v79","v80","v81","v82","v83","v84","v85","v86","v87","v88","v89","v90","v91","v92","v93","v94","v95","v96","v97","v98","v99","v100","v101","v102","v103","v104","v105","v106","v107","v108","v109","v110","v111","v112","v113","v114","v115","v116","v117","v118","v119","v120","v121","v122","v123","v124","v125","v126","v127","v140","v143","v144","v145","v146","v147","v148","v149","v150","v151","v152","v153","v154","v155","v156","v157","v158","v159","v176","v177","v178","v179","v180","v181","v182","v183","v184","v185","v186","v187","v188","v189","v190","v191","v192","v193","v194","v195","v196","v197","v198","v199","v200","v201","v202","v203","v204","v205","v206","v207","v208","v209","v210","v211","v212","v213","v214","v215","v216","v217","v218","v219","v220","v221","v222","v223","v224","v225","v226","v227","v228","v229","v230","v231","v232","v233","v234","v235","v236","v237","v238","v239","v240","v241","v242","v243","v244","v245","v246","v247","s40","s41","s42","s43","s44","s45","s46","s47","vcc","scc","memory");
    } else {
      asm volatile("s_waitcnt lgkmcnt(0)\n\ts_barrier" ::: "memory");
      { const rsrc_t rs = __builtin_amdgcn_make_buffer_rsrc((void*)(((uint64_t)rtl[1] << 32) | rtl[0]), 0, rtl[2], 0x00020000), rd = mkr(ws, (u32)WS_USED);
        for (int i = 0; i < 12; ++i) {
          *(LDSP u32x4*)(uintptr_t)(slot + l16) = __builtin_amdgcn_raw_buffer_load_b128(rs, tvo, tso + i * tst, 0);
          ST16(cat8(trread(ta), trread(ta + 512)), rd, (i < 4 ? dk[0] : i < 8 ? dk[1] : dk[2]), (i & 3) * 256); } }
      oA = f32x16{}; oB = f32x16{}; float ma = -INFINITY, mb = -INFINITY;
      for (int c = 0; c < 8; ++c) {
        asm volatile("s_waitcnt vmcnt(0) lgkmcnt(0)\n\ts_barrier" ::: "memory");
        if (c == 0 && tid == 0) __hip_atomic_fetch_add((u32*)(ws + OFF_CT + xc * 128), 1u, __ATOMIC_RELAXED, __HIP_MEMORY_SCOPE_AGENT);
        if (c + 2 < 8) KV_DMA(c + 2);
        const u32 cb = (c & 3) * KVS;
        for (int tt = 0; tt < 8; ++tt) {
          const bf16x8 km = ldsrd(ka + cb + tt * 1024), v0 = ldsrd(va + cb + (2 * tt) * 640), v1 = ldsrd(va + cb + (2 * tt + 1) * 640);
          const u32 kx = *(const LDSP u32*)(uintptr_t)(lds + KVL + cb + 8192 + ((tt >> 2) * 32 + r) * 16 + 4 * (tt & 3));
#pragma unroll
          for (int k = 0; k < 2; ++k) { f32x16& o = k ? oB : oA; float& m = k ? mb : ma;
            f32x16 s = __builtin_amdgcn_mfma_f32_32x32x16_bf16(km, __builtin_bit_cast(bf16x8, k ? qb : qa), f32x16{}, 0, 0, 0);
            s = __builtin_amdgcn_mfma_f32_32x32x8bf16_1k(__builtin_bit_cast(s16x4, u32x2{kx, 0u}), __builtin_bit_cast(s16x4, u32x2{h == 0 ? (k ? eb : ea) : 0u, 0u}), s, 0, 0, 0);
            float tm = s[0];
#pragma unroll
            for (int i = 1; i < 16; ++i) tm = fmaxf(tm, s[i]);
            tm = xmax(tm, 32);
            const float mn = fmaxf(m, tm), al = __builtin_amdgcn_exp2f(m - mn); m = mn;
            o *= al;
#pragma unroll
            for (int i = 0; i < 16; ++i) s[i] = __builtin_amdgcn_exp2f(s[i] - mn);
            const f32x8 s0 = __builtin_shufflevector(s, s, 0, 1, 2, 3, 4, 5, 6, 7), s1 = __builtin_shufflevector(s, s, 8, 9, 10, 11, 12, 13, 14, 15);
            o = __builtin_amdgcn_smfmac_f32_32x32x32_bf16(v0, __builtin_bit_cast(bf16x16, s0), o, (int)0xdddddddd, 0, 0);
            o = __builtin_amdgcn_smfmac_f32_32x32x32_bf16(v1, __builtin_bit_cast(bf16x16, s1), o, (int)0xdddddddd, 0, 0); } } }
      { const rsrc_t rc = mkr(ws + OFF_CT, 1024);
        for (int spin = 0; spin < (1 << 16); ++spin) { if (__all(__builtin_amdgcn_raw_buffer_load_b32(rc, (lane & 7) * 128, 0, 16) == 32u)) break; __builtin_amdgcn_s_sleep(2); } }
      { const float* rm = (const float*)(ws + OFF_RM) + r * 18 + 4 * h; const char* xp = (const char*)x + (size_t)rt * 36864;
        const f32x4 a = *(const f32x4*)rm, c = *(const f32x4*)(rm + 8); const u32x2 e = *(const u32x2*)(rm + 16), f = *(const u32x2*)(xp + vx[2]);
        const u32x4 x0 = *(const u32x4*)(xp + vx[0] + oxp), x1 = *(const u32x4*)(xp + vx[1] + oxp);
        m1 = *(const f32x2*)(ws + OFF_MEAN1 + token * 4);
        rm01 = __builtin_shufflevector(a, c, 0, 1, 2, 3, 4, 5, 6, 7); rm2x = u32x4{e[0], e[1], f[0], f[1]}; xr = __builtin_shufflevector(x0, x1, 0, 1, 2, 3, 4, 5, 6, 7); }
      asm volatile("s_waitcnt vmcnt(0)\n"
    "s_add_u32 s42, %[o0], 0\n"
    "buffer_load_dwordx4 a[0:3], %[l], %[rs], s42 offen offset:0\n"
    "s_add_u32 s43, %[o0], 18432\n"
    "buffer_load_dwordx4 a[4:7], %[l], %[rs], s43 offen offset:0\n"
    "buffer_load_dwordx4 a[8:11], %[l], %[rs], s42 offen offset:1024\n"
    "buffer_load_dwordx4 a[12:15], %[l], %[rs], s43 offen offset:1024\n"
    "buffer_load_dwordx4 a[16:19], %[l], %[rs], s42 offen offset:2048\n"
    "buffer_load_dwordx4 a[20:23], %[l], %[rs], s43 offen offset:2048\n"
    "buffer_load_dwordx4 a[24:27], %[l], %[rs], s42 offen offset:3072\n"
    "buffer_load_dwordx4 a[28:31], %[l], %[rs], s43 offen offset:3072\n"
    "s_add_u32 s42, %[o0], 4096\n"
    "buffer_load_dwordx4 a[32:35], %[l], %[rs], s42 offen offset:0\n"
    "s_add_u32 s43, %[o0], 22528\n"
    "buffer_load_dwordx4 a[36:39], %[l], %[rs], s43 offen offset:0\n"
    "buffer_load_dwordx4 a[40:43], %[l], %[rs], s42 offen offset:1024\n"
    "buffer_load_dwordx4 a[44:47], %[l], %[rs], s43 offen offset:1024\n"
    "buffer_load_dwordx4 a[48:51], %[l], %[rs], s42 offen offset:2048\n"
    "buffer_load_dwordx4 a[52:55], %[l], %[rs], s43 offen offset:2048\n"
    "buffer_load_dwordx4 a[56:59], %[l], %[rs], s42 offen offset:3072\n"
    "buffer_load_dwordx4 a[60:63], %[l], %[rs], s43 offen offset:3072\n"
    "s_add_u32 s42, %[o0], 8192\n"
    "buffer_load_dwordx4 a[64:67], %[l], %[rs], s42 offen offset:0\n"
    "s_add_u32 s43, %[o0], 26624\n"
    "buffer_load_dwordx4 a[68:71], %[l], %[rs], s43 offen offset:0\n"
    "buffer_load_dwordx4 a[72:75], %[l], %[rs], s42 offen offset:1024\n"
    "buffer_load_dwordx4 a[76:79], %[l], %[rs], s43 offen offset:1024\n"
    "buffer_load_dwordx4 a[80:83], %[l], %[rs], s42 offen offset:2048\n"
    "buffer_load_dwordx4 a[84:87], %[l], %[rs], s43 offen offset:2048\n"
    "buffer_load_dwordx4 a[88:91], %[l], %[rs], s42 offen offset:3072\n"
    "buffer_load_dwordx4 a[92:95], %[l], %[rs], s43 offen offset:3072\n"
    "s_add_u32 s42, %[o0], 12288\n"
    "buffer_load_dwordx4 a[96:99], %[l], %[rs], s42 offen offset:0\n"
    "s_add_u32 s43, %[o0], 30720\n"
    "buffer_load_dwordx4 a[100:103], %[l], %[rs], s43 offen offset:0\n"
    "buffer_load_dwordx4 a[104:107], %[l], %[rs], s42 offen offset:1024\n"
    "buffer_load_dwordx4 a[108:111], %[l], %[rs], s43 offen offset:1024\n"
    "buffer_load_dwordx4 a[112:115], %[l], %[rs], s42 offen offset:2048\n"
    "buffer_load_dwordx4 a[116:119], %[l], %[rs], s43 offen offset:2048\n"
    "buffer_load_dwordx4 a[120:123], %[l], %[rs], s42 offen offset:3072\n"
    "buffer_load_dwordx4 a[124:127], %[l], %[rs], s43 offen offset:3072\n"
    "s_add_u32 s42, %[o0], 16384\n"
    "buffer_load_dwordx4 a[128:131], %[l], %[rs], s42 offen offset:0\n"
    "s_add_u32 s43, %[o0], 34816\n"
    "buffer_load_dwordx4 a[132:135], %[l], %[rs], s43 offen offset:0\n"
    "buffer_load_dwordx4 a[136:139], %[l], %[rs], s42 offen offset:1024\n"
    "buffer_load_dwordx4 a[140:143], %[l], %[rs], s43 offen offset:1024\n"
    "s_waitcnt vmcnt(0)\n"
    : "=&{a[0:31]}"(wa0), "=&{a[32:63]}"(wa1), "=&{a[64:95]}"(wa2), "=&{a[96:127]}"(wa3), "=&{a[128:143]}"(wa4)
    : [l]"v"(l16), [rs]"s"(rws), [o0]"s"(owp)
    : "s42","s43","scc","memory");
    }
    const rsrc_t rO = mkr(ws + OFF_O + (size_t)rt * 36864, 36864);
    u32x4 om[2], oq[2];
#pragma unroll
    for (int k = 0; k < 2; ++k) { f32x16& o = k ? oB : oA;
      const auto lrw = __builtin_amdgcn_permlane32_swap(__float_as_uint(o[10]), __float_as_uint(o[10]), false, false);
      o *= __builtin_amdgcn_rcpf(__uint_as_float(lrw[0]));
      om[k] = u32x4{cvtpk(o[0], o[1]), cvtpk(o[2], o[3]), cvtpk(o[4], o[5]), cvtpk(o[6], o[7])}; const u32 ox = cvtpk(o[8], o[9]);
      ST16(om[k], rO, (lr + k) * 1152 + (r * 2 + h) * 16, 0);
      oq[k] = u32x4{ox, (u32)__builtin_amdgcn_mov_dpp((int)ox, 0x55, 0xf, 0xf, false), (u32)__builtin_amdgcn_mov_dpp((int)ox, 0xaa, 0xf, 0xf, false), (u32)__builtin_amdgcn_mov_dpp((int)ox, 0xff, 0xf, 0xf, false)};
      ST16(oq[k], rO, (lane & 35) == 0 ? (lr + k) * 1152 + 1024 + r * 4 : 0x10000000, 0); }
#define W_DMA(p) dma16s(rfl(lds + (p) * 1024), l16, rws, rfl(ow1 + (p) * 1024))
#pragma unroll
    for (int i = 0; i < 6; ++i) W_DMA(w * 6 + i);
    { const int key = (lr >> 1) & 7;
#pragma unroll
      for (int k = 0; k < 2; ++k) { const f32x16& o = k ? oB : oA;
        f32x2 d2 = (f32x2{o[0], o[1]} * f32x2{rm01[0], rm01[1]} + f32x2{o[2], o[3]} * f32x2{rm01[2], rm01[3]}) + (f32x2{o[4], o[5]} * f32x2{rm01[4], rm01[5]} + f32x2{o[6], o[7]} * f32x2{rm01[6], rm01[7]});
        if (h == 0) d2 += f32x2{o[8], o[9]} * f32x2{__uint_as_float(rm2x[0]), __uint_as_float(rm2x[1])};
        const float d = wavesum(d2[0] + d2[1]) + m1[k];
        *(LDSP u32x4*)(uintptr_t)(lds + KVL + (lr + k) * 1152 + ((r * 2 + h) ^ key) * 16) = om[k];
        if ((lane & 35) == 0) *(LDSP u32x4*)(uintptr_t)(lds + KVL + (lr + k) * 1152 + 1024 + ((r >> 2) ^ key) * 16) = oq[k];
        if (lane == 0) *(LDSP float*)(uintptr_t)(lds + L_MU + (lr + k) * 4) = d; } }
    asm volatile("s_waitcnt vmcnt(%0) lgkmcnt(0)\n\ts_barrier" :: "i"(6) : "memory");
    if (w == 0) {
      const rsrc_t rM = mkr(ws + OFF_MU + rt * 512, 512);
      if (lane < 8) __builtin_amdgcn_raw_buffer_store_b64(u32x2{*(const LDSP u32*)(uintptr_t)(lds + L_MU + (nq * 8 + lane) * 4), 1u}, rM, nq * 128 + lane * 8, 0, 16);
      const int q = (nq + 1 + (lane >> 3) % 3) & 3; u32x2 mt;
      for (int spin = 0; spin < (1 << 16); ++spin) { mt = __builtin_amdgcn_raw_buffer_load_b64(rM, q * 128 + (lane & 7) * 8, 0, 16); if (__all(mt[1] == 1u)) break; __builtin_amdgcn_s_sleep(1); }
      if (lane < 24) *(LDSP u32*)(uintptr_t)(lds + L_MU + (q * 8 + (lane & 7)) * 4) = mt[0];
    } else {
#pragma unroll
      for (int i = 0; i < 6; ++i) dma16(rfl(lds + L_WS + ((w - 1) * 6 + i) * 1024), l16, rws, rfl(ows + ((w - 1) * 6 + i) * 1024));
#pragma unroll
      for (int i = 0; i < (12 * 6 - 4 * 6) / 3; ++i) W_DMA(4 * 6 + (w - 1) * ((12 * 6 - 4 * 6) / 3) + i); }
    asm volatile("s_waitcnt lgkmcnt(0)\n\ts_barrier" ::: "memory");
    { const u32x4 ro4 = mkrsrc(ws + OFF_O + (size_t)rt * 36864, 36864);
#pragma unroll
      for (int k = 0; k < 7; ++k) { const int p = min(w + 4 * k, 26), q = (nq + 1 + p / 9) & 3, i = p % 9, key = (4 * q + (((8 * i + (lane >> 3)) * 57) >> 10)) & 7;
        dma16s(rfl(lds + KVL + q * 9216 + i * 1024), (lane ^ key) * 16, ro4, rfl(q * 9216 + i * 1024)); } }
    const u32 xa0 = lds + KVL + tok * 1152 + ((g ^ ((tok >> 1) & 7)) * 16), xa1 = lds + KVL + tok * 1152 + (((4 + g) ^ ((tok >> 1) & 7)) * 16);
    const u32x4 wad = {xa0, xa1, xa0 + (w & 1) * 18432, xa1 + (w & 1) * 18432};
    f32x2 xf[2][4], xg[2];
#pragma unroll
    for (int m = 0; m < 2; ++m) { const float mu = *(const LDSP float*)(uintptr_t)(lds + L_MU + (16 * m + tok) * 4);
#pragma unroll
      for (int e = 0; e < 4; ++e) { const u32 xx = xr[4 * m + e]; xf[m][e] = f32x2{bflo(xx), bfhi(xx)} - mu; asm volatile("" : "+v"(xf[m][e])); }
      if (m == (w & 1)) { xg[0] = f32x2{bflo(rm2x[2]), bfhi(rm2x[2])} - mu; xg[1] = f32x2{bflo(rm2x[3]), bfhi(rm2x[3])} - mu; } }
    asm volatile("s_waitcnt vmcnt(0)\n\ts_barrier" : "+v"(xg[0]), "+v"(xg[1]) :: "memory");
    f32x16 acc; f32x4 acs;
    asm volatile(
    "ds_read_b128 v[64:67], v40 offset:0\n"
    "ds_read_b128 v[68:71], v40 offset:18432\n"
    "ds_read_b128 v[72:75], v42 offset:0\n"
    "ds_read_b128 v[76:79], v44 offset:0\n"
    "ds_read_b128 v[80:83], v41 offset:0\n"
    "ds_read_b128 v[84:87], v41 offset:18432\n"
    "ds_read_b128 v[88:91], v43 offset:0\n"
    "ds_read_b128 v[92:95], v44 offset:1024\n"
    "s_waitcnt lgkmcnt(4)\n"
    "v_mfma_f32_16x16x32_bf16 v[0:3], a[0:3], v[64:67], 0\n"
    "ds_read_b128 v[96:99], v40 offset:128\n"
    "v_mfma_f32_16x16x32_bf16 v[4:7], a[0:3], v[68:71], 0\n"
    "ds_read_b128 v[100:103], v40 offset:18560\n"
    "v_mfma_f32_16x16x32_bf16 v[8:11], a[4:7], v[64:67], 0\n"
    "ds_read_b128 v[104:107], v42 offset:128\n"
    "v_mfma_f32_16x16x32_bf16 v[12:15], a[4:7], v[68:71], 0\n"
    "ds_read_b128 v[108:111], v44 offset:2048\n"
    "v_mfma_f32_16x16x32_bf16 v[16:19], v[76:79], v[72:75], 0\n"
    "s_waitcnt lgkmcnt(4)\n"
    "v_mfma_f32_16x16x32_bf16 v[0:3], a[8:11], v[80:83], v[0:3]\n"
    "ds_read_b128 v[64:67], v41 offset:128\n"
    "v_mfma_f32_16x16x32_bf16 v[4:7], a[8:11], v[84:87], v[4:7]\n"
    "ds_read_b128 v[68:71], v41 offset:18560\n"
    "v_mfma_f32_16x16x32_bf16 v[8:11], a[12:15], v[80:83], v[8:11]\n"
    "ds_read_b128 v[72:75], v43 offset:128\n"
    "v_mfma_f32_16x16x32_bf16 v[12:15], a[12:15], v[84:87], v[12:15]\n"
    "ds_read_b128 v[76:79], v44 offset:3072\n"
    "v_mfma_f32_16x16x32_bf16 v[16:19], v[92:95], v[88:91], v[16:19]\n"
    "s_waitcnt lgkmcnt(4)\n"
    "v_mfma_f32_16x16x32_bf16 v[0:3], a[16:19], v[96:99], v[0:3]\n"
    "ds_read_b128 v[80:83], v40 offset:256\n"
    "v_mfma_f32_16x16x32_bf16 v[4:7], a[16:19], v[100:103], v[4:7]\n"
    "ds_read_b128 v[84:87], v40 offset:18688\n"
    "v_mfma_f32_16x16x32_bf16 v[8:11], a[20:23], v[96:99], v[8:11]\n"
    "ds_read_b128 v[88:91], v42 offset:256\n"
    "v_mfma_f32_16x16x32_bf16 v[12:15], a[20:23], v[100:103], v[12:15]\n"
    "ds_read_b128 v[92:95], v44 offset:4096\n"
    "v_mfma_f32_16x16x32_bf16 v[16:19], v[108:111], v[104:107], v[16:19]\n"
    "s_waitcnt lgkmcnt(4)\n"
    "v_mfma_f32_16x16x32_bf16 v[0:3], a[24:27], v[64:67], v[0:3]\n"
    "ds_read_b128 v[96:99], v41 offset:256\n"
    "v_mfma_f32_16x16x32_bf16 v[4:7], a[24:27], v[68:71], v[4:7]\n"
    "ds_read_b128 v[100:103], v41 offset:18688\n"
    "v_mfma_f32_16x16x32_bf16 v[8:11], a[28:31], v[64:67], v[8:11]\n"
    "ds_read_b128 v[104:107], v43 offset:256\n"
    "v_mfma_f32_16x16x32_bf16 v[12:15], a[28:31], v[68:71], v[12:15]\n"
    "ds_read_b128 v[108:111], v44 offset:5120\n"
    "v_mfma_f32_16x16x32_bf16 v[16:19], v[76:79], v[72:75], v[16:19]\n"
    "s_waitcnt lgkmcnt(4)\n"
    "v_mfma_f32_16x16x32_bf16 v[0:3], a[32:35], v[80:83], v[0:3]\n"
    "ds_read_b128 v[64:67], v40 offset:384\n"
    "v_mfma_f32_16x16x32_bf16 v[4:7], a[32:35], v[84:87], v[4:7]\n"
    "ds_read_b128 v[68:71], v40 offset:18816\n"
    "v_mfma_f32_16x16x32_bf16 v[8:11], a[36:39], v[80:83], v[8:11]\n"
    "ds_read_b128 v[72:75], v42 offset:384\n"
    "v_mfma_f32_16x16x32_bf16 v[12:15], a[36:39], v[84:87], v[12:15]\n"
    "ds_read_b128 v[76:79], v44 offset:6144\n"
    "v_mfma_f32_16x16x32_bf16 v[16:19], v[92:95], v[88:91], v[16:19]\n"
    "s_waitcnt lgkmcnt(4)\n"
    "v_mfma_f32_16x16x32_bf16 v[0:3], a[40:43], v[96:99], v[0:3]\n"
    "ds_read_b128 v[80:83], v41 offset:384\n"
    "v_mfma_f32_16x16x32_bf16 v[4:7], a[40:43], v[100:103], v[4:7]\n"
    "ds_read_b128 v[84:87], v41 offset:18816\n"
    "v_mfma_f32_16x16x32_bf16 v[8:11], a[44:47], v[96:99], v[8:11]\n"
    "ds_read_b128 v[88:91], v43 offset:384\n"
    "v_mfma_f32_16x16x32_bf16 v[12:15], a[44:47], v[100:103], v[12:15]\n"
    "ds_read_b128 v[92:95], v44 offset:7168\n"
    "v_mfma_f32_16x16x32_bf16 v[16:19], v[108:111], v[104:107], v[16:19]\n"
    "s_waitcnt lgkmcnt(4)\n"
    "v_mfma_f32_16x16x32_bf16 v[0:3], a[48:51], v[64:67], v[0:3]\n"
    "ds_read_b128 v[96:99], v40 offset:512\n"
    "v_mfma_f32_16x16x32_bf16 v[4:7], a[48:51], v[68:71], v[4:7]\n"
    "ds_read_b128 v[100:103], v40 offset:18944\n"
    "v_mfma_f32_16x16x32_bf16 v[8:11], a[52:55], v[64:67], v[8:11]\n"
    "ds_read_b128 v[104:107], v42 offset:512\n"
    "v_mfma_f32_16x16x32_bf16 v[12:15], a[52:55], v[68:71], v[12:15]\n"
    "ds_read_b128 v[108:111], v44 offset:8192\n"
    "v_mfma_f32_16x16x32_bf16 v[16:19], v[76:79], v[72:75], v[16:19]\n"
    "s_waitcnt lgkmcnt(4)\n"
    "v_mfma_f32_16x16x32_bf16 v[0:3], a[56:59], v[80:83], v[0:3]\n"
    "ds_read_b128 v[64:67], v41 offset:512\n"
    "v_mfma_f32_16x16x32_bf16 v[4:7], a[56:59], v[84:87], v[4:7]\n"
    "ds_read_b128 v[68:71], v41 offset:18944\n"
    "v_mfma_f32_16x16x32_bf16 v[8:11], a[60:63], v[80:83], v[8:11]\n"
    "ds_read_b128 v[72:75], v43 offset:512\n"
    "v_mfma_f32_16x16x32_bf16 v[12:15], a[60:63], v[84:87], v[12:15]\n"
    "ds_read_b128 v[76:79], v44 offset:9216\n"
    "v_mfma_f32_16x16x32_bf16 v[16:19], v[92:95], v[88:91], v[16:19]\n"
    "s_waitcnt lgkmcnt(4)\n"
    "v_mfma_f32_16x16x32_bf16 v[0:3], a[64:67], v[96:99], v[0:3]\n"
    "ds_read_b128 v[80:83], v40 offset:640\n"
    "v_mfma_f32_16x16x32_bf16 v[4:7], a[64:67], v[100:103], v[4:7]\n"
    "ds_read_b128 v[84:87], v40 offset:19072\n"
    "v_mfma_f32_16x16x32_bf16 v[8:11], a[68:71], v[96:99], v[8:11]\n"
    "ds_read_b128 v[88:91], v42 offset:640\n"
    "v_mfma_f32_16x16x32_bf16 v[12:15], a[68:71], v[100:103], v[12:15]\n"
    "ds_read_b128 v[92:95], v44 offset:10240\n"
    "v_mfma_f32_16x16x32_bf16 v[16:19], v[108:111], v[104:107], v[16:19]\n"
    "s_waitcnt lgkmcnt(4)\n"
    "v_mfma_f32_16x16x32_bf16 v[0:3], a[72:75], v[64:67], v[0:3]\n"
    "ds_read_b128 v[96:99], v41 offset:640\n"
    "v_mfma_f32_16x16x32_bf16 v[4:7], a[72:75], v[68:71], v[4:7]\n"
    "ds_read_b128 v[100:103], v41 offset:19072\n"
    "v_mfma_f32_16x16x32_bf16 v[8:11], a[76:79], v[64:67], v[8:11]\n"
    "ds_read_b128 v[104:107], v43 offset:640\n"
    "v_mfma_f32_16x16x32_bf16 v[12:15], a[76:79], v[68:71], v[12:15]\n"
    "ds_read_b128 v[108:111], v44 offset:11264\n"
    "v_mfma_f32_16x16x32_bf16 v[16:19], v[76:79], v[72:75], v[16:19]\n"
    "s_waitcnt lgkmcnt(4)\n"
    "v_mfma_f32_16x16x32_bf16 v[0:3], a[80:83], v[80:83], v[0:3]\n"
    "ds_read_b128 v[64:67], v40 offset:768\n"
    "v_mfma_f32_16x16x32_bf16 v[4:7], a[80:83], v[84:87], v[4:7]\n"
    "ds_read_b128 v[68:71], v40 offset:19200\n"
    "v_mfma_f32_16x16x32_bf16 v[8:11], a[84:87], v[80:83], v[8:11]\n"
    "ds_read_b128 v[72:75], v42 offset:768\n"
    "v_mfma_f32_16x16x32_bf16 v[12:15], a[84:87], v[84:87], v[12:15]\n"
    "ds_read_b128 v[76:79], v44 offset:12288\n"
    "v_mfma_f32_16x16x32_bf16 v[16:19], v[92:95], v[88:91], v[16:19]\n"
    "s_waitcnt lgkmcnt(4)\n"
    "v_mfma_f32_16x16x32_bf16 v[0:3], a[88:91], v[96:99], v[0:3]\n"
    "ds_read_b128 v[80:83], v41 offset:768\n"
    "v_mfma_f32_16x16x32_bf16 v[4:7], a[88:91], v[100:103], v[4:7]\n"
    "ds_read_b128 v[84:87], v41 offset:19200\n"
    "v_mfma_f32_16x16x32_bf16 v[8:11], a[92:95], v[96:99], v[8:11]\n"
    "ds_read_b128 v[88:91], v43 offset:768\n"
    "v_mfma_f32_16x16x32_bf16 v[12:15], a[92:95], v[100:103], v[12:15]\n"
    "ds_read_b128 v[92:95], v44 offset:13312\n"
    "v_mfma_f32_16x16x32_bf16 v[16:19], v[108:111], v[104:107], v[16:19]\n"
    "s_waitcnt lgkmcnt(4)\n"
    "v_mfma_f32_16x16x32_bf16 v[0:3], a[96:99], v[64:67], v[0:3]\n"
    "ds_read_b128 v[96:99], v40 offset:896\n"
    "v_mfma_f32_16x16x32_bf16 v[4:7], a[96:99], v[68:71], v[4:7]\n"
    "ds_read_b128 v[100:103], v40 offset:19328\n"
    "v_mfma_f32_16x16x32_bf16 v[8:11], a[100:103], v[64:67], v[8:11]\n"
    "ds_read_b128 v[104:107], v42 offset:896\n"
    "v_mfma_f32_16x16x32_bf16 v[12:15], a[100:103], v[68:71], v[12:15]\n"
    "ds_read_b128 v[108:111], v44 offset:14336\n"
    "v_mfma_f32_16x16x32_bf16 v[16:19], v[76:79], v[72:75], v[16:19]\n"
    "s_waitcnt lgkmcnt(4)\n"
    "v_mfma_f32_16x16x32_bf16 v[0:3], a[104:107], v[80:83], v[0:3]\n"
    "ds_read_b128 v[64:67], v41 offset:896\n"
    "v_mfma_f32_16x16x32_bf16 v[4:7], a[104:107], v[84:87], v[4:7]\n"
    "ds_read_b128 v[68:71], v41 offset:19328\n"
    "v_mfma_f32_16x16x32_bf16 v[8:11], a[108:111], v[80:83], v[8:11]\n"
    "ds_read_b128 v[72:75], v43 offset:896\n"
    "v_mfma_f32_16x16x32_bf16 v[12:15], a[108:111], v[84:87], v[12:15]\n"
    "ds_read_b128 v[76:79], v44 offset:15360\n"
    "v_mfma_f32_16x16x32_bf16 v[16:19], v[92:95], v[88:91], v[16:19]\n"
    "s_waitcnt lgkmcnt(4)\n"
    "v_mfma_f32_16x16x32_bf16 v[0:3], a[112:115], v[96:99], v[0:3]\n"
    "ds_read_b128 v[80:83], v40 offset:1024\n"
    "v_mfma_f32_16x16x32_bf16 v[4:7], a[112:115], v[100:103], v[4:7]\n"
    "ds_read_b128 v[84:87], v40 offset:19456\n"
    "v_mfma_f32_16x16x32_bf16 v[8:11], a[116:119], v[96:99], v[8:11]\n"
    "ds_read_b128 v[88:91], v42 offset:1024\n"
    "v_mfma_f32_16x16x32_bf16 v[12:15], a[116:119], v[100:103], v[12:15]\n"
    "ds_read_b128 v[92:95], v44 offset:16384\n"
    "v_mfma_f32_16x16x32_bf16 v[16:19], v[108:111], v[104:107], v[16:19]\n"
    "s_waitcnt lgkmcnt(4)\n"
    "v_mfma_f32_16x16x32_bf16 v[0:3], a[120:123], v[64:67], v[0:3]\n"
    "ds_read_b128 v[96:99], v41 offset:1024\n"
    "v_mfma_f32_16x16x32_bf16 v[4:7], a[120:123], v[68:71], v[4:7]\n"
    "ds_read_b128 v[100:103], v41 offset:19456\n"
    "v_mfma_f32_16x16x32_bf16 v[8:11], a[124:127], v[64:67], v[8:11]\n"
    "ds_read_b128 v[104:107], v43 offset:1024\n"
    "v_mfma_f32_16x16x32_bf16 v[12:15], a[124:127], v[68:71], v[12:15]\n"
    "ds_read_b128 v[108:111], v44 offset:17408\n"
    "v_mfma_f32_16x16x32_bf16 v[16:19], v[76:79], v[72:75], v[16:19]\n"
    "s_waitcnt lgkmcnt(4)\n"
    "v_mfma_f32_16x16x32_bf16 v[0:3], a[128:131], v[80:83], v[0:3]\n"
    "v_mfma_f32_16x16x32_bf16 v[4:7], a[128:131], v[84:87], v[4:7]\n"
    "v_mfma_f32_16x16x32_bf16 v[8:11], a[132:135], v[80:83], v[8:11]\n"
    "v_mfma_f32_16x16x32_bf16 v[12:15], a[132:135], v[84:87], v[12:15]\n"
    "v_mfma_f32_16x16x32_bf16 v[16:19], v[92:95], v[88:91], v[16:19]\n"
    "s_waitcnt lgkmcnt(0)\n"
    "v_mfma_f32_16x16x32_bf16 v[0:3], a[136:139], v[96:99], v[0:3]\n"
    "v_mfma_f32_16x16x32_bf16 v[4:7], a[136:139], v[100:103], v[4:7]\n"
    "v_mfma_f32_16x16x32_bf16 v[8:11], a[140:143], v[96:99], v[8:11]\n"
    "v_mfma_f32_16x16x32_bf16 v[12:15], a[140:143], v[100:103], v[12:15]\n"
    "v_mfma_f32_16x16x32_bf16 v[16:19], v[108:111], v[104:107], v[16:19]\n"
    "s_nop 7\n"
    : "=&{v[0:15]}"(acc), "=&{v[16:19]}"(acs)
    : "{v[40:43]}"(wad), "{v44}"(lds + L_WS + l16), "{a[0:31]}"(wa0), "{a[32:63]}"(wa1), "{a[64:95]}"(wa2), "{a[96:127]}"(wa3), "{a[128:143]}"(wa4)
    : "v64","v65","v66","v67","v68","v69","v70","v71","v72","v73","v74","v75","v76","v77","v78","v79","v80","v81","v82","v83","v84","v85","v86","v87","v88","v89","v90","v91","v92","v93","v94","v95","v96","v97","v98","v99","v100","v101","v102","v103","v104","v105","v106","v107","v108","v109","v110","v111","memory");
    const rsrc_t rC = mkr(ws + OFF_HC + (size_t)rt * 36864, 36864), rS = mkr(ws + OFF_SS + (size_t)rt * 4096, 4096);
    float ss[3];
#pragma unroll
    for (int m = 0; m < 2; ++m) { f32x2 d[4]; u32 cc[4];
#pragma unroll
      for (int e = 0; e < 4; ++e) { d[e] = xf[m][e] + f32x2{acc[8 * (e >> 1) + 4 * m + 2 * (e & 1)], acc[8 * (e >> 1) + 4 * m + 2 * (e & 1) + 1]}; cc[e] = cvtpk(d[e][0], d[e][1]); }
      ST16((u32x4{cc[0], cc[1], cc[2], cc[3]}), rC, l16, (m * 18 + ub + w) * 1024);
      const f32x2 q2 = (d[0] * d[0] + d[1] * d[1]) + (d[2] * d[2] + d[3] * d[3]); ss[m] = q2[0] + q2[1]; }
    { const f32x2 d0 = xg[0] + f32x2{acs[0], acs[1]}, d1 = xg[1] + f32x2{acs[2], acs[3]};
      if (w < 2) ST8((u32x2{cvtpk(d0[0], d0[1]), cvtpk(d1[0], d1[1])}), rC, l16 + hs_ * 8, (w * 18 + us) * 1024);
      const f32x2 q2 = d0 * d0 + d1 * d1; ss[2] = q2[0] + q2[1]; }
#pragma unroll
    for (int m = 2; m >= 0; --m) { const float s1 = swapadd32(swapadd16(ss[m]));
      if (g == 0 && (m < 2 || w < 2)) __builtin_amdgcn_raw_buffer_store_b32(__float_as_uint(s1), rS, (16 * (m < 2 ? m : w) + tok) * 128 + nq * 32 + (m < 2 ? w * 4 : 16), 0, 16); }
  }
  const int grp = panel * 4 + nq, t16 = panel * 8 + 2 * w;
  const u32 ab = (u32)OFF_A + (t16 * 144 + kq * 36) * 1024, ohf = (u32)OFF_HF + (rt * 4 + nq) * 64, ohp = (u32)OFF_HF + (panel * 4 + w) * 256;
  const u32x8 vin = {lds + l16, lds + l16 + 65536, lds + l16 + 131072, l16 + w * 3072, l16, l16 + 18432, l16 + 147456, 0u};
  const u32x8 sin = {SU((u32)OFF_W1T + (kq * 4 + nq) * 331776), SU((u32)OFF_W2T + (kq * 4 + nq) * 331776), SU((u32)OFF_HC + t16 * 18432), SU(ab + nq * 9216), SU(ab + ((nq + 1) & 3) * 9216), SU(ab + ((nq + 2) & 3) * 9216), SU(ab + ((nq + 3) & 3) * 9216), SU((u32)OFF_SLAB + (grp * 16 + kq * 4 + w) * 7168)};
  const u32x8 sin2 = {SU((u32)OFF_CNT + (panel * 4 + kq) * 512), SU((u32)OFF_CNT + (256 + grp * 4 + kq) * 128), SU(lds + w * 3072), SU((u32)w), SU((u32)OFF_HC + (t16 * 18 + ub + kq) * 1024), SU((u32)OFF_HC + (t16 * 18 + us) * 1024 + hs_ * 8), SU((u32)OFF_SS + t16 * 2048), SU((u32)OFF_MU + t16 * 256)};
  const u32x4 vin2 = {1u, (u32)(tok * 128 + g * 32), (u32)((tok >> 3) * 128 + (tok & 7) * 8), (u32)((lane & 3) * 64)};
  f32x8 csh, sq; f32x16 cown; u32x8 hp; u32x4 hs; f32x4 sm;
  asm volatile(
    "s_waitcnt vmcnt(2)\n"
    "s_barrier\n"
    "s_cmp_lg_u32 s35, 0\n"
    "s_cbranch_scc1 Lsig1_%=\n"
    "s_mov_b64 s[42:43], exec\n"
    "s_mov_b64 exec, 1\n"
    "buffer_store_dword v240, v239, %[rs], %[hf] offen offset:0 sc1\n"
    "s_mov_b64 exec, s[42:43]\n"
    "Lsig1_%=:\n"
    "s_add_u32 m0, s34, 73728\n"
    "s_add_u32 s40, s24, 73728\n"
    "buffer_load_dwordx4 v235, %[rs], s40 offen offset:0 sc1 lds\n"
    "buffer_load_dwordx4 v235, %[rs], s40 offen offset:1024 sc1 lds\n"
    "buffer_load_dwordx4 v235, %[rs], s40 offen offset:2048 sc1 lds\n"
    "s_add_u32 m0, s34, 86016\n"
    "s_add_u32 s40, s24, 86016\n"
    "buffer_load_dwordx4 v235, %[rs], s40 offen offset:0 sc1 lds\n"
    "buffer_load_dwordx4 v235, %[rs], s40 offen offset:1024 sc1 lds\n"
    "buffer_load_dwordx4 v235, %[rs], s40 offen offset:2048 sc1 lds\n"
    "s_add_u32 m0, s34, 98304\n"
    "s_add_u32 s40, s24, 98304\n"
    "buffer_load_dwordx4 v235, %[rs], s40 offen offset:0 sc1 lds\n"
    "buffer_load_dwordx4 v235, %[rs], s40 offen offset:1024 sc1 lds\n"
    "buffer_load_dwordx4 v235, %[rs], s40 offen offset:2048 sc1 lds\n"
    "buffer_load_dword v224, v243, %[rs], %[hp] offen sc1\n"
    "s_add_u32 m0, s34, 110592\n"
    "s_add_u32 s40, s24, 110592\n"
    "buffer_load_dwordx4 v235, %[rs], s40 offen offset:0 sc1 lds\n"
    "buffer_load_dwordx4 v235, %[rs], s40 offen offset:1024 sc1 lds\n"
    "buffer_load_dwordx4 v235, %[rs], s40 offen offset:2048 sc1 lds\n"
    "s_add_u32 m0, s34, 122880\n"
    "s_add_u32 s40, s24, 122880\n"
    "buffer_load_dwordx4 v235, %[rs], s40 offen offset:0 sc1 lds\n"
    "buffer_load_dwordx4 v235, %[rs], s40 offen offset:1024 sc1 lds\n"
    "buffer_load_dwordx4 v235, %[rs], s40 offen offset:2048 sc1 lds\n"
    "s_add_u32 m0, s34, 135168\n"
    "s_add_u32 s40, s24, 135168\n"
    "buffer_load_dwordx4 v235, %[rs], s40 offen offset:0 sc1 lds\n"
    "buffer_load_dwordx4 v235, %[rs], s40 offen offset:1024 sc1 lds\n"
    "buffer_load_dwordx4 v235, %[rs], s40 offen offset:2048 sc1 lds\n"
    "s_add_u32 m0, s34, 147456\n"
    "s_add_u32 s40, s24, 147456\n"
    "buffer_load_dwordx4 v235, %[rs], s40 offen offset:0 sc1 lds\n"
    "buffer_load_dwordx4 v235, %[rs], s40 offen offset:1024 sc1 lds\n"
    "buffer_load_dwordx4 v235, %[rs], s40 offen offset:2048 sc1 lds\n"
    "s_waitcnt vmcnt(12)\n"
    "v_cmp_ne_u32 vcc, 1, v224\n"
    "s_mov_b32 s44, 0x10000\n"
    "s_nop 3\n"
    "s_cbranch_vccz Lp4_e_%=\n"
    "Lp4_p_%=:\n"
    "buffer_load_dword v224, v243, %[rs], %[hp] offen sc1\n"
    "s_waitcnt vmcnt(0)\n"
    "v_cmp_ne_u32 vcc, 1, v224\n"
    "s_sub_u32 s44, s44, 1\n"
    "s_cbranch_scc1 Lp4_e_%=\n"
    "s_sleep 1\n"
    "s_cbranch_vccnz Lp4_p_%=\n"
    "Lp4_e_%=:\n"
    "s_add_u32 s41, s26, 0\n"
    "buffer_load_dwordx4 a[0:3], v236, %[rs], s41 offen offset:0 sc1\n"
    "buffer_load_dwordx4 a[4:7], v237, %[rs], s41 offen offset:0 sc1\n"
    "buffer_load_dwordx4 a[8:11], v236, %[rs], s41 offen offset:1024 sc1\n"
    "buffer_load_dwordx4 a[12:15], v237, %[rs], s41 offen offset:1024 sc1\n"
    "buffer_load_dwordx4 a[16:19], v236, %[rs], s41 offen offset:2048 sc1\n"
    "buffer_load_dwordx4 a[20:23], v237, %[rs], s41 offen offset:2048 sc1\n"
    "buffer_load_dwordx4 a[24:27], v236, %[rs], s41 offen offset:3072 sc1\n"
    "buffer_load_dwordx4 a[28:31], v237, %[rs], s41 offen offset:3072 sc1\n"
    "s_add_u32 s41, s26, 4096\n"
    "buffer_load_dwordx4 a[32:35], v236, %[rs], s41 offen offset:0 sc1\n"
    "buffer_load_dwordx4 a[36:39], v237, %[rs], s41 offen offset:0 sc1\n"
    "ds_read_b128 v[120:123], v232 offset:0\n"
    "ds_read_b128 v[124:127], v232 offset:1024\n"
    "ds_read_b128 v[128:131], v232 offset:2048\n"
    "ds_read_b128 v[132:135], v232 offset:3072\n"
    "ds_read_b128 v[136:139], v232 offset:4096\n"
    "ds_read_b128 v[140:143], v232 offset:5120\n"
    "ds_read_b128 v[144:147], v232 offset:6144\n"
    "s_waitcnt vmcnt(8)\n"
    "s_waitcnt lgkmcnt(3)\n"
    "v_mfma_f32_16x16x32_bf16 v[0:3], v[120:123], a[0:3], 0\n"
    "ds_read_b128 v[148:151], v232 offset:7168\n"
    "v_mfma_f32_16x16x32_bf16 v[4:7], v[120:123], a[4:7], 0\n"
    "buffer_load_dwordx4 a[40:43], v236, %[rs], s41 offen offset:1024 sc1\n"
    "v_mfma_f32_16x16x32_bf16 v[12:15], v[124:127], a[4:7], 0\n"
    "ds_read_b128 v[120:123], v232 offset:8192\n"
    "v_mfma_f32_16x16x32_bf16 v[8:11], v[124:127], a[0:3], 0\n"
    "v_mfma_f32_16x16x32_bf16 v[16:19], v[128:131], a[0:3], 0\n"
    "ds_read_b128 v[124:127], v232 offset:9216\n"
    "v_mfma_f32_16x16x32_bf16 v[20:23], v[128:131], a[4:7], 0\n"
    "buffer_load_dwordx4 a[44:47], v237, %[rs], s41 offen offset:1024 sc1\n"
    "v_mfma_f32_16x16x32_bf16 v[28:31], v[132:135], a[4:7], 0\n"
    "ds_read_b128 v[128:131], v232 offset:10240\n"
    "v_mfma_f32_16x16x32_bf16 v[24:27], v[132:135], a[0:3], 0\n"
    "s_waitcnt lgkmcnt(3)\n"
    "v_mfma_f32_16x16x32_bf16 v[32:35], v[136:139], a[0:3], 0\n"
    "ds_read_b128 v[132:135], v232 offset:11264\n"
    "v_mfma_f32_16x16x32_bf16 v[36:39], v[136:139], a[4:7], 0\n"
    "buffer_load_dwordx4 a[48:51], v236, %[rs], s41 offen offset:2048 sc1\n"
    "v_mfma_f32_16x16x32_bf16 v[44:47], v[140:143], a[4:7], 0\n"
    "ds_read_b128 v[136:139], v232 offset:12288\n"
    "v_mfma_f32_16x16x32_bf16 v[40:43], v[140:143], a[0:3], 0\n"
    "s_waitcnt vmcnt(9)\n"
    "v_mfma_f32_16x16x32_bf16 v[0:3], v[144:147], a[8:11], v[0:3]\n"
    "ds_read_b128 v[140:143], v232 offset:13312\n"
    "v_mfma_f32_16x16x32_bf16 v[4:7], v[144:147], a[12:15], v[4:7]\n"
    "buffer_load_dwordx4 a[52:55], v237, %[rs], s41 offen offset:2048 sc1\n"
    "v_mfma_f32_16x16x32_bf16 v[12:15], v[148:151], a[12:15], v[12:15]\n"
    "ds_read_b128 v[144:147], v232 offset:14336\n"
    "v_mfma_f32_16x16x32_bf16 v[8:11], v[148:151], a[8:11], v[8:11]\n"
    "s_waitcnt lgkmcnt(3)\n"
    "v_mfma_f32_16x16x32_bf16 v[16:19], v[120:123], a[8:11], v[16:19]\n"
    "ds_read_b128 v[148:151], v232 offset:15360\n"
    "v_mfma_f32_16x16x32_bf16 v[20:23], v[120:123], a[12:15], v[20:23]\n"
    "buffer_load_dwordx4 a[56:59], v236, %[rs], s41 offen offset:3072 sc1\n"
    "v_mfma_f32_16x16x32_bf16 v[28:31], v[124:127], a[12:15], v[28:31]\n"
    "ds_read_b128 v[120:123], v232 offset:16384\n"
    "v_mfma_f32_16x16x32_bf16 v[24:27], v[124:127], a[8:11], v[24:27]\n"
    "v_mfma_f32_16x16x32_bf16 v[32:35], v[128:131], a[8:11], v[32:35]\n"
    "ds_read_b128 v[124:127], v232 offset:17408\n"
    "v_mfma_f32_16x16x32_bf16 v[36:39], v[128:131], a[12:15], v[36:39]\n"
    "buffer_load_dwordx4 a[60:63], v237, %[rs], s41 offen offset:3072 sc1\n"
    "v_mfma_f32_16x16x32_bf16 v[44:47], v[132:135], a[12:15], v[44:47]\n"
    "ds_read_b128 v[128:131], v232 offset:18432\n"
    "v_mfma_f32_16x16x32_bf16 v[40:43], v[132:135], a[8:11], v[40:43]\n"
    "s_waitcnt vmcnt(10)\n"
    "s_waitcnt lgkmcnt(3)\n"
    "v_mfma_f32_16x16x32_bf16 v[0:3], v[136:139], a[16:19], v[0:3]\n"
    "ds_read_b128 v[132:135], v232 offset:19456\n"
    "v_mfma_f32_16x16x32_bf16 v[4:7], v[136:139], a[20:23], v[4:7]\n"
    "v_mfma_f32_16x16x32_bf16 v[12:15], v[140:143], a[20:23], v[12:15]\n"
    "ds_read_b128 v[136:139], v232 offset:20480\n"
    "v_mfma_f32_16x16x32_bf16 v[8:11], v[140:143], a[16:19], v[8:11]\n"
    "s_add_u32 s41, s26, 8192\n"
    "buffer_load_dwordx4 a[64:67], v236, %[rs], s41 offen offset:0 sc1\n"
    "v_mfma_f32_16x16x32_bf16 v[16:19], v[144:147], a[16:19], v[16:19]\n"
    "ds_read_b128 v[140:143], v232 offset:21504\n"
    "v_mfma_f32_16x16x32_bf16 v[20:23], v[144:147], a[20:23], v[20:23]\n"
    "v_mfma_f32_16x16x32_bf16 v[28:31], v[148:151], a[20:23], v[28:31]\n"
    "ds_read_b128 v[144:147], v232 offset:22528\n"
    "v_mfma_f32_16x16x32_bf16 v[24:27], v[148:151], a[16:19], v[24:27]\n"
    "buffer_load_dwordx4 a[68:71], v237, %[rs], s41 offen offset:0 sc1\n"
    "s_waitcnt lgkmcnt(3)\n"
    "v_mfma_f32_16x16x32_bf16 v[32:35], v[120:123], a[16:19], v[32:35]\n"
    "ds_read_b128 v[148:151], v232 offset:23552\n"
    "v_mfma_f32_16x16x32_bf16 v[36:39], v[120:123], a[20:23], v[36:39]\n"
    "v_mfma_f32_16x16x32_bf16 v[44:47], v[124:127], a[20:23], v[44:47]\n"
    "ds_read_b128 v[120:123], v232 offset:24576\n"
    "v_mfma_f32_16x16x32_bf16 v[40:43], v[124:127], a[16:19], v[40:43]\n"
    "buffer_load_dwordx4 a[72:75], v236, %[rs], s41 offen offset:1024 sc1\n"
    "s_waitcnt vmcnt(11)\n"
    "v_mfma_f32_16x16x32_bf16 v[0:3], v[128:131], a[24:27], v[0:3]\n"
    "ds_read_b128 v[124:127], v232 offset:25600\n"
    "v_mfma_f32_16x16x32_bf16 v[4:7], v[128:131], a[28:31], v[4:7]\n"
    "v_mfma_f32_16x16x32_bf16 v[12:15], v[132:135], a[28:31], v[12:15]\n"
    "ds_read_b128 v[128:131], v232 offset:26624\n"
    "v_mfma_f32_16x16x32_bf16 v[8:11], v[132:135], a[24:27], v[8:11]\n"
    "buffer_load_dwordx4 a[76:79], v237, %[rs], s41 offen offset:1024 sc1\n"
    "s_waitcnt lgkmcnt(3)\n"
    "v_mfma_f32_16x16x32_bf16 v[16:19], v[136:139], a[24:27], v[16:19]\n"
    "ds_read_b128 v[132:135], v232 offset:27648\n"
    "v_mfma_f32_16x16x32_bf16 v[20:23], v[136:139], a[28:31], v[20:23]\n"
    "v_mfma_f32_16x16x32_bf16 v[28:31], v[140:143], a[28:31], v[28:31]\n"
    "ds_read_b128 v[136:139], v232 offset:28672\n"
    "v_mfma_f32_16x16x32_bf16 v[24:27], v[140:143], a[24:27], v[24:27]\n"
    "buffer_load_dwordx4 a[80:83], v236, %[rs], s41 offen offset:2048 sc1\n"
    "v_mfma_f32_16x16x32_bf16 v[32:35], v[144:147], a[24:27], v[32:35]\n"
    "ds_read_b128 v[140:143], v232 offset:29696\n"
    "v_mfma_f32_16x16x32_bf16 v[36:39], v[144:147], a[28:31], v[36:39]\n"
    "v_mfma_f32_16x16x32_bf16 v[44:47], v[148:151], a[28:31], v[44:47]\n"
    "ds_read_b128 v[144:147], v232 offset:30720\n"
    "v_mfma_f32_16x16x32_bf16 v[40:43], v[148:151], a[24:27], v[40:43]\n"
    "buffer_load_dwordx4 a[84:87], v237, %[rs], s41 offen offset:2048 sc1\n"
    "s_waitcnt vmcnt(12)\n"
    "s_waitcnt lgkmcnt(3)\n"
    "v_mfma_f32_16x16x32_bf16 v[0:3], v[120:123], a[32:35], v[0:3]\n"
    "ds_read_b128 v[148:151], v232 offset:31744\n"
    "v_mfma_f32_16x16x32_bf16 v[4:7], v[120:123], a[36:39], v[4:7]\n"
    "v_mfma_f32_16x16x32_bf16 v[12:15], v[124:127], a[36:39], v[12:15]\n"
    "ds_read_b128 v[120:123], v232 offset:32768\n"
    "v_mfma_f32_16x16x32_bf16 v[8:11], v[124:127], a[32:35], v[8:11]\n"
    "buffer_load_dwordx4 a[88:91], v236, %[rs], s41 offen offset:3072 sc1\n"
    "v_mfma_f32_16x16x32_bf16 v[16:19], v[128:131], a[32:35], v[16:19]\n"
    "ds_read_b128 v[124:127], v232 offset:33792\n"
    "v_mfma_f32_16x16x32_bf16 v[20:23], v[128:131], a[36:39], v[20:23]\n"
    "v_mfma_f32_16x16x32_bf16 v[28:31], v[132:135], a[36:39], v[28:31]\n"
    "ds_read_b128 v[128:131], v232 offset:34816\n"
    "v_mfma_f32_16x16x32_bf16 v[24:27], v[132:135], a[32:35], v[24:27]\n"
    "s_waitcnt lgkmcnt(3)\n"
    "v_mfma_f32_16x16x32_bf16 v[32:35], v[136:139], a[32:35], v[32:35]\n"
    "ds_read_b128 v[132:135], v232 offset:35840\n"
    "v_mfma_f32_16x16x32_bf16 v[36:39], v[136:139], a[36:39], v[36:39]\n"
    "buffer_load_dwordx4 a[92:95], v237, %[rs], s41 offen offset:3072 sc1\n"
    "v_mfma_f32_16x16x32_bf16 v[44:47], v[140:143], a[36:39], v[44:47]\n"
    "ds_read_b128 v[136:139], v232 offset:36864\n"
    "v_mfma_f32_16x16x32_bf16 v[40:43], v[140:143], a[32:35], v[40:43]\n"
    "s_waitcnt vmcnt(12)\n"
    "v_mfma_f32_16x16x32_bf16 v[0:3], v[144:147], a[40:43], v[0:3]\n"
    "ds_read_b128 v[140:143], v232 offset:37888\n"
    "v_mfma_f32_16x16x32_bf16 v[4:7], v[144:147], a[44:47], v[4:7]\n"
    "s_add_u32 s41, s26, 12288\n"
    "buffer_load_dwordx4 a[96:99], v236, %[rs], s41 offen offset:0 sc1\n"
    "v_mfma_f32_16x16x32_bf16 v[12:15], v[148:151], a[44:47], v[12:15]\n"
    "ds_read_b128 v[144:147], v232 offset:38912\n"
    "v_mfma_f32_16x16x32_bf16 v[8:11], v[148:151], a[40:43], v[8:11]\n"
    "s_waitcnt lgkmcnt(3)\n"
    "v_mfma_f32_16x16x32_bf16 v[16:19], v[120:123], a[40:43], v[16:19]\n"
    "ds_read_b128 v[148:151], v232 offset:39936\n"
    "v_mfma_f32_16x16x32_bf16 v[20:23], v[120:123], a[44:47], v[20:23]\n"
    "buffer_load_dwordx4 a[100:103], v237, %[rs], s41 offen offset:0 sc1\n"
    "v_mfma_f32_16x16x32_bf16 v[28:31], v[124:127], a[44:47], v[28:31]\n"
    "ds_read_b128 v[120:123], v232 offset:40960\n"
    "v_mfma_f32_16x16x32_bf16 v[24:27], v[124:127], a[40:43], v[24:27]\n"
    "v_mfma_f32_16x16x32_bf16 v[32:35], v[128:131], a[40:43], v[32:35]\n"
    "ds_read_b128 v[124:127], v232 offset:41984\n"
    "v_mfma_f32_16x16x32_bf16 v[36:39], v[128:131], a[44:47], v[36:39]\n"
    "buffer_load_dwordx4 a[104:107], v236, %[rs], s41 offen offset:1024 sc1\n"
    "v_mfma_f32_16x16x32_bf16 v[44:47], v[132:135], a[44:47], v[44:47]\n"
    "ds_read_b128 v[128:131], v232 offset:43008\n"
    "v_mfma_f32_16x16x32_bf16 v[40:43], v[132:135], a[40:43], v[40:43]\n"
    "s_waitcnt vmcnt(13)\n"
    "s_waitcnt lgkmcnt(3)\n"
    "v_mfma_f32_16x16x32_bf16 v[0:3], v[136:139], a[48:51], v[0:3]\n"
    "ds_read_b128 v[132:135], v232 offset:44032\n"
    "v_mfma_f32_16x16x32_bf16 v[4:7], v[136:139], a[52:55], v[4:7]\n"
    "buffer_load_dwordx4 a[108:111], v237, %[rs], s41 offen offset:1024 sc1\n"
    "v_mfma_f32_16x16x32_bf16 v[12:15], v[140:143], a[52:55], v[12:15]\n"
    "ds_read_b128 v[136:139], v232 offset:45056\n"
    "v_mfma_f32_16x16x32_bf16 v[8:11], v[140:143], a[48:51], v[8:11]\n"
    "v_mfma_f32_16x16x32_bf16 v[16:19], v[144:147], a[48:51], v[16:19]\n"
    "ds_read_b128 v[140:143], v232 offset:46080\n"
    "v_mfma_f32_16x16x32_bf16 v[20:23], v[144:147], a[52:55], v[20:23]\n"
    "buffer_load_dwordx4 a[112:115], v236, %[rs], s41 offen offset:2048 sc1\n"
    "v_mfma_f32_16x16x32_bf16 v[28:31], v[148:151], a[52:55], v[28:31]\n"
    "ds_read_b128 v[144:147], v232 offset:47104\n"
    "v_mfma_f32_16x16x32_bf16 v[24:27], v[148:151], a[48:51], v[24:27]\n"
    "s_waitcnt lgkmcnt(3)\n"
    "v_mfma_f32_16x16x32_bf16 v[32:35], v[120:123], a[48:51], v[32:35]\n"
    "ds_read_b128 v[148:151], v232 offset:48128\n"
    "v_mfma_f32_16x16x32_bf16 v[36:39], v[120:123], a[52:55], v[36:39]\n"
    "buffer_load_dwordx4 a[116:119], v237, %[rs], s41 offen offset:2048 sc1\n"
    "v_mfma_f32_16x16x32_bf16 v[44:47], v[124:127], a[52:55], v[44:47]\n"
    "ds_read_b128 v[120:123], v232 offset:49152\n"
    "v_mfma_f32_16x16x32_bf16 v[40:43], v[124:127], a[48:51], v[40:43]\n"
    "s_waitcnt vmcnt(14)\n"
    "v_mfma_f32_16x16x32_bf16 v[0:3], v[128:131], a[56:59], v[0:3]\n"
    "ds_read_b128 v[124:127], v232 offset:50176\n"
    "v_mfma_f32_16x16x32_bf16 v[4:7], v[128:131], a[60:63], v[4:7]\n"
    "v_mfma_f32_16x16x32_bf16 v[12:15], v[132:135], a[60:63], v[12:15]\n"
    "ds_read_b128 v[128:131], v232 offset:51200\n"
    "v_mfma_f32_16x16x32_bf16 v[8:11], v[132:135], a[56:59], v[8:11]\n"
    "buffer_load_dwordx4 a[120:123], v236, %[rs], s41 offen offset:3072 sc1\n"
    "s_waitcnt lgkmcnt(3)\n"
    "v_mfma_f32_16x16x32_bf16 v[16:19], v[136:139], a[56:59], v[16:19]\n"
    "ds_read_b128 v[132:135], v232 offset:52224\n"
    "v_mfma_f32_16x16x32_bf16 v[20:23], v[136:139], a[60:63], v[20:23]\n"
    "v_mfma_f32_16x16x32_bf16 v[28:31], v[140:143], a[60:63], v[28:31]\n"
    "ds_read_b128 v[136:139], v232 offset:53248\n"
    "v_mfma_f32_16x16x32_bf16 v[24:27], v[140:143], a[56:59], v[24:27]\n"
    "buffer_load_dwordx4 a[124:127], v237, %[rs], s41 offen offset:3072 sc1\n"
    "v_mfma_f32_16x16x32_bf16 v[32:35], v[144:147], a[56:59], v[32:35]\n"
    "ds_read_b128 v[140:143], v232 offset:54272\n"
    "v_mfma_f32_16x16x32_bf16 v[36:39], v[144:147], a[60:63], v[36:39]\n"
    "v_mfma_f32_16x16x32_bf16 v[44:47], v[148:151], a[60:63], v[44:47]\n"
    "ds_read_b128 v[144:147], v232 offset:55296\n"
    "v_mfma_f32_16x16x32_bf16 v[40:43], v[148:151], a[56:59], v[40:43]\n"
    "s_add_u32 s41, s26, 16384\n"
    "buffer_load_dwordx4 a[128:131], v236, %[rs], s41 offen offset:0 sc1\n"
    "s_waitcnt vmcnt(15)\n"
    "s_waitcnt lgkmcnt(3)\n"
    "v_mfma_f32_16x16x32_bf16 v[0:3], v[120:123], a[64:67], v[0:3]\n"
    "ds_read_b128 v[148:151], v232 offset:56320\n"
    "v_mfma_f32_16x16x32_bf16 v[4:7], v[120:123], a[68:71], v[4:7]\n"
    "v_mfma_f32_16x16x32_bf16 v[12:15], v[124:127], a[68:71], v[12:15]\n"
    "ds_read_b128 v[120:123], v232 offset:57344\n"
    "v_mfma_f32_16x16x32_bf16 v[8:11], v[124:127], a[64:67], v[8:11]\n"
    "buffer_load_dwordx4 a[132:135], v237, %[rs], s41 offen offset:0 sc1\n"
    "v_mfma_f32_16x16x32_bf16 v[16:19], v[128:131], a[64:67], v[16:19]\n"
    "ds_read_b128 v[124:127], v232 offset:58368\n"
    "v_mfma_f32_16x16x32_bf16 v[20:23], v[128:131], a[68:71], v[20:23]\n"
    "v_mfma_f32_16x16x32_bf16 v[28:31], v[132:135], a[68:71], v[28:31]\n"
    "ds_read_b128 v[128:131], v232 offset:59392\n"
    "v_mfma_f32_16x16x32_bf16 v[24:27], v[132:135], a[64:67], v[24:27]\n"
    "buffer_load_dwordx4 a[136:139], v236, %[rs], s41 offen offset:1024 sc1\n"
    "s_waitcnt lgkmcnt(3)\n"
    "v_mfma_f32_16x16x32_bf16 v[32:35], v[136:139], a[64:67], v[32:35]\n"
    "ds_read_b128 v[132:135], v232 offset:60416\n"
    "v_mfma_f32_16x16x32_bf16 v[36:39], v[136:139], a[68:71], v[36:39]\n"
    "v_mfma_f32_16x16x32_bf16 v[44:47], v[140:143], a[68:71], v[44:47]\n"
    "ds_read_b128 v[136:139], v232 offset:61440\n"
    "v_mfma_f32_16x16x32_bf16 v[40:43], v[140:143], a[64:67], v[40:43]\n"
    "buffer_load_dwordx4 a[140:143], v237, %[rs], s41 offen offset:1024 sc1\n"
    "s_waitcnt vmcnt(16)\n"
    "v_mfma_f32_16x16x32_bf16 v[0:3], v[144:147], a[72:75], v[0:3]\n"
    "ds_read_b128 v[140:143], v232 offset:62464\n"
    "v_mfma_f32_16x16x32_bf16 v[4:7], v[144:147], a[76:79], v[4:7]\n"
    "v_mfma_f32_16x16x32_bf16 v[12:15], v[148:151], a[76:79], v[12:15]\n"
    "ds_read_b128 v[144:147], v232 offset:63488\n"
    "v_mfma_f32_16x16x32_bf16 v[8:11], v[148:151], a[72:75], v[8:11]\n"
    "s_waitcnt lgkmcnt(3)\n"
    "v_mfma_f32_16x16x32_bf16 v[16:19], v[120:123], a[72:75], v[16:19]\n"
    "ds_read_b128 v[148:151], v232 offset:64512\n"
    "v_mfma_f32_16x16x32_bf16 v[20:23], v[120:123], a[76:79], v[20:23]\n"
    "v_mfma_f32_16x16x32_bf16 v[28:31], v[124:127], a[76:79], v[28:31]\n"
    "ds_read_b128 v[120:123], v233 offset:0\n"
    "v_mfma_f32_16x16x32_bf16 v[24:27], v[124:127], a[72:75], v[24:27]\n"
    "v_mfma_f32_16x16x32_bf16 v[32:35], v[128:131], a[72:75], v[32:35]\n"
    "ds_read_b128 v[124:127], v233 offset:1024\n"
    "v_mfma_f32_16x16x32_bf16 v[36:39], v[128:131], a[76:79], v[36:39]\n"
    "v_mfma_f32_16x16x32_bf16 v[44:47], v[132:135], a[76:79], v[44:47]\n"
    "ds_read_b128 v[128:131], v233 offset:2048\n"
    "v_mfma_f32_16x16x32_bf16 v[40:43], v[132:135], a[72:75], v[40:43]\n"
    "s_waitcnt vmcnt(14)\n"
    "s_waitcnt lgkmcnt(3)\n"
    "v_mfma_f32_16x16x32_bf16 v[0:3], v[136:139], a[80:83], v[0:3]\n"
    "ds_read_b128 v[132:135], v233 offset:3072\n"
    "v_mfma_f32_16x16x32_bf16 v[4:7], v[136:139], a[84:87], v[4:7]\n"
    "v_mfma_f32_16x16x32_bf16 v[12:15], v[140:143], a[84:87], v[12:15]\n"
    "ds_read_b128 v[136:139], v233 offset:4096\n"
    "v_mfma_f32_16x16x32_bf16 v[8:11], v[140:143], a[80:83], v[8:11]\n"
    "v_mfma_f32_16x16x32_bf16 v[16:19], v[144:147], a[80:83], v[16:19]\n"
    "ds_read_b128 v[140:143], v233 offset:5120\n"
    "v_mfma_f32_16x16x32_bf16 v[20:23], v[144:147], a[84:87], v[20:23]\n"
    "v_mfma_f32_16x16x32_bf16 v[28:31], v[148:151], a[84:87], v[28:31]\n"
    "ds_read_b128 v[144:147], v233 offset:6144\n"
    "v_mfma_f32_16x16x32_bf16 v[24:27], v[148:151], a[80:83], v[24:27]\n"
    "s_waitcnt lgkmcnt(3)\n"
    "v_mfma_f32_16x16x32_bf16 v[32:35], v[120:123], a[80:83], v[32:35]\n"
    "ds_read_b128 v[148:151], v233 offset:7168\n"
    "v_mfma_f32_16x16x32_bf16 v[36:39], v[120:123], a[84:87], v[36:39]\n"
    "v_mfma_f32_16x16x32_bf16 v[44:47], v[124:127], a[84:87], v[44:47]\n"
    "s_barrier\n"
    "s_cmp_lg_u32 s35, 0\n"
    "s_cbranch_scc1 Lsig2_%=\n"
    "s_mov_b64 s[42:43], exec\n"
    "s_mov_b64 exec, 1\n"
    "buffer_store_dword v240, v239, %[rs], %[hf] offen offset:32 sc1\n"
    "s_mov_b64 exec, s[42:43]\n"
    "Lsig2_%=:\n"
    "ds_read_b128 v[120:123], v233 offset:8192\n"
    "v_mfma_f32_16x16x32_bf16 v[40:43], v[124:127], a[80:83], v[40:43]\n"
    "s_add_u32 m0, s34, 0\n"
    "s_add_u32 s40, s24, 159744\n"
    "buffer_load_dwordx4 v235, %[rs], s40 offen offset:0 sc1 lds\n"
    "s_waitcnt vmcnt(13)\n"
    "v_mfma_f32_16x16x32_bf16 v[0:3], v[128:131], a[88:91], v[0:3]\n"
    "ds_read_b128 v[124:127], v233 offset:9216\n"
    "v_mfma_f32_16x16x32_bf16 v[4:7], v[128:131], a[92:95], v[4:7]\n"
    "v_mfma_f32_16x16x32_bf16 v[12:15], v[132:135], a[92:95], v[12:15]\n"
    "ds_read_b128 v[128:131], v233 offset:10240\n"
    "v_mfma_f32_16x16x32_bf16 v[8:11], v[132:135], a[88:91], v[8:11]\n"
    "buffer_load_dwordx4 v235, %[rs], s40 offen offset:1024 sc1 lds\n"
    "s_waitcnt lgkmcnt(3)\n"
    "v_mfma_f32_16x16x32_bf16 v[16:19], v[136:139], a[88:91], v[16:19]\n"
    "ds_read_b128 v[132:135], v233 offset:11264\n"
    "v_mfma_f32_16x16x32_bf16 v[20:23], v[136:139], a[92:95], v[20:23]\n"
    "v_mfma_f32_16x16x32_bf16 v[28:31], v[140:143], a[92:95], v[28:31]\n"
    "ds_read_b128 v[136:139], v233 offset:12288\n"
    "v_mfma_f32_16x16x32_bf16 v[24:27], v[140:143], a[88:91], v[24:27]\n"
    "buffer_load_dwordx4 v235, %[rs], s40 offen offset:2048 sc1 lds\n"
    "v_mfma_f32_16x16x32_bf16 v[32:35], v[144:147], a[88:91], v[32:35]\n"
    "ds_read_b128 v[140:143], v233 offset:13312\n"
    "v_mfma_f32_16x16x32_bf16 v[36:39], v[144:147], a[92:95], v[36:39]\n"
    "v_mfma_f32_16x16x32_bf16 v[44:47], v[148:151], a[92:95], v[44:47]\n"
    "ds_read_b128 v[144:147], v233 offset:14336\n"
    "v_mfma_f32_16x16x32_bf16 v[40:43], v[148:151], a[88:91], v[40:43]\n"
    "s_add_u32 m0, s34, 12288\n"
    "s_add_u32 s40, s24, 172032\n"
    "buffer_load_dwordx4 v235, %[rs], s40 offen offset:0 sc1 lds\n"
    "s_waitcnt vmcnt(14)\n"
    "s_waitcnt lgkmcnt(3)\n"
    "v_mfma_f32_16x16x32_bf16 v[0:3], v[120:123], a[96:99], v[0:3]\n"
    "ds_read_b128 v[148:151], v233 offset:15360\n"
    "v_mfma_f32_16x16x32_bf16 v[4:7], v[120:123], a[100:103], v[4:7]\n"
    "v_mfma_f32_16x16x32_bf16 v[12:15], v[124:127], a[100:103], v[12:15]\n"
    "ds_read_b128 v[120:123], v233 offset:16384\n"
    "v_mfma_f32_16x16x32_bf16 v[8:11], v[124:127], a[96:99], v[8:11]\n"
    "buffer_load_dwordx4 v235, %[rs], s40 offen offset:1024 sc1 lds\n"
    "v_mfma_f32_16x16x32_bf16 v[16:19], v[128:131], a[96:99], v[16:19]\n"
    "ds_read_b128 v[124:127], v233 offset:17408\n"
    "v_mfma_f32_16x16x32_bf16 v[20:23], v[128:131], a[100:103], v[20:23]\n"
    "v_mfma_f32_16x16x32_bf16 v[28:31], v[132:135], a[100:103], v[28:31]\n"
    "ds_read_b128 v[128:131], v233 offset:18432\n"
    "v_mfma_f32_16x16x32_bf16 v[24:27], v[132:135], a[96:99], v[24:27]\n"
    "buffer_load_dwordx4 v235, %[rs], s40 offen offset:2048 sc1 lds\n"
    "s_waitcnt lgkmcnt(3)\n"
    "v_mfma_f32_16x16x32_bf16 v[32:35], v[136:139], a[96:99], v[32:35]\n"
    "ds_read_b128 v[132:135], v233 offset:19456\n"
    "v_mfma_f32_16x16x32_bf16 v[36:39], v[136:139], a[100:103], v[36:39]\n"
    "v_mfma_f32_16x16x32_bf16 v[44:47], v[140:143], a[100:103], v[44:47]\n"
    "ds_read_b128 v[136:139], v233 offset:20480\n"
    "v_mfma_f32_16x16x32_bf16 v[40:43], v[140:143], a[96:99], v[40:43]\n"
    "s_add_u32 m0, s34, 24576\n"
    "s_add_u32 s40, s24, 184320\n"
    "buffer_load_dwordx4 v235, %[rs], s40 offen offset:0 sc1 lds\n"
    "s_waitcnt vmcnt(15)\n"
    "v_mfma_f32_16x16x32_bf16 v[0:3], v[144:147], a[104:107], v[0:3]\n"
    "ds_read_b128 v[140:143], v233 offset:21504\n"
    "v_mfma_f32_16x16x32_bf16 v[4:7], v[144:147], a[108:111], v[4:7]\n"
    "v_mfma_f32_16x16x32_bf16 v[12:15], v[148:151], a[108:111], v[12:15]\n"
    "ds_read_b128 v[144:147], v233 offset:22528\n"
    "v_mfma_f32_16x16x32_bf16 v[8:11], v[148:151], a[104:107], v[8:11]\n"
    "s_waitcnt lgkmcnt(3)\n"
    "v_mfma_f32_16x16x32_bf16 v[16:19], v[120:123], a[104:107], v[16:19]\n"
    "ds_read_b128 v[148:151], v233 offset:23552\n"
    "v_mfma_f32_16x16x32_bf16 v[20:23], v[120:123], a[108:111], v[20:23]\n"
    "buffer_load_dwordx4 v235, %[rs], s40 offen offset:1024 sc1 lds\n"
    "v_mfma_f32_16x16x32_bf16 v[28:31], v[124:127], a[108:111], v[28:31]\n"
    "ds_read_b128 v[120:123], v233 offset:24576\n"
    "v_mfma_f32_16x16x32_bf16 v[24:27], v[124:127], a[104:107], v[24:27]\n"
    "v_mfma_f32_16x16x32_bf16 v[32:35], v[128:131], a[104:107], v[32:35]\n"
    "ds_read_b128 v[124:127], v233 offset:25600\n"
    "v_mfma_f32_16x16x32_bf16 v[36:39], v[128:131], a[108:111], v[36:39]\n"
    "buffer_load_dwordx4 v235, %[rs], s40 offen offset:2048 sc1 lds\n"
    "v_mfma_f32_16x16x32_bf16 v[44:47], v[132:135], a[108:111], v[44:47]\n"
    "ds_read_b128 v[128:131], v233 offset:26624\n"
    "v_mfma_f32_16x16x32_bf16 v[40:43], v[132:135], a[104:107], v[40:43]\n"
    "s_waitcnt vmcnt(15)\n"
    "s_waitcnt lgkmcnt(3)\n"
    "v_mfma_f32_16x16x32_bf16 v[0:3], v[136:139], a[112:115], v[0:3]\n"
    "ds_read_b128 v[132:135], v233 offset:27648\n"
    "v_mfma_f32_16x16x32_bf16 v[4:7], v[136:139], a[116:119], v[4:7]\n"
    "s_add_u32 m0, s34, 36864\n"
    "s_add_u32 s40, s24, 196608\n"
    "buffer_load_dwordx4 v235, %[rs], s40 offen offset:0 sc1 lds\n"
    "v_mfma_f32_16x16x32_bf16 v[12:15], v[140:143], a[116:119], v[12:15]\n"
    "ds_read_b128 v[136:139], v233 offset:28672\n"
    "v_mfma_f32_16x16x32_bf16 v[8:11], v[140:143], a[112:115], v[8:11]\n"
    "v_mfma_f32_16x16x32_bf16 v[16:19], v[144:147], a[112:115], v[16:19]\n"
    "ds_read_b128 v[140:143], v233 offset:29696\n"
    "v_mfma_f32_16x16x32_bf16 v[20:23], v[144:147], a[116:119], v[20:23]\n"
    "buffer_load_dwordx4 v235, %[rs], s40 offen offset:1024 sc1 lds\n"
    "v_mfma_f32_16x16x32_bf16 v[28:31], v[148:151], a[116:119], v[28:31]\n"
    "ds_read_b128 v[144:147], v233 offset:30720\n"
    "v_mfma_f32_16x16x32_bf16 v[24:27], v[148:151], a[112:115], v[24:27]\n"
    "s_waitcnt lgkmcnt(3)\n"
    "v_mfma_f32_16x16x32_bf16 v[32:35], v[120:123], a[112:115], v[32:35]\n"
    "ds_read_b128 v[148:151], v233 offset:31744\n"
    "v_mfma_f32_16x16x32_bf16 v[36:39], v[120:123], a[116:119], v[36:39]\n"
    "buffer_load_dwordx4 v235, %[rs], s40 offen offset:2048 sc1 lds\n"
    "v_mfma_f32_16x16x32_bf16 v[44:47], v[124:127], a[116:119], v[44:47]\n"
    "ds_read_b128 v[120:123], v233 offset:32768\n"
    "v_mfma_f32_16x16x32_bf16 v[40:43], v[124:127], a[112:115], v[40:43]\n"
    "s_waitcnt vmcnt(16)\n"
    "v_mfma_f32_16x16x32_bf16 v[0:3], v[128:131], a[120:123], v[0:3]\n"
    "ds_read_b128 v[124:127], v233 offset:33792\n"
    "v_mfma_f32_16x16x32_bf16 v[4:7], v[128:131], a[124:127], v[4:7]\n"
    "s_add_u32 m0, s34, 49152\n"
    "s_add_u32 s40, s24, 208896\n"
    "buffer_load_dwordx4 v235, %[rs], s40 offen offset:0 sc1 lds\n"
    "v_mfma_f32_16x16x32_bf16 v[12:15], v[132:135], a[124:127], v[12:15]\n"
    "ds_read_b128 v[128:131], v233 offset:34816\n"
    "v_mfma_f32_16x16x32_bf16 v[8:11], v[132:135], a[120:123], v[8:11]\n"
    "s_waitcnt lgkmcnt(3)\n"
    "v_mfma_f32_16x16x32_bf16 v[16:19], v[136:139], a[120:123], v[16:19]\n"
    "ds_read_b128 v[132:135], v233 offset:35840\n"
    "v_mfma_f32_16x16x32_bf16 v[20:23], v[136:139], a[124:127], v[20:23]\n"
    "buffer_load_dwordx4 v235, %[rs], s40 offen offset:1024 sc1 lds\n"
    "v_mfma_f32_16x16x32_bf16 v[28:31], v[140:143], a[124:127], v[28:31]\n"
    "ds_read_b128 v[136:139], v233 offset:36864\n"
    "v_mfma_f32_16x16x32_bf16 v[24:27], v[140:143], a[120:123], v[24:27]\n"
    "v_mfma_f32_16x16x32_bf16 v[32:35], v[144:147], a[120:123], v[32:35]\n"
    "ds_read_b128 v[140:143], v233 offset:37888\n"
    "v_mfma_f32_16x16x32_bf16 v[36:39], v[144:147], a[124:127], v[36:39]\n"
    "v_mfma_f32_16x16x32_bf16 v[44:47], v[148:151], a[124:127], v[44:47]\n"
    "ds_read_b128 v[144:147], v233 offset:38912\n"
    "v_mfma_f32_16x16x32_bf16 v[40:43], v[148:151], a[120:123], v[40:43]\n"
    "buffer_load_dwordx4 v235, %[rs], s40 offen offset:2048 sc1 lds\n"
    "s_waitcnt vmcnt(17)\n"
    "s_waitcnt lgkmcnt(3)\n"
    "v_mfma_f32_16x16x32_bf16 v[0:3], v[120:123], a[128:131], v[0:3]\n"
    "ds_read_b128 v[148:151], v233 offset:39936\n"
    "v_mfma_f32_16x16x32_bf16 v[4:7], v[120:123], a[132:135], v[4:7]\n"
    "v_mfma_f32_16x16x32_bf16 v[12:15], v[124:127], a[132:135], v[12:15]\n"
    "ds_read_b128 v[120:123], v233 offset:40960\n"
    "v_mfma_f32_16x16x32_bf16 v[8:11], v[124:127], a[128:131], v[8:11]\n"
    "v_mfma_f32_16x16x32_bf16 v[16:19], v[128:131], a[128:131], v[16:19]\n"
    "ds_read_b128 v[124:127], v233 offset:41984\n"
    "v_mfma_f32_16x16x32_bf16 v[20:23], v[128:131], a[132:135], v[20:23]\n"
    "v_mfma_f32_16x16x32_bf16 v[28:31], v[132:135], a[132:135], v[28:31]\n"
    "ds_read_b128 v[128:131], v233 offset:43008\n"
    "v_mfma_f32_16x16x32_bf16 v[24:27], v[132:135], a[128:131], v[24:27]\n"
    "s_waitcnt lgkmcnt(3)\n"
    "v_mfma_f32_16x16x32_bf16 v[32:35], v[136:139], a[128:131], v[32:35]\n"
    "ds_read_b128 v[132:135], v233 offset:44032\n"
    "v_mfma_f32_16x16x32_bf16 v[36:39], v[136:139], a[132:135], v[36:39]\n"
    "v_mfma_f32_16x16x32_bf16 v[44:47], v[140:143], a[132:135], v[44:47]\n"
    "s_barrier\n"
    "ds_read_b128 v[136:139], v233 offset:45056\n"
    "v_mfma_f32_16x16x32_bf16 v[40:43], v[140:143], a[128:131], v[40:43]\n"
    "s_add_u32 m0, s34, 61440\n"
    "s_add_u32 s40, s24, 221184\n"
    "buffer_load_dwordx4 v235, %[rs], s40 offen offset:0 sc1 lds\n"
    "s_waitcnt vmcnt(16)\n"
    "v_mfma_f32_16x16x32_bf16 v[0:3], v[144:147], a[136:139], v[0:3]\n"
    "ds_read_b128 v[140:143], v233 offset:46080\n"
    "v_mfma_f32_16x16x32_bf16 v[4:7], v[144:147], a[140:143], v[4:7]\n"
    "v_mfma_f32_16x16x32_bf16 v[12:15], v[148:151], a[140:143], v[12:15]\n"
    "ds_read_b128 v[144:147], v233 offset:47104\n"
    "v_mfma_f32_16x16x32_bf16 v[8:11], v[148:151], a[136:139], v[8:11]\n"
    "buffer_load_dwordx4 v235, %[rs], s40 offen offset:1024 sc1 lds\n"
    "s_waitcnt lgkmcnt(3)\n"
    "v_mfma_f32_16x16x32_bf16 v[16:19], v[120:123], a[136:139], v[16:19]\n"
    "ds_read_b128 v[148:151], v233 offset:48128\n"
    "v_mfma_f32_16x16x32_bf16 v[20:23], v[120:123], a[140:143], v[20:23]\n"
    "v_mfma_f32_16x16x32_bf16 v[28:31], v[124:127], a[140:143], v[28:31]\n"
    "ds_read_b128 v[120:123], v233 offset:49152\n"
    "v_mfma_f32_16x16x32_bf16 v[24:27], v[124:127], a[136:139], v[24:27]\n"
    "buffer_load_dwordx4 v235, %[rs], s40 offen offset:2048 sc1 lds\n"
    "v_mfma_f32_16x16x32_bf16 v[32:35], v[128:131], a[136:139], v[32:35]\n"
    "ds_read_b128 v[124:127], v233 offset:50176\n"
    "v_mfma_f32_16x16x32_bf16 v[36:39], v[128:131], a[140:143], v[36:39]\n"
    "v_cvt_pk_bf16_f32 v152, v0, v1\n"
    "v_cvt_pk_bf16_f32 v153, v2, v3\n"
    "v_cvt_pk_bf16_f32 v154, v8, v9\n"
    "v_mfma_f32_16x16x32_bf16 v[44:47], v[132:135], a[140:143], v[44:47]\n"
    "ds_read_b128 v[128:131], v233 offset:51200\n"
    "v_mfma_f32_16x16x32_bf16 v[40:43], v[132:135], a[136:139], v[40:43]\n"
    "s_add_u32 m0, s34, 73728\n"
    "s_add_u32 s40, s24, 233472\n"
    "buffer_load_dwordx4 v235, %[rs], s40 offen offset:0 sc1 lds\n"
    "s_waitcnt lgkmcnt(3)\n"
    "v_mfma_f32_16x16x32_bf16 v[48:51], v[136:139], a[0:3], 0\n"
    "ds_read_b128 v[132:135], v233 offset:52224\n"
    "v_mfma_f32_16x16x32_bf16 v[52:55], v[136:139], a[4:7], 0\n"
    "v_cvt_pk_bf16_f32 v155, v10, v11\n"
    "v_pk_max_i16 v152, v152, 0\n"
    "v_mfma_f32_16x16x32_bf16 v[60:63], v[140:143], a[4:7], 0\n"
    "ds_read_b128 v[136:139], v233 offset:53248\n"
    "v_mfma_f32_16x16x32_bf16 v[56:59], v[140:143], a[0:3], 0\n"
    "buffer_load_dwordx4 v235, %[rs], s40 offen offset:1024 sc1 lds\n"
    "v_pk_max_i16 v153, v153, 0\n"
    "v_mfma_f32_16x16x32_bf16 v[64:67], v[144:147], a[0:3], 0\n"
    "ds_read_b128 v[140:143], v233 offset:54272\n"
    "v_mfma_f32_16x16x32_bf16 v[68:71], v[144:147], a[4:7], 0\n"
    "v_pk_max_i16 v154, v154, 0\n"
    "v_pk_max_i16 v155, v155, 0\n"
    "v_cvt_pk_bf16_f32 v156, v4, v5\n"
    "v_mfma_f32_16x16x32_bf16 v[76:79], v[148:151], a[4:7], 0\n"
    "ds_read_b128 v[144:147], v233 offset:55296\n"
    "v_mfma_f32_16x16x32_bf16 v[72:75], v[148:151], a[0:3], 0\n"
    "s_add_u32 s46, s27, 0\n"
    "buffer_store_dwordx4 v[152:155], v236, %[rs], s46 offen offset:0 sc1\n"
    "v_cvt_pk_bf16_f32 v157, v6, v7\n"
    "s_waitcnt lgkmcnt(3)\n"
    "v_mfma_f32_16x16x32_bf16 v[48:51], v[120:123], a[8:11], v[48:51]\n"
    "ds_read_b128 v[148:151], v233 offset:56320\n"
    "v_mfma_f32_16x16x32_bf16 v[52:55], v[120:123], a[12:15], v[52:55]\n"
    "v_cvt_pk_bf16_f32 v158, v12, v13\n"
    "v_cvt_pk_bf16_f32 v159, v14, v15\n"
    "v_mfma_f32_16x16x32_bf16 v[60:63], v[124:127], a[12:15], v[60:63]\n"
    "ds_read_b128 v[120:123], v233 offset:57344\n"
    "v_mfma_f32_16x16x32_bf16 v[56:59], v[124:127], a[8:11], v[56:59]\n"
    "buffer_load_dwordx4 v235, %[rs], s40 offen offset:2048 sc1 lds\n"
    "v_pk_max_i16 v156, v156, 0\n"
    "v_mfma_f32_16x16x32_bf16 v[64:67], v[128:131], a[8:11], v[64:67]\n"
    "ds_read_b128 v[124:127], v233 offset:58368\n"
    "v_mfma_f32_16x16x32_bf16 v[68:71], v[128:131], a[12:15], v[68:71]\n"
    "v_pk_max_i16 v157, v157, 0\n"
    "v_pk_max_i16 v158, v158, 0\n"
    "v_pk_max_i16 v159, v159, 0\n"
    "v_mfma_f32_16x16x32_bf16 v[76:79], v[132:135], a[12:15], v[76:79]\n"
    "ds_read_b128 v[128:131], v233 offset:59392\n"
    "v_mfma_f32_16x16x32_bf16 v[72:75], v[132:135], a[8:11], v[72:75]\n"
    "v_cvt_pk_bf16_f32 v160, v16, v17\n"
    "v_cvt_pk_bf16_f32 v161, v18, v19\n"
    "v_cvt_pk_bf16_f32 v162, v24, v25\n"
    "s_waitcnt lgkmcnt(3)\n"
    "v_mfma_f32_16x16x32_bf16 v[48:51], v[136:139], a[16:19], v[48:51]\n"
    "ds_read_b128 v[132:135], v233 offset:60416\n"
    "v_mfma_f32_16x16x32_bf16 v[52:55], v[136:139], a[20:23], v[52:55]\n"
    "s_add_u32 m0, s34, 86016\n"
    "s_add_u32 s40, s24, 245760\n"
    "buffer_load_dwordx4 v235, %[rs], s40 offen offset:0 sc1 lds\n"
    "v_mfma_f32_16x16x32_bf16 v[60:63], v[140:143], a[20:23], v[60:63]\n"
    "ds_read_b128 v[136:139], v233 offset:61440\n"
    "v_mfma_f32_16x16x32_bf16 v[56:59], v[140:143], a[16:19], v[56:59]\n"
    "v_cvt_pk_bf16_f32 v163, v26, v27\n"
    "v_mfma_f32_16x16x32_bf16 v[64:67], v[144:147], a[16:19], v[64:67]\n"
    "ds_read_b128 v[140:143], v233 offset:62464\n"
    "v_mfma_f32_16x16x32_bf16 v[68:71], v[144:147], a[20:23], v[68:71]\n"
    "buffer_load_dwordx4 v235, %[rs], s40 offen offset:1024 sc1 lds\n"
    "v_pk_max_i16 v160, v160, 0\n"
    "v_pk_max_i16 v161, v161, 0\n"
    "v_mfma_f32_16x16x32_bf16 v[76:79], v[148:151], a[20:23], v[76:79]\n"
    "ds_read_b128 v[144:147], v233 offset:63488\n"
    "v_mfma_f32_16x16x32_bf16 v[72:75], v[148:151], a[16:19], v[72:75]\n"
    "v_pk_max_i16 v162, v162, 0\n"
    "v_pk_max_i16 v163, v163, 0\n"
    "v_cvt_pk_bf16_f32 v164, v20, v21\n"
    "s_waitcnt lgkmcnt(3)\n"
    "v_mfma_f32_16x16x32_bf16 v[48:51], v[120:123], a[24:27], v[48:51]\n"
    "ds_read_b128 v[148:151], v233 offset:64512\n"
    "v_mfma_f32_16x16x32_bf16 v[52:55], v[120:123], a[28:31], v[52:55]\n"
    "buffer_load_dwordx4 v235, %[rs], s40 offen offset:2048 sc1 lds\n"
    "v_cvt_pk_bf16_f32 v165, v22, v23\n"
    "v_mfma_f32_16x16x32_bf16 v[60:63], v[124:127], a[28:31], v[60:63]\n"
    "ds_read_b128 v[120:123], v234 offset:0\n"
    "v_mfma_f32_16x16x32_bf16 v[56:59], v[124:127], a[24:27], v[56:59]\n"
    "v_cvt_pk_bf16_f32 v166, v28, v29\n"
    "v_cvt_pk_bf16_f32 v167, v30, v31\n"
    "v_mfma_f32_16x16x32_bf16 v[64:67], v[128:131], a[24:27], v[64:67]\n"
    "ds_read_b128 v[124:127], v234 offset:1024\n"
    "v_mfma_f32_16x16x32_bf16 v[68:71], v[128:131], a[28:31], v[68:71]\n"
    "buffer_store_dwordx4 v[156:159], v238, %[rs], s46 offen offset:0 sc1\n"
    "v_pk_max_i16 v164, v164, 0\n"
    "v_pk_max_i16 v165, v165, 0\n"
    "v_mfma_f32_16x16x32_bf16 v[76:79], v[132:135], a[28:31], v[76:79]\n"
    "ds_read_b128 v[128:131], v234 offset:2048\n"
    "v_mfma_f32_16x16x32_bf16 v[72:75], v[132:135], a[24:27], v[72:75]\n"
    "v_pk_max_i16 v166, v166, 0\n"
    "v_pk_max_i16 v167, v167, 0\n"
    "v_cvt_pk_bf16_f32 v168, v32, v33\n"
    "s_waitcnt lgkmcnt(3)\n"
    "v_mfma_f32_16x16x32_bf16 v[48:51], v[136:139], a[32:35], v[48:51]\n"
    "ds_read_b128 v[132:135], v234 offset:3072\n"
    "v_mfma_f32_16x16x32_bf16 v[52:55], v[136:139], a[36:39], v[52:55]\n"
    "v_cvt_pk_bf16_f32 v169, v34, v35\n"
    "v_cvt_pk_bf16_f32 v170, v40, v41\n"
    "v_mfma_f32_16x16x32_bf16 v[60:63], v[140:143], a[36:39], v[60:63]\n"
    "ds_read_b128 v[136:139], v234 offset:4096\n"
    "v_mfma_f32_16x16x32_bf16 v[56:59], v[140:143], a[32:35], v[56:59]\n"
    "v_cvt_pk_bf16_f32 v171, v42, v43\n"
    "v_pk_max_i16 v168, v168, 0\n"
    "v_mfma_f32_16x16x32_bf16 v[64:67], v[144:147], a[32:35], v[64:67]\n"
    "ds_read_b128 v[140:143], v234 offset:5120\n"
    "v_mfma_f32_16x16x32_bf16 v[68:71], v[144:147], a[36:39], v[68:71]\n"
    "v_pk_max_i16 v169, v169, 0\n"
    "v_pk_max_i16 v170, v170, 0\n"
    "v_pk_max_i16 v171, v171, 0\n"
    "v_mfma_f32_16x16x32_bf16 v[76:79], v[148:151], a[36:39], v[76:79]\n"
    "ds_read_b128 v[144:147], v234 offset:6144\n"
    "v_mfma_f32_16x16x32_bf16 v[72:75], v[148:151], a[32:35], v[72:75]\n"
    "v_cvt_pk_bf16_f32 v172, v36, v37\n"
    "v_cvt_pk_bf16_f32 v173, v38, v39\n"
    "v_cvt_pk_bf16_f32 v174, v44, v45\n"
    "s_waitcnt lgkmcnt(3)\n"
    "v_mfma_f32_16x16x32_bf16 v[48:51], v[120:123], a[40:43], v[48:51]\n"
    "ds_read_b128 v[148:151], v234 offset:7168\n"
    "v_mfma_f32_16x16x32_bf16 v[52:55], v[120:123], a[44:47], v[52:55]\n"
    "v_cvt_pk_bf16_f32 v175, v46, v47\n"
    "v_pk_max_i16 v172, v172, 0\n"
    "v_mfma_f32_16x16x32_bf16 v[60:63], v[124:127], a[44:47], v[60:63]\n"
    "ds_read_b128 v[120:123], v234 offset:8192\n"
    "v_mfma_f32_16x16x32_bf16 v[56:59], v[124:127], a[40:43], v[56:59]\n"
    "v_pk_max_i16 v173, v173, 0\n"
    "v_pk_max_i16 v174, v174, 0\n"
    "v_mfma_f32_16x16x32_bf16 v[64:67], v[128:131], a[40:43], v[64:67]\n"
    "ds_read_b128 v[124:127], v234 offset:9216\n"
    "v_mfma_f32_16x16x32_bf16 v[68:71], v[128:131], a[44:47], v[68:71]\n"
    "v_pk_max_i16 v175, v175, 0\n"
    "v_mfma_f32_16x16x32_bf16 v[76:79], v[132:135], a[44:47], v[76:79]\n"
    "ds_read_b128 v[128:131], v234 offset:10240\n"
    "v_mfma_f32_16x16x32_bf16 v[72:75], v[132:135], a[40:43], v[72:75]\n"
    "s_waitcnt lgkmcnt(3)\n"
    "v_mfma_f32_16x16x32_bf16 v[48:51], v[136:139], a[48:51], v[48:51]\n"
    "ds_read_b128 v[132:135], v234 offset:11264\n"
    "v_mfma_f32_16x16x32_bf16 v[52:55], v[136:139], a[52:55], v[52:55]\n"
    "v_mfma_f32_16x16x32_bf16 v[60:63], v[140:143], a[52:55], v[60:63]\n"
    "ds_read_b128 v[136:139], v234 offset:12288\n"
    "v_mfma_f32_16x16x32_bf16 v[56:59], v[140:143], a[48:51], v[56:59]\n"
    "buffer_store_dwordx4 v[160:163], v236, %[rs], s46 offen offset:1024 sc1\n"
    "v_mfma_f32_16x16x32_bf16 v[64:67], v[144:147], a[48:51], v[64:67]\n"
    "ds_read_b128 v[140:143], v234 offset:13312\n"
    "v_mfma_f32_16x16x32_bf16 v[68:71], v[144:147], a[52:55], v[68:71]\n"
    "v_mfma_f32_16x16x32_bf16 v[76:79], v[148:151], a[52:55], v[76:79]\n"
    "ds_read_b128 v[144:147], v234 offset:14336\n"
    "v_mfma_f32_16x16x32_bf16 v[72:75], v[148:151], a[48:51], v[72:75]\n"
    "s_waitcnt lgkmcnt(3)\n"
    "v_mfma_f32_16x16x32_bf16 v[48:51], v[120:123], a[56:59], v[48:51]\n"
    "ds_read_b128 v[148:151], v234 offset:15360\n"
    "v_mfma_f32_16x16x32_bf16 v[52:55], v[120:123], a[60:63], v[52:55]\n"
    "v_mfma_f32_16x16x32_bf16 v[60:63], v[124:127], a[60:63], v[60:63]\n"
    "s_waitcnt vmcnt(21)\n"
    "s_barrier\n"
    "ds_read_b128 v[120:123], v234 offset:16384\n"
    "v_mfma_f32_16x16x32_bf16 v[56:59], v[124:127], a[56:59], v[56:59]\n"
    "s_add_u32 m0, s34, 98304\n"
    "s_add_u32 s40, s24, 258048\n"
    "buffer_load_dwordx4 v235, %[rs], s40 offen offset:0 sc1 lds\n"
    "v_mfma_f32_16x16x32_bf16 v[64:67], v[128:131], a[56:59], v[64:67]\n"
    "ds_read_b128 v[124:127], v234 offset:17408\n"
    "v_mfma_f32_16x16x32_bf16 v[68:71], v[128:131], a[60:63], v[68:71]\n"
    "v_mfma_f32_16x16x32_bf16 v[76:79], v[132:135], a[60:63], v[76:79]\n"
    "ds_read_b128 v[128:131], v234 offset:18432\n"
    "v_mfma_f32_16x16x32_bf16 v[72:75], v[132:135], a[56:59], v[72:75]\n"
    "buffer_load_dwordx4 v235, %[rs], s40 offen offset:1024 sc1 lds\n"
    "s_waitcnt lgkmcnt(3)\n"
    "v_mfma_f32_16x16x32_bf16 v[48:51], v[136:139], a[64:67], v[48:51]\n"
    "ds_read_b128 v[132:135], v234 offset:19456\n"
    "v_mfma_f32_16x16x32_bf16 v[52:55], v[136:139], a[68:71], v[52:55]\n"
    "v_mfma_f32_16x16x32_bf16 v[60:63], v[140:143], a[68:71], v[60:63]\n"
    "ds_read_b128 v[136:139], v234 offset:20480\n"
    "v_mfma_f32_16x16x32_bf16 v[56:59], v[140:143], a[64:67], v[56:59]\n"
    "buffer_load_dwordx4 v235, %[rs], s40 offen offset:2048 sc1 lds\n"
    "v_mfma_f32_16x16x32_bf16 v[64:67], v[144:147], a[64:67], v[64:67]\n"
    "ds_read_b128 v[140:143], v234 offset:21504\n"
    "v_mfma_f32_16x16x32_bf16 v[68:71], v[144:147], a[68:71], v[68:71]\n"
    "v_mfma_f32_16x16x32_bf16 v[76:79], v[148:151], a[68:71], v[76:79]\n"
    "ds_read_b128 v[144:147], v234 offset:22528\n"
    "v_mfma_f32_16x16x32_bf16 v[72:75], v[148:151], a[64:67], v[72:75]\n"
    "s_add_u32 m0, s34, 110592\n"
    "s_add_u32 s40, s24, 270336\n"
    "buffer_load_dwordx4 v235, %[rs], s40 offen offset:0 sc1 lds\n"
    "s_waitcnt lgkmcnt(3)\n"
    "v_mfma_f32_16x16x32_bf16 v[48:51], v[120:123], a[72:75], v[48:51]\n"
    "ds_read_b128 v[148:151], v234 offset:23552\n"
    "v_mfma_f32_16x16x32_bf16 v[52:55], v[120:123], a[76:79], v[52:55]\n"
    "v_mfma_f32_16x16x32_bf16 v[60:63], v[124:127], a[76:79], v[60:63]\n"
    "ds_read_b128 v[120:123], v234 offset:24576\n"
    "v_mfma_f32_16x16x32_bf16 v[56:59], v[124:127], a[72:75], v[56:59]\n"
    "buffer_store_dwordx4 v[164:167], v238, %[rs], s46 offen offset:1024 sc1\n"
    "v_mfma_f32_16x16x32_bf16 v[64:67], v[128:131], a[72:75], v[64:67]\n"
    "ds_read_b128 v[124:127], v234 offset:25600\n"
    "v_mfma_f32_16x16x32_bf16 v[68:71], v[128:131], a[76:79], v[68:71]\n"
    "v_mfma_f32_16x16x32_bf16 v[76:79], v[132:135], a[76:79], v[76:79]\n"
    "ds_read_b128 v[128:131], v234 offset:26624\n"
    "v_mfma_f32_16x16x32_bf16 v[72:75], v[132:135], a[72:75], v[72:75]\n"
    "buffer_load_dwordx4 v235, %[rs], s40 offen offset:1024 sc1 lds\n"
    "s_waitcnt lgkmcnt(3)\n"
    "v_mfma_f32_16x16x32_bf16 v[48:51], v[136:139], a[80:83], v[48:51]\n"
    "ds_read_b128 v[132:135], v234 offset:27648\n"
    "v_mfma_f32_16x16x32_bf16 v[52:55], v[136:139], a[84:87], v[52:55]\n"
    "v_mfma_f32_16x16x32_bf16 v[60:63], v[140:143], a[84:87], v[60:63]\n"
    "ds_read_b128 v[136:139], v232 offset:0\n"
    "v_mfma_f32_16x16x32_bf16 v[56:59], v[140:143], a[80:83], v[56:59]\n"
    "buffer_load_dwordx4 v235, %[rs], s40 offen offset:2048 sc1 lds\n"
    "v_mfma_f32_16x16x32_bf16 v[64:67], v[144:147], a[80:83], v[64:67]\n"
    "ds_read_b128 v[140:143], v232 offset:1024\n"
    "v_mfma_f32_16x16x32_bf16 v[68:71], v[144:147], a[84:87], v[68:71]\n"
    "v_mfma_f32_16x16x32_bf16 v[76:79], v[148:151], a[84:87], v[76:79]\n"
    "ds_read_b128 v[144:147], v232 offset:2048\n"
    "v_mfma_f32_16x16x32_bf16 v[72:75], v[148:151], a[80:83], v[72:75]\n"
    "s_waitcnt lgkmcnt(3)\n"
    "v_mfma_f32_16x16x32_bf16 v[48:51], v[120:123], a[88:91], v[48:51]\n"
    "ds_read_b128 v[148:151], v232 offset:3072\n"
    "v_mfma_f32_16x16x32_bf16 v[52:55], v[120:123], a[92:95], v[52:55]\n"
    "s_add_u32 m0, s34, 122880\n"
    "s_add_u32 s40, s24, 282624\n"
    "buffer_load_dwordx4 v235, %[rs], s40 offen offset:0 sc1 lds\n"
    "v_mfma_f32_16x16x32_bf16 v[60:63], v[124:127], a[92:95], v[60:63]\n"
    "ds_read_b128 v[120:123], v232 offset:4096\n"
    "v_mfma_f32_16x16x32_bf16 v[56:59], v[124:127], a[88:91], v[56:59]\n"
    "v_mfma_f32_16x16x32_bf16 v[64:67], v[128:131], a[88:91], v[64:67]\n"
    "ds_read_b128 v[124:127], v232 offset:5120\n"
    "v_mfma_f32_16x16x32_bf16 v[68:71], v[128:131], a[92:95], v[68:71]\n"
    "buffer_load_dwordx4 v235, %[rs], s40 offen offset:1024 sc1 lds\n"
    "v_mfma_f32_16x16x32_bf16 v[76:79], v[132:135], a[92:95], v[76:79]\n"
    "ds_read_b128 v[128:131], v232 offset:6144\n"
    "v_mfma_f32_16x16x32_bf16 v[72:75], v[132:135], a[88:91], v[72:75]\n"
    "s_waitcnt lgkmcnt(3)\n"
    "v_mfma_f32_16x16x32_bf16 v[48:51], v[136:139], a[96:99], v[48:51]\n"
    "ds_read_b128 v[132:135], v232 offset:7168\n"
    "v_mfma_f32_16x16x32_bf16 v[52:55], v[136:139], a[100:103], v[52:55]\n"
    "buffer_store_dwordx4 v[168:171], v236, %[rs], s46 offen offset:2048 sc1\n"
    "v_mfma_f32_16x16x32_bf16 v[60:63], v[140:143], a[100:103], v[60:63]\n"
    "ds_read_b128 v[136:139], v232 offset:8192\n"
    "v_mfma_f32_16x16x32_bf16 v[56:59], v[140:143], a[96:99], v[56:59]\n"
    "v_mfma_f32_16x16x32_bf16 v[64:67], v[144:147], a[96:99], v[64:67]\n"
    "ds_read_b128 v[140:143], v232 offset:9216\n"
    "v_mfma_f32_16x16x32_bf16 v[68:71], v[144:147], a[100:103], v[68:71]\n"
    "buffer_load_dwordx4 v235, %[rs], s40 offen offset:2048 sc1 lds\n"
    "v_mfma_f32_16x16x32_bf16 v[76:79], v[148:151], a[100:103], v[76:79]\n"
    "ds_read_b128 v[144:147], v232 offset:10240\n"
    "v_mfma_f32_16x16x32_bf16 v[72:75], v[148:151], a[96:99], v[72:75]\n"
    "s_waitcnt lgkmcnt(3)\n"
    "v_mfma_f32_16x16x32_bf16 v[48:51], v[120:123], a[104:107], v[48:51]\n"
    "ds_read_b128 v[148:151], v232 offset:11264\n"
    "v_mfma_f32_16x16x32_bf16 v[52:55], v[120:123], a[108:111], v[52:55]\n"
    "v_mfma_f32_16x16x32_bf16 v[60:63], v[124:127], a[108:111], v[60:63]\n"
    "ds_read_b128 v[120:123], v232 offset:12288\n"
    "v_mfma_f32_16x16x32_bf16 v[56:59], v[124:127], a[104:107], v[56:59]\n"
    "v_mfma_f32_16x16x32_bf16 v[64:67], v[128:131], a[104:107], v[64:67]\n"
    "ds_read_b128 v[124:127], v232 offset:13312\n"
    "v_mfma_f32_16x16x32_bf16 v[68:71], v[128:131], a[108:111], v[68:71]\n"
    "v_mfma_f32_16x16x32_bf16 v[76:79], v[132:135], a[108:111], v[76:79]\n"
    "ds_read_b128 v[128:131], v232 offset:14336\n"
    "v_mfma_f32_16x16x32_bf16 v[72:75], v[132:135], a[104:107], v[72:75]\n"
    "s_waitcnt lgkmcnt(3)\n"
    "v_mfma_f32_16x16x32_bf16 v[48:51], v[136:139], a[112:115], v[48:51]\n"
    "ds_read_b128 v[132:135], v232 offset:15360\n"
    "v_mfma_f32_16x16x32_bf16 v[52:55], v[136:139], a[116:119], v[52:55]\n"
    "v_mfma_f32_16x16x32_bf16 v[60:63], v[140:143], a[116:119], v[60:63]\n"
    "ds_read_b128 v[136:139], v232 offset:16384\n"
    "v_mfma_f32_16x16x32_bf16 v[56:59], v[140:143], a[112:115], v[56:59]\n"
    "v_mfma_f32_16x16x32_bf16 v[64:67], v[144:147], a[112:115], v[64:67]\n"
    "ds_read_b128 v[140:143], v232 offset:17408\n"
    "v_mfma_f32_16x16x32_bf16 v[68:71], v[144:147], a[116:119], v[68:71]\n"
    "v_mfma_f32_16x16x32_bf16 v[76:79], v[148:151], a[116:119], v[76:79]\n"
    "ds_read_b128 v[144:147], v232 offset:18432\n"
    "v_mfma_f32_16x16x32_bf16 v[72:75], v[148:151], a[112:115], v[72:75]\n"
    "buffer_store_dwordx4 v[172:175], v238, %[rs], s46 offen offset:2048 sc1\n"
    "s_waitcnt lgkmcnt(3)\n"
    "v_mfma_f32_16x16x32_bf16 v[48:51], v[120:123], a[120:123], v[48:51]\n"
    "ds_read_b128 v[148:151], v232 offset:19456\n"
    "v_mfma_f32_16x16x32_bf16 v[52:55], v[120:123], a[124:127], v[52:55]\n"
    "v_mfma_f32_16x16x32_bf16 v[60:63], v[124:127], a[124:127], v[60:63]\n"
    "ds_read_b128 v[120:123], v232 offset:20480\n"
    "v_mfma_f32_16x16x32_bf16 v[56:59], v[124:127], a[120:123], v[56:59]\n"
    "v_mfma_f32_16x16x32_bf16 v[64:67], v[128:131], a[120:123], v[64:67]\n"
    "ds_read_b128 v[124:127], v232 offset:21504\n"
    "v_mfma_f32_16x16x32_bf16 v[68:71], v[128:131], a[124:127], v[68:71]\n"
    "v_mfma_f32_16x16x32_bf16 v[76:79], v[132:135], a[124:127], v[76:79]\n"
    "ds_read_b128 v[128:131], v232 offset:22528\n"
    "v_mfma_f32_16x16x32_bf16 v[72:75], v[132:135], a[120:123], v[72:75]\n"
    "s_waitcnt lgkmcnt(3)\n"
    "v_mfma_f32_16x16x32_bf16 v[48:51], v[136:139], a[128:131], v[48:51]\n"
    "ds_read_b128 v[132:135], v232 offset:23552\n"
    "v_mfma_f32_16x16x32_bf16 v[52:55], v[136:139], a[132:135], v[52:55]\n"
    "v_mfma_f32_16x16x32_bf16 v[60:63], v[140:143], a[132:135], v[60:63]\n"
    "s_waitcnt vmcnt(24)\n"
    "s_barrier\n"
    "ds_read_b128 v[136:139], v232 offset:24576\n"
    "v_mfma_f32_16x16x32_bf16 v[56:59], v[140:143], a[128:131], v[56:59]\n"
    "s_add_u32 m0, s34, 135168\n"
    "s_add_u32 s40, s24, 294912\n"
    "buffer_load_dwordx4 v235, %[rs], s40 offen offset:0 sc1 lds\n"
    "v_mfma_f32_16x16x32_bf16 v[64:67], v[144:147], a[128:131], v[64:67]\n"
    "ds_read_b128 v[140:143], v232 offset:25600\n"
    "v_mfma_f32_16x16x32_bf16 v[68:71], v[144:147], a[132:135], v[68:71]\n"
    "v_mfma_f32_16x16x32_bf16 v[76:79], v[148:151], a[132:135], v[76:79]\n"
    "ds_read_b128 v[144:147], v232 offset:26624\n"
    "v_mfma_f32_16x16x32_bf16 v[72:75], v[148:151], a[128:131], v[72:75]\n"
    "buffer_load_dwordx4 v235, %[rs], s40 offen offset:1024 sc1 lds\n"
    "s_waitcnt lgkmcnt(3)\n"
    "v_mfma_f32_16x16x32_bf16 v[48:51], v[120:123], a[136:139], v[48:51]\n"
    "ds_read_b128 v[148:151], v232 offset:27648\n"
    "v_mfma_f32_16x16x32_bf16 v[52:55], v[120:123], a[140:143], v[52:55]\n"
    "v_mfma_f32_16x16x32_bf16 v[60:63], v[124:127], a[140:143], v[60:63]\n"
    "ds_read_b128 v[120:123], v232 offset:28672\n"
    "v_mfma_f32_16x16x32_bf16 v[56:59], v[124:127], a[136:139], v[56:59]\n"
    "buffer_load_dwordx4 v235, %[rs], s40 offen offset:2048 sc1 lds\n"
    "v_mfma_f32_16x16x32_bf16 v[64:67], v[128:131], a[136:139], v[64:67]\n"
    "ds_read_b128 v[124:127], v232 offset:29696\n"
    "v_mfma_f32_16x16x32_bf16 v[68:71], v[128:131], a[140:143], v[68:71]\n"
    "v_mfma_f32_16x16x32_bf16 v[76:79], v[132:135], a[140:143], v[76:79]\n"
    "ds_read_b128 v[128:131], v232 offset:30720\n"
    "v_mfma_f32_16x16x32_bf16 v[72:75], v[132:135], a[136:139], v[72:75]\n"
    "s_add_u32 m0, s34, 147456\n"
    "s_add_u32 s40, s24, 307200\n"
    "buffer_load_dwordx4 v235, %[rs], s40 offen offset:0 sc1 lds\n"
    "s_waitcnt lgkmcnt(3)\n"
    "v_mfma_f32_16x16x32_bf16 v[0:3], v[136:139], a[0:3], 0\n"
    "ds_read_b128 v[132:135], v232 offset:31744\n"
    "v_mfma_f32_16x16x32_bf16 v[4:7], v[136:139], a[4:7], 0\n"
    "v_cvt_pk_bf16_f32 v176, v48, v49\n"
    "v_cvt_pk_bf16_f32 v177, v50, v51\n"
    "v_mfma_f32_16x16x32_bf16 v[12:15], v[140:143], a[4:7], 0\n"
    "ds_read_b128 v[136:139], v232 offset:32768\n"
    "v_mfma_f32_16x16x32_bf16 v[8:11], v[140:143], a[0:3], 0\n"
    "buffer_load_dwordx4 v235, %[rs], s40 offen offset:1024 sc1 lds\n"
    "v_cvt_pk_bf16_f32 v178, v56, v57\n"
    "v_cvt_pk_bf16_f32 v179, v58, v59\n"
    "v_mfma_f32_16x16x32_bf16 v[16:19], v[144:147], a[0:3], 0\n"
    "ds_read_b128 v[140:143], v232 offset:33792\n"
    "v_mfma_f32_16x16x32_bf16 v[20:23], v[144:147], a[4:7], 0\n"
    "v_pk_max_i16 v176, v176, 0\n"
    "v_pk_max_i16 v177, v177, 0\n"
    "v_mfma_f32_16x16x32_bf16 v[28:31], v[148:151], a[4:7], 0\n"
    "ds_read_b128 v[144:147], v232 offset:34816\n"
    "v_mfma_f32_16x16x32_bf16 v[24:27], v[148:151], a[0:3], 0\n"
    "buffer_load_dwordx4 v235, %[rs], s40 offen offset:2048 sc1 lds\n"
    "v_pk_max_i16 v178, v178, 0\n"
    "v_pk_max_i16 v179, v179, 0\n"
    "s_waitcnt lgkmcnt(3)\n"
    "v_mfma_f32_16x16x32_bf16 v[0:3], v[120:123], a[8:11], v[0:3]\n"
    "ds_read_b128 v[148:151], v232 offset:35840\n"
    "v_mfma_f32_16x16x32_bf16 v[4:7], v[120:123], a[12:15], v[4:7]\n"
    "v_cvt_pk_bf16_f32 v180, v52, v53\n"
    "v_cvt_pk_bf16_f32 v181, v54, v55\n"
    "v_mfma_f32_16x16x32_bf16 v[12:15], v[124:127], a[12:15], v[12:15]\n"
    "ds_read_b128 v[120:123], v232 offset:36864\n"
    "v_mfma_f32_16x16x32_bf16 v[8:11], v[124:127], a[8:11], v[8:11]\n"
    "buffer_store_dwordx4 v[176:179], v236, %[rs], s46 offen offset:3072 sc1\n"
    "v_cvt_pk_bf16_f32 v182, v60, v61\n"
    "v_cvt_pk_bf16_f32 v183, v62, v63\n"
    "v_mfma_f32_16x16x32_bf16 v[16:19], v[128:131], a[8:11], v[16:19]\n"
    "ds_read_b128 v[124:127], v232 offset:37888\n"
    "v_mfma_f32_16x16x32_bf16 v[20:23], v[128:131], a[12:15], v[20:23]\n"
    "v_pk_max_i16 v180, v180, 0\n"
    "v_pk_max_i16 v181, v181, 0\n"
    "v_mfma_f32_16x16x32_bf16 v[28:31], v[132:135], a[12:15], v[28:31]\n"
    "ds_read_b128 v[128:131], v232 offset:38912\n"
    "v_mfma_f32_16x16x32_bf16 v[24:27], v[132:135], a[8:11], v[24:27]\n"
    "v_pk_max_i16 v182, v182, 0\n"
    "v_pk_max_i16 v183, v183, 0\n"
    "v_cvt_pk_bf16_f32 v184, v64, v65\n"
    "s_waitcnt lgkmcnt(3)\n"
    "v_mfma_f32_16x16x32_bf16 v[0:3], v[136:139], a[16:19], v[0:3]\n"
    "ds_read_b128 v[132:135], v232 offset:39936\n"
    "v_mfma_f32_16x16x32_bf16 v[4:7], v[136:139], a[20:23], v[4:7]\n"
    "s_add_u32 m0, s34, 0\n"
    "s_add_u32 s40, s24, 319488\n"
    "buffer_load_dwordx4 v235, %[rs], s40 offen offset:0 sc1 lds\n"
    "v_mfma_f32_16x16x32_bf16 v[12:15], v[140:143], a[20:23], v[12:15]\n"
    "ds_read_b128 v[136:139], v232 offset:40960\n"
    "v_mfma_f32_16x16x32_bf16 v[8:11], v[140:143], a[16:19], v[8:11]\n"
    "v_cvt_pk_bf16_f32 v185, v66, v67\n"
    "v_cvt_pk_bf16_f32 v186, v72, v73\n"
    "v_mfma_f32_16x16x32_bf16 v[16:19], v[144:147], a[16:19], v[16:19]\n"
    "ds_read_b128 v[140:143], v232 offset:41984\n"
    "v_mfma_f32_16x16x32_bf16 v[20:23], v[144:147], a[20:23], v[20:23]\n"
    "buffer_load_dwordx4 v235, %[rs], s40 offen offset:1024 sc1 lds\n"
    "v_cvt_pk_bf16_f32 v187, v74, v75\n"
    "v_mfma_f32_16x16x32_bf16 v[28:31], v[148:151], a[20:23], v[28:31]\n"
    "ds_read_b128 v[144:147], v232 offset:43008\n"
    "v_mfma_f32_16x16x32_bf16 v[24:27], v[148:151], a[16:19], v[24:27]\n"
    "v_pk_max_i16 v184, v184, 0\n"
    "v_pk_max_i16 v185, v185, 0\n"
    "v_pk_max_i16 v186, v186, 0\n"
    "s_waitcnt lgkmcnt(3)\n"
    "v_mfma_f32_16x16x32_bf16 v[0:3], v[120:123], a[24:27], v[0:3]\n"
    "ds_read_b128 v[148:151], v232 offset:44032\n"
    "v_mfma_f32_16x16x32_bf16 v[4:7], v[120:123], a[28:31], v[4:7]\n"
    "buffer_load_dwordx4 v235, %[rs], s40 offen offset:2048 sc1 lds\n"
    "v_pk_max_i16 v187, v187, 0\n"
    "v_mfma_f32_16x16x32_bf16 v[12:15], v[124:127], a[28:31], v[12:15]\n"
    "ds_read_b128 v[120:123], v232 offset:45056\n"
    "v_mfma_f32_16x16x32_bf16 v[8:11], v[124:127], a[24:27], v[8:11]\n"
    "v_cvt_pk_bf16_f32 v188, v68, v69\n"
    "v_cvt_pk_bf16_f32 v189, v70, v71\n"
    "v_cvt_pk_bf16_f32 v190, v76, v77\n"
    "v_mfma_f32_16x16x32_bf16 v[16:19], v[128:131], a[24:27], v[16:19]\n"
    "ds_read_b128 v[124:127], v232 offset:46080\n"
    "v_mfma_f32_16x16x32_bf16 v[20:23], v[128:131], a[28:31], v[20:23]\n"
    "v_cvt_pk_bf16_f32 v191, v78, v79\n"
    "v_pk_max_i16 v188, v188, 0\n"
    "v_mfma_f32_16x16x32_bf16 v[28:31], v[132:135], a[28:31], v[28:31]\n"
    "ds_read_b128 v[128:131], v232 offset:47104\n"
    "v_mfma_f32_16x16x32_bf16 v[24:27], v[132:135], a[24:27], v[24:27]\n"
    "v_pk_max_i16 v189, v189, 0\n"
    "v_pk_max_i16 v190, v190, 0\n"
    "v_pk_max_i16 v191, v191, 0\n"
    "s_waitcnt lgkmcnt(3)\n"
    "v_mfma_f32_16x16x32_bf16 v[0:3], v[136:139], a[32:35], v[0:3]\n"
    "ds_read_b128 v[132:135], v232 offset:48128\n"
    "v_mfma_f32_16x16x32_bf16 v[4:7], v[136:139], a[36:39], v[4:7]\n"
    "buffer_store_dwordx4 v[180:183], v238, %[rs], s46 offen offset:3072 sc1\n"
    "v_mfma_f32_16x16x32_bf16 v[12:15], v[140:143], a[36:39], v[12:15]\n"
    "ds_read_b128 v[136:139], v232 offset:49152\n"
    "v_mfma_f32_16x16x32_bf16 v[8:11], v[140:143], a[32:35], v[8:11]\n"
    "v_mfma_f32_16x16x32_bf16 v[16:19], v[144:147], a[32:35], v[16:19]\n"
    "ds_read_b128 v[140:143], v232 offset:50176\n"
    "v_mfma_f32_16x16x32_bf16 v[20:23], v[144:147], a[36:39], v[20:23]\n"
    "v_mfma_f32_16x16x32_bf16 v[28:31], v[148:151], a[36:39], v[28:31]\n"
    "ds_read_b128 v[144:147], v232 offset:51200\n"
    "v_mfma_f32_16x16x32_bf16 v[24:27], v[148:151], a[32:35], v[24:27]\n"
    "s_waitcnt lgkmcnt(3)\n"
    "v_mfma_f32_16x16x32_bf16 v[0:3], v[120:123], a[40:43], v[0:3]\n"
    "ds_read_b128 v[148:151], v232 offset:52224\n"
    "v_mfma_f32_16x16x32_bf16 v[4:7], v[120:123], a[44:47], v[4:7]\n"
    "v_mfma_f32_16x16x32_bf16 v[12:15], v[124:127], a[44:47], v[12:15]\n"
    "ds_read_b128 v[120:123], v232 offset:53248\n"
    "v_mfma_f32_16x16x32_bf16 v[8:11], v[124:127], a[40:43], v[8:11]\n"
    "v_mfma_f32_16x16x32_bf16 v[16:19], v[128:131], a[40:43], v[16:19]\n"
    "ds_read_b128 v[124:127], v232 offset:54272\n"
    "v_mfma_f32_16x16x32_bf16 v[20:23], v[128:131], a[44:47], v[20:23]\n"
    "v_mfma_f32_16x16x32_bf16 v[28:31], v[132:135], a[44:47], v[28:31]\n"
    "ds_read_b128 v[128:131], v232 offset:55296\n"
    "v_mfma_f32_16x16x32_bf16 v[24:27], v[132:135], a[40:43], v[24:27]\n"
    "s_waitcnt lgkmcnt(3)\n"
    "v_mfma_f32_16x16x32_bf16 v[0:3], v[136:139], a[48:51], v[0:3]\n"
    "ds_read_b128 v[132:135], v232 offset:56320\n"
    "v_mfma_f32_16x16x32_bf16 v[4:7], v[136:139], a[52:55], v[4:7]\n"
    "v_mfma_f32_16x16x32_bf16 v[12:15], v[140:143], a[52:55], v[12:15]\n"
    "ds_read_b128 v[136:139], v232 offset:57344\n"
    "v_mfma_f32_16x16x32_bf16 v[8:11], v[140:143], a[48:51], v[8:11]\n"
    "v_mfma_f32_16x16x32_bf16 v[16:19], v[144:147], a[48:51], v[16:19]\n"
    "ds_read_b128 v[140:143], v232 offset:58368\n"
    "v_mfma_f32_16x16x32_bf16 v[20:23], v[144:147], a[52:55], v[20:23]\n"
    "v_mfma_f32_16x16x32_bf16 v[28:31], v[148:151], a[52:55], v[28:31]\n"
    "ds_read_b128 v[144:147], v232 offset:59392\n"
    "v_mfma_f32_16x16x32_bf16 v[24:27], v[148:151], a[48:51], v[24:27]\n"
    "s_add_u32 s46, s27, 4096\n"
    "buffer_store_dwordx4 v[184:187], v236, %[rs], s46 offen offset:0 sc1\n"
    "s_waitcnt lgkmcnt(3)\n"
    "v_mfma_f32_16x16x32_bf16 v[0:3], v[120:123], a[56:59], v[0:3]\n"
    "ds_read_b128 v[148:151], v232 offset:60416\n"
    "v_mfma_f32_16x16x32_bf16 v[4:7], v[120:123], a[60:63], v[4:7]\n"
    "v_mfma_f32_16x16x32_bf16 v[12:15], v[124:127], a[60:63], v[12:15]\n"
    "s_waitcnt vmcnt(12)\n"
    "s_barrier\n"
    "s_cmp_lg_u32 s35, 0\n"
    "s_cbranch_scc1 Lsig3_%=\n"
    "s_mov_b64 s[42:43], exec\n"
    "s_mov_b64 exec, 1\n"
    "buffer_atomic_add v240, v239, %[rs], s32 offen offset:0\n"
    "s_mov_b64 exec, s[42:43]\n"
    "Lsig3_%=:\n"
    "ds_read_b128 v[120:123], v232 offset:61440\n"
    "v_mfma_f32_16x16x32_bf16 v[8:11], v[124:127], a[56:59], v[8:11]\n"
    "s_add_u32 m0, s34, 12288\n"
    "s_add_u32 s40, s25, 0\n"
    "buffer_load_dwordx4 v235, %[rs], s40 offen offset:0 sc1 lds\n"
    "v_mfma_f32_16x16x32_bf16 v[16:19], v[128:131], a[56:59], v[16:19]\n"
    "ds_read_b128 v[124:127], v232 offset:62464\n"
    "v_mfma_f32_16x16x32_bf16 v[20:23], v[128:131], a[60:63], v[20:23]\n"
    "v_mfma_f32_16x16x32_bf16 v[28:31], v[132:135], a[60:63], v[28:31]\n"
    "ds_read_b128 v[128:131], v232 offset:63488\n"
    "v_mfma_f32_16x16x32_bf16 v[24:27], v[132:135], a[56:59], v[24:27]\n"
    "buffer_load_dwordx4 v235, %[rs], s40 offen offset:1024 sc1 lds\n"
    "s_waitcnt lgkmcnt(3)\n"
    "v_mfma_f32_16x16x32_bf16 v[0:3], v[136:139], a[64:67], v[0:3]\n"
    "ds_read_b128 v[132:135], v232 offset:64512\n"
    "v_mfma_f32_16x16x32_bf16 v[4:7], v[136:139], a[68:71], v[4:7]\n"
    "v_mfma_f32_16x16x32_bf16 v[12:15], v[140:143], a[68:71], v[12:15]\n"
    "ds_read_b128 v[136:139], v233 offset:0\n"
    "v_mfma_f32_16x16x32_bf16 v[8:11], v[140:143], a[64:67], v[8:11]\n"
    "buffer_load_dwordx4 v235, %[rs], s40 offen offset:2048 sc1 lds\n"
    "v_mfma_f32_16x16x32_bf16 v[16:19], v[144:147], a[64:67], v[16:19]\n"
    "ds_read_b128 v[140:143], v233 offset:1024\n"
    "v_mfma_f32_16x16x32_bf16 v[20:23], v[144:147], a[68:71], v[20:23]\n"
    "v_mfma_f32_16x16x32_bf16 v[28:31], v[148:151], a[68:71], v[28:31]\n"
    "ds_read_b128 v[144:147], v233 offset:2048\n"
    "v_mfma_f32_16x16x32_bf16 v[24:27], v[148:151], a[64:67], v[24:27]\n"
    "s_add_u32 m0, s34, 24576\n"
    "s_add_u32 s40, s25, 12288\n"
    "buffer_load_dwordx4 v235, %[rs], s40 offen offset:0 sc1 lds\n"
    "s_waitcnt lgkmcnt(3)\n"
    "v_mfma_f32_16x16x32_bf16 v[0:3], v[120:123], a[72:75], v[0:3]\n"
    "ds_read_b128 v[148:151], v233 offset:3072\n"
    "v_mfma_f32_16x16x32_bf16 v[4:7], v[120:123], a[76:79], v[4:7]\n"
    "v_mfma_f32_16x16x32_bf16 v[12:15], v[124:127], a[76:79], v[12:15]\n"
    "ds_read_b128 v[120:123], v233 offset:4096\n"
    "v_mfma_f32_16x16x32_bf16 v[8:11], v[124:127], a[72:75], v[8:11]\n"
    "buffer_load_dwordx4 v235, %[rs], s40 offen offset:1024 sc1 lds\n"
    "v_mfma_f32_16x16x32_bf16 v[16:19], v[128:131], a[72:75], v[16:19]\n"
    "ds_read_b128 v[124:127], v233 offset:5120\n"
    "v_mfma_f32_16x16x32_bf16 v[20:23], v[128:131], a[76:79], v[20:23]\n"
    "v_mfma_f32_16x16x32_bf16 v[28:31], v[132:135], a[76:79], v[28:31]\n"
    "ds_read_b128 v[128:131], v233 offset:6144\n"
    "v_mfma_f32_16x16x32_bf16 v[24:27], v[132:135], a[72:75], v[24:27]\n"
    "buffer_store_dwordx4 v[188:191], v238, %[rs], s46 offen offset:0 sc1\n"
    "s_waitcnt lgkmcnt(3)\n"
    "v_mfma_f32_16x16x32_bf16 v[0:3], v[136:139], a[80:83], v[0:3]\n"
    "ds_read_b128 v[132:135], v233 offset:7168\n"
    "v_mfma_f32_16x16x32_bf16 v[4:7], v[136:139], a[84:87], v[4:7]\n"
    "v_mfma_f32_16x16x32_bf16 v[12:15], v[140:143], a[84:87], v[12:15]\n"
    "ds_read_b128 v[136:139], v233 offset:8192\n"
    "v_mfma_f32_16x16x32_bf16 v[8:11], v[140:143], a[80:83], v[8:11]\n"
    "v_mfma_f32_16x16x32_bf16 v[16:19], v[144:147], a[80:83], v[16:19]\n"
    "ds_read_b128 v[140:143], v233 offset:9216\n"
    "v_mfma_f32_16x16x32_bf16 v[20:23], v[144:147], a[84:87], v[20:23]\n"
    "buffer_load_dword v225, v243, %[rs], %[hp] offen offset:32 sc1\n"
    "v_mfma_f32_16x16x32_bf16 v[28:31], v[148:151], a[84:87], v[28:31]\n"
    "ds_read_b128 v[144:147], v233 offset:10240\n"
    "v_mfma_f32_16x16x32_bf16 v[24:27], v[148:151], a[80:83], v[24:27]\n"
    "s_waitcnt lgkmcnt(3)\n"
    "v_mfma_f32_16x16x32_bf16 v[0:3], v[120:123], a[88:91], v[0:3]\n"
    "ds_read_b128 v[148:151], v233 offset:11264\n"
    "v_mfma_f32_16x16x32_bf16 v[4:7], v[120:123], a[92:95], v[4:7]\n"
    "buffer_load_dwordx4 v235, %[rs], s40 offen offset:2048 sc1 lds\n"
    "v_mfma_f32_16x16x32_bf16 v[12:15], v[124:127], a[92:95], v[12:15]\n"
    "ds_read_b128 v[120:123], v233 offset:12288\n"
    "v_mfma_f32_16x16x32_bf16 v[8:11], v[124:127], a[88:91], v[8:11]\n"
    "v_mfma_f32_16x16x32_bf16 v[16:19], v[128:131], a[88:91], v[16:19]\n"
    "ds_read_b128 v[124:127], v233 offset:13312\n"
    "v_mfma_f32_16x16x32_bf16 v[20:23], v[128:131], a[92:95], v[20:23]\n"
    "s_add_u32 m0, s34, 36864\n"
    "s_add_u32 s40, s25, 24576\n"
    "buffer_load_dwordx4 v235, %[rs], s40 offen offset:0 sc1 lds\n"
    "v_mfma_f32_16x16x32_bf16 v[28:31], v[132:135], a[92:95], v[28:31]\n"
    "ds_read_b128 v[128:131], v233 offset:14336\n"
    "v_mfma_f32_16x16x32_bf16 v[24:27], v[132:135], a[88:91], v[24:27]\n"
    "s_waitcnt lgkmcnt(3)\n"
    "v_mfma_f32_16x16x32_bf16 v[0:3], v[136:139], a[96:99], v[0:3]\n"
    "ds_read_b128 v[132:135], v233 offset:15360\n"
    "v_mfma_f32_16x16x32_bf16 v[4:7], v[136:139], a[100:103], v[4:7]\n"
    "buffer_load_dwordx4 v235, %[rs], s40 offen offset:1024 sc1 lds\n"
    "v_mfma_f32_16x16x32_bf16 v[12:15], v[140:143], a[100:103], v[12:15]\n"
    "ds_read_b128 v[136:139], v233 offset:16384\n"
    "v_mfma_f32_16x16x32_bf16 v[8:11], v[140:143], a[96:99], v[8:11]\n"
    "v_mfma_f32_16x16x32_bf16 v[16:19], v[144:147], a[96:99], v[16:19]\n"
    "ds_read_b128 v[140:143], v233 offset:17408\n"
    "v_mfma_f32_16x16x32_bf16 v[20:23], v[144:147], a[100:103], v[20:23]\n"
    "buffer_load_dwordx4 v235, %[rs], s40 offen offset:2048 sc1 lds\n"
    "v_mfma_f32_16x16x32_bf16 v[28:31], v[148:151], a[100:103], v[28:31]\n"
    "ds_read_b128 v[144:147], v233 offset:18432\n"
    "v_mfma_f32_16x16x32_bf16 v[24:27], v[148:151], a[96:99], v[24:27]\n"
    "s_waitcnt lgkmcnt(3)\n"
    "v_mfma_f32_16x16x32_bf16 v[0:3], v[120:123], a[104:107], v[0:3]\n"
    "ds_read_b128 v[148:151], v233 offset:19456\n"
    "v_mfma_f32_16x16x32_bf16 v[4:7], v[120:123], a[108:111], v[4:7]\n"
    "v_mfma_f32_16x16x32_bf16 v[12:15], v[124:127], a[108:111], v[12:15]\n"
    "ds_read_b128 v[120:123], v233 offset:20480\n"
    "v_mfma_f32_16x16x32_bf16 v[8:11], v[124:127], a[104:107], v[8:11]\n"
    "v_mfma_f32_16x16x32_bf16 v[16:19], v[128:131], a[104:107], v[16:19]\n"
    "ds_read_b128 v[124:127], v233 offset:21504\n"
    "v_mfma_f32_16x16x32_bf16 v[20:23], v[128:131], a[108:111], v[20:23]\n"
    "v_mfma_f32_16x16x32_bf16 v[28:31], v[132:135], a[108:111], v[28:31]\n"
    "ds_read_b128 v[128:131], v233 offset:22528\n"
    "v_mfma_f32_16x16x32_bf16 v[24:27], v[132:135], a[104:107], v[24:27]\n"
    "s_waitcnt lgkmcnt(3)\n"
    "v_mfma_f32_16x16x32_bf16 v[0:3], v[136:139], a[112:115], v[0:3]\n"
    "ds_read_b128 v[132:135], v233 offset:23552\n"
    "v_mfma_f32_16x16x32_bf16 v[4:7], v[136:139], a[116:119], v[4:7]\n"
    "v_mfma_f32_16x16x32_bf16 v[12:15], v[140:143], a[116:119], v[12:15]\n"
    "ds_read_b128 v[136:139], v233 offset:24576\n"
    "v_mfma_f32_16x16x32_bf16 v[8:11], v[140:143], a[112:115], v[8:11]\n"
    "v_mfma_f32_16x16x32_bf16 v[16:19], v[144:147], a[112:115], v[16:19]\n"
    "ds_read_b128 v[140:143], v233 offset:25600\n"
    "v_mfma_f32_16x16x32_bf16 v[20:23], v[144:147], a[116:119], v[20:23]\n"
    "v_mfma_f32_16x16x32_bf16 v[28:31], v[148:151], a[116:119], v[28:31]\n"
    "ds_read_b128 v[144:147], v233 offset:26624\n"
    "v_mfma_f32_16x16x32_bf16 v[24:27], v[148:151], a[112:115], v[24:27]\n"
    "s_waitcnt lgkmcnt(3)\n"
    "v_mfma_f32_16x16x32_bf16 v[0:3], v[120:123], a[120:123], v[0:3]\n"
    "ds_read_b128 v[148:151], v233 offset:27648\n"
    "v_mfma_f32_16x16x32_bf16 v[4:7], v[120:123], a[124:127], v[4:7]\n"
    "v_mfma_f32_16x16x32_bf16 v[12:15], v[124:127], a[124:127], v[12:15]\n"
    "ds_read_b128 v[120:123], v233 offset:28672\n"
    "v_mfma_f32_16x16x32_bf16 v[8:11], v[124:127], a[120:123], v[8:11]\n"
    "v_mfma_f32_16x16x32_bf16 v[16:19], v[128:131], a[120:123], v[16:19]\n"
    "ds_read_b128 v[124:127], v233 offset:29696\n"
    "v_mfma_f32_16x16x32_bf16 v[20:23], v[128:131], a[124:127], v[20:23]\n"
    "v_mfma_f32_16x16x32_bf16 v[28:31], v[132:135], a[124:127], v[28:31]\n"
    "ds_read_b128 v[128:131], v233 offset:30720\n"
    "v_mfma_f32_16x16x32_bf16 v[24:27], v[132:135], a[120:123], v[24:27]\n"
    "s_waitcnt lgkmcnt(3)\n"
    "v_mfma_f32_16x16x32_bf16 v[0:3], v[136:139], a[128:131], v[0:3]\n"
    "ds_read_b128 v[132:135], v233 offset:31744\n"
    "v_mfma_f32_16x16x32_bf16 v[4:7], v[136:139], a[132:135], v[4:7]\n"
    "v_mfma_f32_16x16x32_bf16 v[12:15], v[140:143], a[132:135], v[12:15]\n"
    "s_barrier\n"
    "ds_read_b128 v[136:139], v233 offset:32768\n"
    "v_mfma_f32_16x16x32_bf16 v[8:11], v[140:143], a[128:131], v[8:11]\n"
    "s_add_u32 m0, s34, 49152\n"
    "s_add_u32 s40, s25, 36864\n"
    "buffer_load_dwordx4 v235, %[rs], s40 offen offset:0 sc1 lds\n"
    "v_mfma_f32_16x16x32_bf16 v[16:19], v[144:147], a[128:131], v[16:19]\n"
    "ds_read_b128 v[140:143], v233 offset:33792\n"
    "v_mfma_f32_16x16x32_bf16 v[20:23], v[144:147], a[132:135], v[20:23]\n"
    "s_waitcnt vmcnt(5)\n"
    "v_cmp_ne_u32 vcc, 1, v225\n"
    "s_mov_b32 s44, 0x10000\n"
    "s_nop 3\n"
    "s_cbranch_vccz Lp5_e_%=\n"
    "Lp5_p_%=:\n"
    "buffer_load_dword v225, v243, %[rs], %[hp] offen offset:32 sc1\n"
    "s_waitcnt vmcnt(0)\n"
    "v_cmp_ne_u32 vcc, 1, v225\n"
    "s_sub_u32 s44, s44, 1\n"
    "s_cbranch_scc1 Lp5_e_%=\n"
    "s_sleep 1\n"
    "s_cbranch_vccnz Lp5_p_%=\n"
    "Lp5_e_%=:\n"
    "v_mfma_f32_16x16x32_bf16 v[28:31], v[148:151], a[132:135], v[28:31]\n"
    "ds_read_b128 v[144:147], v233 offset:34816\n"
    "v_mfma_f32_16x16x32_bf16 v[24:27], v[148:151], a[128:131], v[24:27]\n"
    "buffer_load_dwordx4 v235, %[rs], s40 offen offset:1024 sc1 lds\n"
    "s_waitcnt lgkmcnt(3)\n"
    "v_mfma_f32_16x16x32_bf16 v[0:3], v[120:123], a[136:139], v[0:3]\n"
    "ds_read_b128 v[148:151], v233 offset:35840\n"
    "v_mfma_f32_16x16x32_bf16 v[4:7], v[120:123], a[140:143], v[4:7]\n"
    "v_mfma_f32_16x16x32_bf16 v[12:15], v[124:127], a[140:143], v[12:15]\n"
    "ds_read_b128 v[120:123], v233 offset:36864\n"
    "v_mfma_f32_16x16x32_bf16 v[8:11], v[124:127], a[136:139], v[8:11]\n"
    "buffer_load_dwordx4 v235, %[rs], s40 offen offset:2048 sc1 lds\n"
    "v_mfma_f32_16x16x32_bf16 v[16:19], v[128:131], a[136:139], v[16:19]\n"
    "ds_read_b128 v[124:127], v233 offset:37888\n"
    "v_mfma_f32_16x16x32_bf16 v[20:23], v[128:131], a[140:143], v[20:23]\n"
    "v_mfma_f32_16x16x32_bf16 v[28:31], v[132:135], a[140:143], v[28:31]\n"
    "ds_read_b128 v[128:131], v233 offset:38912\n"
    "v_mfma_f32_16x16x32_bf16 v[24:27], v[132:135], a[136:139], v[24:27]\n"
    "s_add_u32 m0, s34, 61440\n"
    "s_add_u32 s40, s25, 49152\n"
    "buffer_load_dwordx4 v235, %[rs], s40 offen offset:0 sc1 lds\n"
    "s_waitcnt lgkmcnt(3)\n"
    "v_mfma_f32_16x16x32_bf16 v[56:59], v[136:139], a[0:3], 0\n"
    "ds_read_b128 v[132:135], v233 offset:39936\n"
    "v_mfma_f32_16x16x32_bf16 v[60:63], v[136:139], a[4:7], 0\n"
    "v_cvt_pk_bf16_f32 v192, v0, v1\n"
    "v_cvt_pk_bf16_f32 v193, v2, v3\n"
    "v_mfma_f32_16x16x32_bf16 v[68:71], v[140:143], a[4:7], 0\n"
    "ds_read_b128 v[136:139], v233 offset:40960\n"
    "v_mfma_f32_16x16x32_bf16 v[64:67], v[140:143], a[0:3], 0\n"
    "buffer_load_dwordx4 v235, %[rs], s40 offen offset:1024 sc1 lds\n"
    "v_cvt_pk_bf16_f32 v194, v8, v9\n"
    "v_cvt_pk_bf16_f32 v195, v10, v11\n"
    "v_mfma_f32_16x16x32_bf16 v[72:75], v[144:147], a[0:3], 0\n"
    "ds_read_b128 v[140:143], v233 offset:41984\n"
    "v_mfma_f32_16x16x32_bf16 v[76:79], v[144:147], a[4:7], 0\n"
    "v_pk_max_i16 v192, v192, 0\n"
    "v_pk_max_i16 v193, v193, 0\n"
    "v_mfma_f32_16x16x32_bf16 v[84:87], v[148:151], a[4:7], 0\n"
    "ds_read_b128 v[144:147], v233 offset:43008\n"
    "v_mfma_f32_16x16x32_bf16 v[80:83], v[148:151], a[0:3], 0\n"
    "buffer_load_dwordx4 v235, %[rs], s40 offen offset:2048 sc1 lds\n"
    "v_pk_max_i16 v194, v194, 0\n"
    "v_pk_max_i16 v195, v195, 0\n"
    "s_waitcnt lgkmcnt(3)\n"
    "v_mfma_f32_16x16x32_bf16 v[56:59], v[120:123], a[8:11], v[56:59]\n"
    "ds_read_b128 v[148:151], v233 offset:44032\n"
    "v_mfma_f32_16x16x32_bf16 v[60:63], v[120:123], a[12:15], v[60:63]\n"
    "v_cvt_pk_bf16_f32 v196, v4, v5\n"
    "v_cvt_pk_bf16_f32 v197, v6, v7\n"
    "v_mfma_f32_16x16x32_bf16 v[68:71], v[124:127], a[12:15], v[68:71]\n"
    "ds_read_b128 v[120:123], v233 offset:45056\n"
    "v_mfma_f32_16x16x32_bf16 v[64:67], v[124:127], a[8:11], v[64:67]\n"
    "buffer_store_dwordx4 v[192:195], v236, %[rs], s46 offen offset:1024 sc1\n"
    "v_cvt_pk_bf16_f32 v198, v12, v13\n"
    "v_cvt_pk_bf16_f32 v199, v14, v15\n"
    "v_mfma_f32_16x16x32_bf16 v[72:75], v[128:131], a[8:11], v[72:75]\n"
    "ds_read_b128 v[124:127], v233 offset:46080\n"
    "v_mfma_f32_16x16x32_bf16 v[76:79], v[128:131], a[12:15], v[76:79]\n"
    "v_pk_max_i16 v196, v196, 0\n"
    "v_pk_max_i16 v197, v197, 0\n"
    "v_mfma_f32_16x16x32_bf16 v[84:87], v[132:135], a[12:15], v[84:87]\n"
    "ds_read_b128 v[128:131], v233 offset:47104\n"
    "v_mfma_f32_16x16x32_bf16 v[80:83], v[132:135], a[8:11], v[80:83]\n"
    "v_pk_max_i16 v198, v198, 0\n"
    "v_pk_max_i16 v199, v199, 0\n"
    "v_cvt_pk_bf16_f32 v200, v16, v17\n"
    "s_waitcnt lgkmcnt(3)\n"
    "v_mfma_f32_16x16x32_bf16 v[56:59], v[136:139], a[16:19], v[56:59]\n"
    "ds_read_b128 v[132:135], v233 offset:48128\n"
    "v_mfma_f32_16x16x32_bf16 v[60:63], v[136:139], a[20:23], v[60:63]\n"
    "s_add_u32 m0, s34, 73728\n"
    "s_add_u32 s40, s25, 61440\n"
    "buffer_load_dwordx4 v235, %[rs], s40 offen offset:0 sc1 lds\n"
    "v_mfma_f32_16x16x32_bf16 v[68:71], v[140:143], a[20:23], v[68:71]\n"
    "ds_read_b128 v[136:139], v233 offset:49152\n"
    "v_mfma_f32_16x16x32_bf16 v[64:67], v[140:143], a[16:19], v[64:67]\n"
    "v_cvt_pk_bf16_f32 v201, v18, v19\n"
    "v_cvt_pk_bf16_f32 v202, v24, v25\n"
    "v_mfma_f32_16x16x32_bf16 v[72:75], v[144:147], a[16:19], v[72:75]\n"
    "ds_read_b128 v[140:143], v233 offset:50176\n"
    "v_mfma_f32_16x16x32_bf16 v[76:79], v[144:147], a[20:23], v[76:79]\n"
    "buffer_load_dwordx4 v235, %[rs], s40 offen offset:1024 sc1 lds\n"
    "v_cvt_pk_bf16_f32 v203, v26, v27\n"
    "v_mfma_f32_16x16x32_bf16 v[84:87], v[148:151], a[20:23], v[84:87]\n"
    "ds_read_b128 v[144:147], v233 offset:51200\n"
    "v_mfma_f32_16x16x32_bf16 v[80:83], v[148:151], a[16:19], v[80:83]\n"
    "v_pk_max_i16 v200, v200, 0\n"
    "v_pk_max_i16 v201, v201, 0\n"
    "v_pk_max_i16 v202, v202, 0\n"
    "s_waitcnt lgkmcnt(3)\n"
    "v_mfma_f32_16x16x32_bf16 v[56:59], v[120:123], a[24:27], v[56:59]\n"
    "ds_read_b128 v[148:151], v233 offset:52224\n"
    "v_mfma_f32_16x16x32_bf16 v[60:63], v[120:123], a[28:31], v[60:63]\n"
    "buffer_load_dwordx4 v235, %[rs], s40 offen offset:2048 sc1 lds\n"
    "v_pk_max_i16 v203, v203, 0\n"
    "v_mfma_f32_16x16x32_bf16 v[68:71], v[124:127], a[28:31], v[68:71]\n"
    "ds_read_b128 v[120:123], v233 offset:53248\n"
    "v_mfma_f32_16x16x32_bf16 v[64:67], v[124:127], a[24:27], v[64:67]\n"
    "v_cvt_pk_bf16_f32 v204, v20, v21\n"
    "v_cvt_pk_bf16_f32 v205, v22, v23\n"
    "v_cvt_pk_bf16_f32 v206, v28, v29\n"
    "v_mfma_f32_16x16x32_bf16 v[72:75], v[128:131], a[24:27], v[72:75]\n"
    "ds_read_b128 v[124:127], v233 offset:54272\n"
    "v_mfma_f32_16x16x32_bf16 v[76:79], v[128:131], a[28:31], v[76:79]\n"
    "v_cvt_pk_bf16_f32 v207, v30, v31\n"
    "v_pk_max_i16 v204, v204, 0\n"
    "v_mfma_f32_16x16x32_bf16 v[84:87], v[132:135], a[28:31], v[84:87]\n"
    "ds_read_b128 v[128:131], v233 offset:55296\n"
    "v_mfma_f32_16x16x32_bf16 v[80:83], v[132:135], a[24:27], v[80:83]\n"
    "v_pk_max_i16 v205, v205, 0\n"
    "v_pk_max_i16 v206, v206, 0\n"
    "v_pk_max_i16 v207, v207, 0\n"
    "s_waitcnt lgkmcnt(3)\n"
    "v_mfma_f32_16x16x32_bf16 v[56:59], v[136:139], a[32:35], v[56:59]\n"
    "ds_read_b128 v[132:135], v233 offset:56320\n"
    "v_mfma_f32_16x16x32_bf16 v[60:63], v[136:139], a[36:39], v[60:63]\n"
    "buffer_store_dwordx4 v[196:199], v238, %[rs], s46 offen offset:1024 sc1\n"
    "v_mfma_f32_16x16x32_bf16 v[68:71], v[140:143], a[36:39], v[68:71]\n"
    "ds_read_b128 v[136:139], v233 offset:57344\n"
    "v_mfma_f32_16x16x32_bf16 v[64:67], v[140:143], a[32:35], v[64:67]\n"
    "v_mfma_f32_16x16x32_bf16 v[72:75], v[144:147], a[32:35], v[72:75]\n"
    "ds_read_b128 v[140:143], v233 offset:58368\n"
    "v_mfma_f32_16x16x32_bf16 v[76:79], v[144:147], a[36:39], v[76:79]\n"
    "buffer_load_dword v224, v239, %[rs], s32 offen offset:0 sc1\n"
    "v_mfma_f32_16x16x32_bf16 v[84:87], v[148:151], a[36:39], v[84:87]\n"
    "ds_read_b128 v[144:147], v233 offset:59392\n"
    "v_mfma_f32_16x16x32_bf16 v[80:83], v[148:151], a[32:35], v[80:83]\n"
    "s_waitcnt lgkmcnt(3)\n"
    "v_mfma_f32_16x16x32_bf16 v[56:59], v[120:123], a[40:43], v[56:59]\n"
    "ds_read_b128 v[148:151], v233 offset:60416\n"
    "v_mfma_f32_16x16x32_bf16 v[60:63], v[120:123], a[44:47], v[60:63]\n"
    "v_mfma_f32_16x16x32_bf16 v[68:71], v[124:127], a[44:47], v[68:71]\n"
    "ds_read_b128 v[120:123], v233 offset:61440\n"
    "v_mfma_f32_16x16x32_bf16 v[64:67], v[124:127], a[40:43], v[64:67]\n"
    "v_mfma_f32_16x16x32_bf16 v[72:75], v[128:131], a[40:43], v[72:75]\n"
    "ds_read_b128 v[124:127], v233 offset:62464\n"
    "v_mfma_f32_16x16x32_bf16 v[76:79], v[128:131], a[44:47], v[76:79]\n"
    "v_mfma_f32_16x16x32_bf16 v[84:87], v[132:135], a[44:47], v[84:87]\n"
    "ds_read_b128 v[128:131], v233 offset:63488\n"
    "v_mfma_f32_16x16x32_bf16 v[80:83], v[132:135], a[40:43], v[80:83]\n"
    "s_waitcnt lgkmcnt(3)\n"
    "v_mfma_f32_16x16x32_bf16 v[56:59], v[136:139], a[48:51], v[56:59]\n"
    "ds_read_b128 v[132:135], v233 offset:64512\n"
    "v_mfma_f32_16x16x32_bf16 v[60:63], v[136:139], a[52:55], v[60:63]\n"
    "v_mfma_f32_16x16x32_bf16 v[68:71], v[140:143], a[52:55], v[68:71]\n"
    "ds_read_b128 v[136:139], v234 offset:0\n"
    "v_mfma_f32_16x16x32_bf16 v[64:67], v[140:143], a[48:51], v[64:67]\n"
    "v_mfma_f32_16x16x32_bf16 v[72:75], v[144:147], a[48:51], v[72:75]\n"
    "ds_read_b128 v[140:143], v234 offset:1024\n"
    "v_mfma_f32_16x16x32_bf16 v[76:79], v[144:147], a[52:55], v[76:79]\n"
    "v_mfma_f32_16x16x32_bf16 v[84:87], v[148:151], a[52:55], v[84:87]\n"
    "ds_read_b128 v[144:147], v234 offset:2048\n"
    "v_mfma_f32_16x16x32_bf16 v[80:83], v[148:151], a[48:51], v[80:83]\n"
    "buffer_store_dwordx4 v[200:203], v236, %[rs], s46 offen offset:2048 sc1\n"
    "s_waitcnt lgkmcnt(3)\n"
    "v_mfma_f32_16x16x32_bf16 v[56:59], v[120:123], a[56:59], v[56:59]\n"
    "ds_read_b128 v[148:151], v234 offset:3072\n"
    "v_mfma_f32_16x16x32_bf16 v[60:63], v[120:123], a[60:63], v[60:63]\n"
    "v_mfma_f32_16x16x32_bf16 v[68:71], v[124:127], a[60:63], v[68:71]\n"
    "s_barrier\n"
    "s_cmp_lg_u32 s35, 0\n"
    "s_cbranch_scc1 Lsig4_%=\n"
    "s_mov_b64 s[42:43], exec\n"
    "s_mov_b64 exec, 1\n"
    "buffer_atomic_add v240, v239, %[rs], s32 offen offset:128\n"
    "s_mov_b64 exec, s[42:43]\n"
    "Lsig4_%=:\n"
    "ds_read_b128 v[120:123], v234 offset:4096\n"
    "v_mfma_f32_16x16x32_bf16 v[64:67], v[124:127], a[56:59], v[64:67]\n"
    "s_add_u32 m0, s34, 86016\n"
    "s_add_u32 s40, s25, 73728\n"
    "buffer_load_dwordx4 v235, %[rs], s40 offen offset:0 sc1 lds\n"
    "v_mfma_f32_16x16x32_bf16 v[72:75], v[128:131], a[56:59], v[72:75]\n"
    "ds_read_b128 v[124:127], v234 offset:5120\n"
    "v_mfma_f32_16x16x32_bf16 v[76:79], v[128:131], a[60:63], v[76:79]\n"
    "v_mfma_f32_16x16x32_bf16 v[84:87], v[132:135], a[60:63], v[84:87]\n"
    "ds_read_b128 v[128:131], v234 offset:6144\n"
    "v_mfma_f32_16x16x32_bf16 v[80:83], v[132:135], a[56:59], v[80:83]\n"
    "buffer_load_dwordx4 v235, %[rs], s40 offen offset:1024 sc1 lds\n"
    "s_waitcnt lgkmcnt(3)\n"
    "v_mfma_f32_16x16x32_bf16 v[56:59], v[136:139], a[64:67], v[56:59]\n"
    "ds_read_b128 v[132:135], v234 offset:7168\n"
    "v_mfma_f32_16x16x32_bf16 v[60:63], v[136:139], a[68:71], v[60:63]\n"
    "v_mfma_f32_16x16x32_bf16 v[68:71], v[140:143], a[68:71], v[68:71]\n"
    "ds_read_b128 v[136:139], v234 offset:8192\n"
    "v_mfma_f32_16x16x32_bf16 v[64:67], v[140:143], a[64:67], v[64:67]\n"
    "buffer_load_dwordx4 v235, %[rs], s40 offen offset:2048 sc1 lds\n"
    "v_mfma_f32_16x16x32_bf16 v[72:75], v[144:147], a[64:67], v[72:75]\n"
    "ds_read_b128 v[140:143], v234 offset:9216\n"
    "v_mfma_f32_16x16x32_bf16 v[76:79], v[144:147], a[68:71], v[76:79]\n"
    "v_mfma_f32_16x16x32_bf16 v[84:87], v[148:151], a[68:71], v[84:87]\n"
    "ds_read_b128 v[144:147], v234 offset:10240\n"
    "v_mfma_f32_16x16x32_bf16 v[80:83], v[148:151], a[64:67], v[80:83]\n"
    "s_add_u32 m0, s34, 98304\n"
    "s_add_u32 s40, s25, 86016\n"
    "buffer_load_dwordx4 v235, %[rs], s40 offen offset:0 sc1 lds\n"
    "s_waitcnt lgkmcnt(3)\n"
    "v_mfma_f32_16x16x32_bf16 v[56:59], v[120:123], a[72:75], v[56:59]\n"
    "ds_read_b128 v[148:151], v234 offset:11264\n"
    "v_mfma_f32_16x16x32_bf16 v[60:63], v[120:123], a[76:79], v[60:63]\n"
    "v_mfma_f32_16x16x32_bf16 v[68:71], v[124:127], a[76:79], v[68:71]\n"
    "ds_read_b128 v[120:123], v234 offset:12288\n"
    "v_mfma_f32_16x16x32_bf16 v[64:67], v[124:127], a[72:75], v[64:67]\n"
    "buffer_load_dwordx4 v235, %[rs], s40 offen offset:1024 sc1 lds\n"
    "v_mfma_f32_16x16x32_bf16 v[72:75], v[128:131], a[72:75], v[72:75]\n"
    "ds_read_b128 v[124:127], v234 offset:13312\n"
    "v_mfma_f32_16x16x32_bf16 v[76:79], v[128:131], a[76:79], v[76:79]\n"
    "v_mfma_f32_16x16x32_bf16 v[84:87], v[132:135], a[76:79], v[84:87]\n"
    "ds_read_b128 v[128:131], v234 offset:14336\n"
    "v_mfma_f32_16x16x32_bf16 v[80:83], v[132:135], a[72:75], v[80:83]\n"
    "buffer_store_dwordx4 v[204:207], v238, %[rs], s46 offen offset:2048 sc1\n"
    "s_waitcnt lgkmcnt(3)\n"
    "v_mfma_f32_16x16x32_bf16 v[56:59], v[136:139], a[80:83], v[56:59]\n"
    "ds_read_b128 v[132:135], v234 offset:15360\n"
    "v_mfma_f32_16x16x32_bf16 v[60:63], v[136:139], a[84:87], v[60:63]\n"
    "v_mfma_f32_16x16x32_bf16 v[68:71], v[140:143], a[84:87], v[68:71]\n"
    "ds_read_b128 v[136:139], v234 offset:16384\n"
    "v_mfma_f32_16x16x32_bf16 v[64:67], v[140:143], a[80:83], v[64:67]\n"
    "v_mfma_f32_16x16x32_bf16 v[72:75], v[144:147], a[80:83], v[72:75]\n"
    "ds_read_b128 v[140:143], v234 offset:17408\n"
    "v_mfma_f32_16x16x32_bf16 v[76:79], v[144:147], a[84:87], v[76:79]\n"
    "s_waitcnt vmcnt(7)\n"
    "v_readfirstlane_b32 s45, v224\n"
    "s_mov_b32 s44, 0x10000\n"
    "s_nop 0\n"
    "s_cmp_ge_u32 s45, 4\n"
    "s_cbranch_scc1 Lpe5_%=\n"
    "Lpoll5_%=:\n"
    "buffer_load_dword v224, v239, %[rs], s32 offen offset:0 sc1\n"
    "s_waitcnt vmcnt(0)\n"
    "v_readfirstlane_b32 s45, v224\n"
    "s_sub_u32 s44, s44, 1\n"
    "s_cbranch_scc1 Lpe5_%=\n"
    "s_cmp_lt_u32 s45, 4\n"
    "s_cbranch_scc1 Lpoll5_%=\n"
    "Lpe5_%=:\n"
    "s_add_u32 s41, s28, 0\n"
    "buffer_load_dwordx4 a[144:147], v236, %[rs], s41 offen offset:0 sc1\n"
    "v_mfma_f32_16x16x32_bf16 v[84:87], v[148:151], a[84:87], v[84:87]\n"
    "ds_read_b128 v[144:147], v234 offset:18432\n"
    "v_mfma_f32_16x16x32_bf16 v[80:83], v[148:151], a[80:83], v[80:83]\n"
    "s_waitcnt lgkmcnt(3)\n"
    "v_mfma_f32_16x16x32_bf16 v[56:59], v[120:123], a[88:91], v[56:59]\n"
    "ds_read_b128 v[148:151], v234 offset:19456\n"
    "v_mfma_f32_16x16x32_bf16 v[60:63], v[120:123], a[92:95], v[60:63]\n"
    "buffer_load_dwordx4 v235, %[rs], s40 offen offset:2048 sc1 lds\n"
    "v_mfma_f32_16x16x32_bf16 v[68:71], v[124:127], a[92:95], v[68:71]\n"
    "ds_read_b128 v[120:123], v234 offset:20480\n"
    "v_mfma_f32_16x16x32_bf16 v[64:67], v[124:127], a[88:91], v[64:67]\n"
    "v_mfma_f32_16x16x32_bf16 v[72:75], v[128:131], a[88:91], v[72:75]\n"
    "ds_read_b128 v[124:127], v234 offset:21504\n"
    "v_mfma_f32_16x16x32_bf16 v[76:79], v[128:131], a[92:95], v[76:79]\n"
    "buffer_load_dwordx4 a[148:151], v238, %[rs], s41 offen offset:0 sc1\n"
    "v_mfma_f32_16x16x32_bf16 v[84:87], v[132:135], a[92:95], v[84:87]\n"
    "ds_read_b128 v[128:131], v234 offset:22528\n"
    "v_mfma_f32_16x16x32_bf16 v[80:83], v[132:135], a[88:91], v[80:83]\n"
    "s_waitcnt lgkmcnt(3)\n"
    "v_mfma_f32_16x16x32_bf16 v[56:59], v[136:139], a[96:99], v[56:59]\n"
    "ds_read_b128 v[132:135], v234 offset:23552\n"
    "v_mfma_f32_16x16x32_bf16 v[60:63], v[136:139], a[100:103], v[60:63]\n"
    "s_add_u32 m0, s34, 110592\n"
    "s_add_u32 s40, s25, 98304\n"
    "buffer_load_dwordx4 v235, %[rs], s40 offen offset:0 sc1 lds\n"
    "v_mfma_f32_16x16x32_bf16 v[68:71], v[140:143], a[100:103], v[68:71]\n"
    "ds_read_b128 v[136:139], v234 offset:24576\n"
    "v_mfma_f32_16x16x32_bf16 v[64:67], v[140:143], a[96:99], v[64:67]\n"
    "v_mfma_f32_16x16x32_bf16 v[72:75], v[144:147], a[96:99], v[72:75]\n"
    "ds_read_b128 v[140:143], v234 offset:25600\n"
    "v_mfma_f32_16x16x32_bf16 v[76:79], v[144:147], a[100:103], v[76:79]\n"
    "buffer_load_dwordx4 v235, %[rs], s40 offen offset:1024 sc1 lds\n"
    "v_mfma_f32_16x16x32_bf16 v[84:87], v[148:151], a[100:103], v[84:87]\n"
    "ds_read_b128 v[144:147], v234 offset:26624\n"
    "v_mfma_f32_16x16x32_bf16 v[80:83], v[148:151], a[96:99], v[80:83]\n"
    "s_waitcnt lgkmcnt(3)\n"
    "v_mfma_f32_16x16x32_bf16 v[56:59], v[120:123], a[104:107], v[56:59]\n"
    "ds_read_b128 v[148:151], v234 offset:27648\n"
    "v_mfma_f32_16x16x32_bf16 v[60:63], v[120:123], a[108:111], v[60:63]\n"
    "buffer_load_dwordx4 v235, %[rs], s40 offen offset:2048 sc1 lds\n"
    "v_mfma_f32_16x16x32_bf16 v[68:71], v[124:127], a[108:111], v[68:71]\n"
    "ds_read_b128 v[120:123], v232 offset:0\n"
    "v_mfma_f32_16x16x32_bf16 v[64:67], v[124:127], a[104:107], v[64:67]\n"
    "v_mfma_f32_16x16x32_bf16 v[72:75], v[128:131], a[104:107], v[72:75]\n"
    "ds_read_b128 v[124:127], v232 offset:1024\n"
    "v_mfma_f32_16x16x32_bf16 v[76:79], v[128:131], a[108:111], v[76:79]\n"
    "buffer_load_dwordx4 a[152:155], v236, %[rs], s41 offen offset:1024 sc1\n"
    "v_mfma_f32_16x16x32_bf16 v[84:87], v[132:135], a[108:111], v[84:87]\n"
    "ds_read_b128 v[128:131], v232 offset:2048\n"
    "v_mfma_f32_16x16x32_bf16 v[80:83], v[132:135], a[104:107], v[80:83]\n"
    "s_waitcnt lgkmcnt(3)\n"
    "v_mfma_f32_16x16x32_bf16 v[56:59], v[136:139], a[112:115], v[56:59]\n"
    "ds_read_b128 v[132:135], v232 offset:3072\n"
    "v_mfma_f32_16x16x32_bf16 v[60:63], v[136:139], a[116:119], v[60:63]\n"
    "v_mfma_f32_16x16x32_bf16 v[68:71], v[140:143], a[116:119], v[68:71]\n"
    "ds_read_b128 v[136:139], v232 offset:4096\n"
    "v_mfma_f32_16x16x32_bf16 v[64:67], v[140:143], a[112:115], v[64:67]\n"
    "buffer_load_dwordx4 a[156:159], v238, %[rs], s41 offen offset:1024 sc1\n"
    "v_mfma_f32_16x16x32_bf16 v[72:75], v[144:147], a[112:115], v[72:75]\n"
    "ds_read_b128 v[140:143], v232 offset:5120\n"
    "v_mfma_f32_16x16x32_bf16 v[76:79], v[144:147], a[116:119], v[76:79]\n"
    "v_mfma_f32_16x16x32_bf16 v[84:87], v[148:151], a[116:119], v[84:87]\n"
    "ds_read_b128 v[144:147], v232 offset:6144\n"
    "v_mfma_f32_16x16x32_bf16 v[80:83], v[148:151], a[112:115], v[80:83]\n"
    "s_waitcnt lgkmcnt(3)\n"
    "v_mfma_f32_16x16x32_bf16 v[56:59], v[120:123], a[120:123], v[56:59]\n"
    "ds_read_b128 v[148:151], v232 offset:7168\n"
    "v_mfma_f32_16x16x32_bf16 v[60:63], v[120:123], a[124:127], v[60:63]\n"
    "buffer_load_dwordx4 a[160:163], v236, %[rs], s41 offen offset:2048 sc1\n"
    "v_mfma_f32_16x16x32_bf16 v[68:71], v[124:127], a[124:127], v[68:71]\n"
    "ds_read_b128 v[120:123], v232 offset:8192\n"
    "v_mfma_f32_16x16x32_bf16 v[64:67], v[124:127], a[120:123], v[64:67]\n"
    "v_mfma_f32_16x16x32_bf16 v[72:75], v[128:131], a[120:123], v[72:75]\n"
    "ds_read_b128 v[124:127], v232 offset:9216\n"
    "v_mfma_f32_16x16x32_bf16 v[76:79], v[128:131], a[124:127], v[76:79]\n"
    "v_mfma_f32_16x16x32_bf16 v[84:87], v[132:135], a[124:127], v[84:87]\n"
    "ds_read_b128 v[128:131], v232 offset:10240\n"
    "v_mfma_f32_16x16x32_bf16 v[80:83], v[132:135], a[120:123], v[80:83]\n"
    "buffer_load_dwordx4 a[164:167], v238, %[rs], s41 offen offset:2048 sc1\n"
    "s_waitcnt lgkmcnt(3)\n"
    "v_mfma_f32_16x16x32_bf16 v[56:59], v[136:139], a[128:131], v[56:59]\n"
    "ds_read_b128 v[132:135], v232 offset:11264\n"
    "v_mfma_f32_16x16x32_bf16 v[60:63], v[136:139], a[132:135], v[60:63]\n"
    "v_mfma_f32_16x16x32_bf16 v[68:71], v[140:143], a[132:135], v[68:71]\n"
    "s_barrier\n"
    "ds_read_b128 v[136:139], v232 offset:12288\n"
    "v_mfma_f32_16x16x32_bf16 v[64:67], v[140:143], a[128:131], v[64:67]\n"
    "s_add_u32 m0, s34, 122880\n"
    "s_add_u32 s40, s25, 110592\n"
    "buffer_load_dwordx4 v235, %[rs], s40 offen offset:0 sc1 lds\n"
    "v_mfma_f32_16x16x32_bf16 v[72:75], v[144:147], a[128:131], v[72:75]\n"
    "ds_read_b128 v[140:143], v232 offset:13312\n"
    "v_mfma_f32_16x16x32_bf16 v[76:79], v[144:147], a[132:135], v[76:79]\n"
    "v_mfma_f32_16x16x32_bf16 v[84:87], v[148:151], a[132:135], v[84:87]\n"
    "ds_read_b128 v[144:147], v232 offset:14336\n"
    "v_mfma_f32_16x16x32_bf16 v[80:83], v[148:151], a[128:131], v[80:83]\n"
    "s_add_u32 s41, s29, 0\n"
    "buffer_load_dwordx4 a[168:171], v236, %[rs], s41 offen offset:0 sc1\n"
    "s_waitcnt lgkmcnt(3)\n"
    "v_mfma_f32_16x16x32_bf16 v[56:59], v[120:123], a[136:139], v[56:59]\n"
    "ds_read_b128 v[148:151], v232 offset:15360\n"
    "v_mfma_f32_16x16x32_bf16 v[60:63], v[120:123], a[140:143], v[60:63]\n"
    "v_mfma_f32_16x16x32_bf16 v[68:71], v[124:127], a[140:143], v[68:71]\n"
    "ds_read_b128 v[120:123], v232 offset:16384\n"
    "v_mfma_f32_16x16x32_bf16 v[64:67], v[124:127], a[136:139], v[64:67]\n"
    "buffer_load_dwordx4 v235, %[rs], s40 offen offset:1024 sc1 lds\n"
    "v_mfma_f32_16x16x32_bf16 v[72:75], v[128:131], a[136:139], v[72:75]\n"
    "ds_read_b128 v[124:127], v232 offset:17408\n"
    "v_mfma_f32_16x16x32_bf16 v[76:79], v[128:131], a[140:143], v[76:79]\n"
    "v_mfma_f32_16x16x32_bf16 v[84:87], v[132:135], a[140:143], v[84:87]\n"
    "ds_read_b128 v[128:131], v232 offset:18432\n"
    "v_mfma_f32_16x16x32_bf16 v[80:83], v[132:135], a[136:139], v[80:83]\n"
    "buffer_load_dwordx4 a[172:175], v238, %[rs], s41 offen offset:0 sc1\n"
    "s_waitcnt lgkmcnt(3)\n"
    "v_mfma_f32_16x16x32_bf16 v[0:3], v[136:139], v[152:155], 0\n"
    "ds_read_b128 v[132:135], v232 offset:19456\n"
    "v_mfma_f32_16x16x32_bf16 v[4:7], v[136:139], v[156:159], 0\n"
    "v_cvt_pk_bf16_f32 v208, v56, v57\n"
    "v_cvt_pk_bf16_f32 v209, v58, v59\n"
    "v_mfma_f32_16x16x32_bf16 v[12:15], v[140:143], v[156:159], 0\n"
    "ds_read_b128 v[136:139], v232 offset:20480\n"
    "v_mfma_f32_16x16x32_bf16 v[8:11], v[140:143], v[152:155], 0\n"
    "buffer_load_dwordx4 v235, %[rs], s40 offen offset:2048 sc1 lds\n"
    "v_cvt_pk_bf16_f32 v210, v64, v65\n"
    "v_cvt_pk_bf16_f32 v211, v66, v67\n"
    "v_mfma_f32_16x16x32_bf16 v[16:19], v[144:147], v[152:155], 0\n"
    "ds_read_b128 v[140:143], v232 offset:21504\n"
    "v_mfma_f32_16x16x32_bf16 v[20:23], v[144:147], v[156:159], 0\n"
    "v_pk_max_i16 v208, v208, 0\n"
    "v_pk_max_i16 v209, v209, 0\n"
    "v_pk_max_i16 v210, v210, 0\n"
    "v_mfma_f32_16x16x32_bf16 v[28:31], v[148:151], v[156:159], 0\n"
    "ds_read_b128 v[144:147], v232 offset:22528\n"
    "v_mfma_f32_16x16x32_bf16 v[24:27], v[148:151], v[152:155], 0\n"
    "buffer_load_dwordx4 a[176:179], v236, %[rs], s41 offen offset:1024 sc1\n"
    "v_pk_max_i16 v211, v211, 0\n"
    "s_waitcnt lgkmcnt(3)\n"
    "v_mfma_f32_16x16x32_bf16 v[32:35], v[120:123], v[152:155], 0\n"
    "ds_read_b128 v[148:151], v232 offset:23552\n"
    "v_mfma_f32_16x16x32_bf16 v[36:39], v[120:123], v[156:159], 0\n"
    "v_cvt_pk_bf16_f32 v212, v60, v61\n"
    "v_cvt_pk_bf16_f32 v213, v62, v63\n"
    "v_mfma_f32_16x16x32_bf16 v[44:47], v[124:127], v[156:159], 0\n"
    "ds_read_b128 v[120:123], v232 offset:24576\n"
    "v_mfma_f32_16x16x32_bf16 v[40:43], v[124:127], v[152:155], 0\n"
    "v_cvt_pk_bf16_f32 v214, v68, v69\n"
    "v_cvt_pk_bf16_f32 v215, v70, v71\n"
    "v_pk_max_i16 v212, v212, 0\n"
    "v_mfma_f32_16x16x32_bf16 v[48:51], v[128:131], v[152:155], 0\n"
    "ds_read_b128 v[124:127], v232 offset:25600\n"
    "v_mfma_f32_16x16x32_bf16 v[52:55], v[128:131], v[156:159], 0\n"
    "buffer_store_dwordx4 v[208:211], v236, %[rs], s46 offen offset:3072 sc1\n"
    "v_pk_max_i16 v213, v213, 0\n"
    "v_pk_max_i16 v214, v214, 0\n"
    "v_mfma_f32_16x16x32_bf16 v[4:7], v[132:135], v[164:167], v[4:7]\n"
    "ds_read_b128 v[128:131], v232 offset:26624\n"
    "v_mfma_f32_16x16x32_bf16 v[0:3], v[132:135], v[160:163], v[0:3]\n"
    "v_pk_max_i16 v215, v215, 0\n"
    "v_cvt_pk_bf16_f32 v216, v72, v73\n"
    "s_waitcnt lgkmcnt(3)\n"
    "v_mfma_f32_16x16x32_bf16 v[8:11], v[136:139], v[160:163], v[8:11]\n"
    "ds_read_b128 v[132:135], v232 offset:27648\n"
    "v_mfma_f32_16x16x32_bf16 v[12:15], v[136:139], v[164:167], v[12:15]\n"
    "buffer_load_dwordx4 a[180:183], v238, %[rs], s41 offen offset:1024 sc1\n"
    "v_cvt_pk_bf16_f32 v217, v74, v75\n"
    "v_mfma_f32_16x16x32_bf16 v[20:23], v[140:143], v[164:167], v[20:23]\n"
    "ds_read_b128 v[136:139], v232 offset:28672\n"
    "v_mfma_f32_16x16x32_bf16 v[16:19], v[140:143], v[160:163], v[16:19]\n"
    "v_cvt_pk_bf16_f32 v218, v80, v81\n"
    "v_cvt_pk_bf16_f32 v219, v82, v83\n"
    "v_pk_max_i16 v216, v216, 0\n"
    "v_mfma_f32_16x16x32_bf16 v[24:27], v[144:147], v[160:163], v[24:27]\n"
    "ds_read_b128 v[140:143], v232 offset:29696\n"
    "v_mfma_f32_16x16x32_bf16 v[28:31], v[144:147], v[164:167], v[28:31]\n"
    "s_add_u32 m0, s34, 135168\n"
    "s_add_u32 s40, s25, 122880\n"
    "buffer_load_dwordx4 v235, %[rs], s40 offen offset:0 sc1 lds\n"
    "v_mfma_f32_16x16x32_bf16 v[36:39], v[148:151], v[164:167], v[36:39]\n"
    "ds_read_b128 v[144:147], v232 offset:30720\n"
    "v_mfma_f32_16x16x32_bf16 v[32:35], v[148:151], v[160:163], v[32:35]\n"
    "v_pk_max_i16 v217, v217, 0\n"
    "v_pk_max_i16 v218, v218, 0\n"
    "s_waitcnt lgkmcnt(3)\n"
    "v_mfma_f32_16x16x32_bf16 v[40:43], v[120:123], v[160:163], v[40:43]\n"
    "ds_read_b128 v[148:151], v232 offset:31744\n"
    "v_mfma_f32_16x16x32_bf16 v[44:47], v[120:123], v[164:167], v[44:47]\n"
    "buffer_load_dwordx4 a[184:187], v236, %[rs], s41 offen offset:2048 sc1\n"
    "v_pk_max_i16 v219, v219, 0\n"
    "v_mfma_f32_16x16x32_bf16 v[52:55], v[124:127], v[164:167], v[52:55]\n"
    "ds_read_b128 v[120:123], v232 offset:32768\n"
    "v_mfma_f32_16x16x32_bf16 v[48:51], v[124:127], v[160:163], v[48:51]\n"
    "v_cvt_pk_bf16_f32 v220, v76, v77\n"
    "v_cvt_pk_bf16_f32 v221, v78, v79\n"
    "v_cvt_pk_bf16_f32 v222, v84, v85\n"
    "v_mfma_f32_16x16x32_bf16 v[0:3], v[128:131], v[168:171], v[0:3]\n"
    "ds_read_b128 v[124:127], v232 offset:33792\n"
    "v_mfma_f32_16x16x32_bf16 v[4:7], v[128:131], v[172:175], v[4:7]\n"
    "buffer_load_dwordx4 v235, %[rs], s40 offen offset:1024 sc1 lds\n"
    "v_cvt_pk_bf16_f32 v223, v86, v87\n"
    "v_pk_max_i16 v220, v220, 0\n"
    "v_mfma_f32_16x16x32_bf16 v[12:15], v[132:135], v[172:175], v[12:15]\n"
    "ds_read_b128 v[128:131], v232 offset:34816\n"
    "v_mfma_f32_16x16x32_bf16 v[8:11], v[132:135], v[168:171], v[8:11]\n"
    "v_pk_max_i16 v221, v221, 0\n"
    "v_pk_max_i16 v222, v222, 0\n"
    "s_waitcnt lgkmcnt(3)\n"
    "v_mfma_f32_16x16x32_bf16 v[16:19], v[136:139], v[168:171], v[16:19]\n"
    "ds_read_b128 v[132:135], v232 offset:35840\n"
    "v_mfma_f32_16x16x32_bf16 v[20:23], v[136:139], v[172:175], v[20:23]\n"
    "buffer_load_dwordx4 a[188:191], v238, %[rs], s41 offen offset:2048 sc1\n"
    "v_pk_max_i16 v223, v223, 0\n"
    "v_mfma_f32_16x16x32_bf16 v[28:31], v[140:143], v[172:175], v[28:31]\n"
    "ds_read_b128 v[136:139], v232 offset:36864\n"
    "v_mfma_f32_16x16x32_bf16 v[24:27], v[140:143], v[168:171], v[24:27]\n"
    "v_mfma_f32_16x16x32_bf16 v[32:35], v[144:147], v[168:171], v[32:35]\n"
    "ds_read_b128 v[140:143], v232 offset:37888\n"
    "v_mfma_f32_16x16x32_bf16 v[36:39], v[144:147], v[172:175], v[36:39]\n"
    "buffer_load_dword v224, v239, %[rs], s32 offen offset:128 sc1\n"
    "v_mfma_f32_16x16x32_bf16 v[44:47], v[148:151], v[172:175], v[44:47]\n"
    "ds_read_b128 v[144:147], v232 offset:38912\n"
    "v_mfma_f32_16x16x32_bf16 v[40:43], v[148:151], v[168:171], v[40:43]\n"
    "s_waitcnt lgkmcnt(3)\n"
    "v_mfma_f32_16x16x32_bf16 v[48:51], v[120:123], v[168:171], v[48:51]\n"
    "ds_read_b128 v[148:151], v232 offset:39936\n"
    "v_mfma_f32_16x16x32_bf16 v[52:55], v[120:123], v[172:175], v[52:55]\n"
    "v_mfma_f32_16x16x32_bf16 v[4:7], v[124:127], v[180:183], v[4:7]\n"
    "ds_read_b128 v[120:123], v232 offset:40960\n"
    "v_mfma_f32_16x16x32_bf16 v[0:3], v[124:127], v[176:179], v[0:3]\n"
    "buffer_store_dwordx4 v[212:215], v238, %[rs], s46 offen offset:3072 sc1\n"
    "v_mfma_f32_16x16x32_bf16 v[8:11], v[128:131], v[176:179], v[8:11]\n"
    "ds_read_b128 v[124:127], v232 offset:41984\n"
    "v_mfma_f32_16x16x32_bf16 v[12:15], v[128:131], v[180:183], v[12:15]\n"
    "v_mfma_f32_16x16x32_bf16 v[20:23], v[132:135], v[180:183], v[20:23]\n"
    "ds_read_b128 v[128:131], v232 offset:43008\n"
    "v_mfma_f32_16x16x32_bf16 v[16:19], v[132:135], v[176:179], v[16:19]\n"
    "buffer_load_dwordx4 v235, %[rs], s40 offen offset:2048 sc1 lds\n"
    "s_waitcnt lgkmcnt(3)\n"
    "v_mfma_f32_16x16x32_bf16 v[24:27], v[136:139], v[176:179], v[24:27]\n"
    "ds_read_b128 v[132:135], v232 offset:44032\n"
    "v_mfma_f32_16x16x32_bf16 v[28:31], v[136:139], v[180:183], v[28:31]\n"
    "v_mfma_f32_16x16x32_bf16 v[36:39], v[140:143], v[180:183], v[36:39]\n"
    "ds_read_b128 v[136:139], v232 offset:45056\n"
    "v_mfma_f32_16x16x32_bf16 v[32:35], v[140:143], v[176:179], v[32:35]\n"
    "s_add_u32 m0, s34, 147456\n"
    "s_add_u32 s40, s25, 135168\n"
    "buffer_load_dwordx4 v235, %[rs], s40 offen offset:0 sc1 lds\n"
    "v_mfma_f32_16x16x32_bf16 v[40:43], v[144:147], v[176:179], v[40:43]\n"
    "ds_read_b128 v[140:143], v232 offset:46080\n"
    "v_mfma_f32_16x16x32_bf16 v[44:47], v[144:147], v[180:183], v[44:47]\n"
    "v_mfma_f32_16x16x32_bf16 v[52:55], v[148:151], v[180:183], v[52:55]\n"
    "ds_read_b128 v[144:147], v232 offset:47104\n"
    "v_mfma_f32_16x16x32_bf16 v[48:51], v[148:151], v[176:179], v[48:51]\n"
    "buffer_load_dwordx4 v235, %[rs], s40 offen offset:1024 sc1 lds\n"
    "s_waitcnt lgkmcnt(3)\n"
    "v_mfma_f32_16x16x32_bf16 v[0:3], v[120:123], v[184:187], v[0:3]\n"
    "ds_read_b128 v[148:151], v232 offset:48128\n"
    "v_mfma_f32_16x16x32_bf16 v[4:7], v[120:123], v[188:191], v[4:7]\n"
    "v_mfma_f32_16x16x32_bf16 v[12:15], v[124:127], v[188:191], v[12:15]\n"
    "s_waitcnt vmcnt(27)\n"
    "s_barrier\n"
    "s_cmp_lg_u32 s35, 0\n"
    "s_cbranch_scc1 Lsig6_%=\n"
    "s_mov_b64 s[42:43], exec\n"
    "s_mov_b64 exec, 1\n"
    "buffer_atomic_add v240, v239, %[rs], s32 offen offset:256\n"
    "s_mov_b64 exec, s[42:43]\n"
    "Lsig6_%=:\n"
    "ds_read_b128 v[120:123], v232 offset:49152\n"
    "v_mfma_f32_16x16x32_bf16 v[8:11], v[124:127], v[184:187], v[8:11]\n"
    "buffer_load_dwordx4 v235, %[rs], s40 offen offset:2048 sc1 lds\n"
    "v_mfma_f32_16x16x32_bf16 v[16:19], v[128:131], v[184:187], v[16:19]\n"
    "ds_read_b128 v[124:127], v232 offset:50176\n"
    "v_mfma_f32_16x16x32_bf16 v[20:23], v[128:131], v[188:191], v[20:23]\n"
    "v_mfma_f32_16x16x32_bf16 v[28:31], v[132:135], v[188:191], v[28:31]\n"
    "ds_read_b128 v[128:131], v232 offset:51200\n"
    "v_mfma_f32_16x16x32_bf16 v[24:27], v[132:135], v[184:187], v[24:27]\n"
    "s_add_u32 s41, s30, 0\n"
    "buffer_load_dwordx4 a[192:195], v236, %[rs], s41 offen offset:0 sc1\n"
    "s_waitcnt lgkmcnt(3)\n"
    "v_mfma_f32_16x16x32_bf16 v[32:35], v[136:139], v[184:187], v[32:35]\n"
    "ds_read_b128 v[132:135], v232 offset:52224\n"
    "v_mfma_f32_16x16x32_bf16 v[36:39], v[136:139], v[188:191], v[36:39]\n"
    "v_mfma_f32_16x16x32_bf16 v[44:47], v[140:143], v[188:191], v[44:47]\n"
    "ds_read_b128 v[136:139], v232 offset:53248\n"
    "v_mfma_f32_16x16x32_bf16 v[40:43], v[140:143], v[184:187], v[40:43]\n"
    "s_add_u32 s46, s27, 8192\n"
    "buffer_store_dwordx4 v[216:219], v236, %[rs], s46 offen offset:0 sc1\n"
    "v_mfma_f32_16x16x32_bf16 v[48:51], v[144:147], v[184:187], v[48:51]\n"
    "ds_read_b128 v[140:143], v232 offset:54272\n"
    "v_mfma_f32_16x16x32_bf16 v[52:55], v[144:147], v[188:191], v[52:55]\n"
    "v_mfma_f32_16x16x32_bf16 v[4:7], v[148:151], v[196:199], v[4:7]\n"
    "ds_read_b128 v[144:147], v232 offset:55296\n"
    "v_mfma_f32_16x16x32_bf16 v[0:3], v[148:151], v[192:195], v[0:3]\n"
    "s_waitcnt lgkmcnt(3)\n"
    "v_mfma_f32_16x16x32_bf16 v[8:11], v[120:123], v[192:195], v[8:11]\n"
    "ds_read_b128 v[148:151], v232 offset:56320\n"
    "v_mfma_f32_16x16x32_bf16 v[12:15], v[120:123], v[196:199], v[12:15]\n"
    "buffer_load_dwordx4 a[196:199], v238, %[rs], s41 offen offset:0 sc1\n"
    "v_mfma_f32_16x16x32_bf16 v[20:23], v[124:127], v[196:199], v[20:23]\n"
    "ds_read_b128 v[120:123], v232 offset:57344\n"
    "v_mfma_f32_16x16x32_bf16 v[16:19], v[124:127], v[192:195], v[16:19]\n"
    "v_mfma_f32_16x16x32_bf16 v[24:27], v[128:131], v[192:195], v[24:27]\n"
    "ds_read_b128 v[124:127], v232 offset:58368\n"
    "v_mfma_f32_16x16x32_bf16 v[28:31], v[128:131], v[196:199], v[28:31]\n"
    "s_add_u32 m0, s34, 0\n"
    "s_add_u32 s40, s25, 147456\n"
    "buffer_load_dwordx4 v235, %[rs], s40 offen offset:0 sc1 lds\n"
    "v_mfma_f32_16x16x32_bf16 v[36:39], v[132:135], v[196:199], v[36:39]\n"
    "ds_read_b128 v[128:131], v232 offset:59392\n"
    "v_mfma_f32_16x16x32_bf16 v[32:35], v[132:135], v[192:195], v[32:35]\n"
    "s_waitcnt lgkmcnt(3)\n"
    "v_mfma_f32_16x16x32_bf16 v[40:43], v[136:139], v[192:195], v[40:43]\n"
    "ds_read_b128 v[132:135], v232 offset:60416\n"
    "v_mfma_f32_16x16x32_bf16 v[44:47], v[136:139], v[196:199], v[44:47]\n"
    "buffer_load_dwordx4 a[200:203], v236, %[rs], s41 offen offset:1024 sc1\n"
    "v_mfma_f32_16x16x32_bf16 v[52:55], v[140:143], v[196:199], v[52:55]\n"
    "ds_read_b128 v[136:139], v232 offset:61440\n"
    "v_mfma_f32_16x16x32_bf16 v[48:51], v[140:143], v[192:195], v[48:51]\n"
    "v_mfma_f32_16x16x32_bf16 v[0:3], v[144:147], v[200:203], v[0:3]\n"
    "ds_read_b128 v[140:143], v232 offset:62464\n"
    "v_mfma_f32_16x16x32_bf16 v[4:7], v[144:147], v[204:207], v[4:7]\n"
    "s_waitcnt vmcnt(10)\n"
    "v_readfirstlane_b32 s45, v224\n"
    "s_mov_b32 s44, 0x10000\n"
    "s_nop 0\n"
    "s_cmp_ge_u32 s45, 4\n"
    "s_cbranch_scc1 Lpe7_%=\n"
    "Lpoll7_%=:\n"
    "buffer_load_dword v224, v239, %[rs], s32 offen offset:128 sc1\n"
    "s_waitcnt vmcnt(0)\n"
    "v_readfirstlane_b32 s45, v224\n"
    "s_sub_u32 s44, s44, 1\n"
    "s_cbranch_scc1 Lpe7_%=\n"
    "s_cmp_lt_u32 s45, 4\n"
    "s_cbranch_scc1 Lpoll7_%=\n"
    "Lpe7_%=:\n"
    "buffer_load_dwordx4 v235, %[rs], s40 offen offset:1024 sc1 lds\n"
    "v_mfma_f32_16x16x32_bf16 v[12:15], v[148:151], v[204:207], v[12:15]\n"
    "ds_read_b128 v[144:147], v232 offset:63488\n"
    "v_mfma_f32_16x16x32_bf16 v[8:11], v[148:151], v[200:203], v[8:11]\n"
    "s_waitcnt lgkmcnt(3)\n"
    "v_mfma_f32_16x16x32_bf16 v[16:19], v[120:123], v[200:203], v[16:19]\n"
    "ds_read_b128 v[148:151], v232 offset:64512\n"
    "v_mfma_f32_16x16x32_bf16 v[20:23], v[120:123], v[204:207], v[20:23]\n"
    "buffer_store_dwordx4 v[220:223], v238, %[rs], s46 offen offset:0 sc1\n"
    "v_mfma_f32_16x16x32_bf16 v[28:31], v[124:127], v[204:207], v[28:31]\n"
    "ds_read_b128 v[120:123], v233 offset:0\n"
    "v_mfma_f32_16x16x32_bf16 v[24:27], v[124:127], v[200:203], v[24:27]\n"
    "v_mfma_f32_16x16x32_bf16 v[32:35], v[128:131], v[200:203], v[32:35]\n"
    "ds_read_b128 v[124:127], v233 offset:1024\n"
    "v_mfma_f32_16x16x32_bf16 v[36:39], v[128:131], v[204:207], v[36:39]\n"
    "buffer_load_dwordx4 a[204:207], v238, %[rs], s41 offen offset:1024 sc1\n"
    "v_mfma_f32_16x16x32_bf16 v[44:47], v[132:135], v[204:207], v[44:47]\n"
    "ds_read_b128 v[128:131], v233 offset:2048\n"
    "v_mfma_f32_16x16x32_bf16 v[40:43], v[132:135], v[200:203], v[40:43]\n"
    "s_waitcnt lgkmcnt(3)\n"
    "v_mfma_f32_16x16x32_bf16 v[48:51], v[136:139], v[200:203], v[48:51]\n"
    "ds_read_b128 v[132:135], v233 offset:3072\n"
    "v_mfma_f32_16x16x32_bf16 v[52:55], v[136:139], v[204:207], v[52:55]\n"
    "v_mfma_f32_16x16x32_bf16 v[4:7], v[140:143], v[212:215], v[4:7]\n"
    "ds_read_b128 v[136:139], v233 offset:4096\n"
    "v_mfma_f32_16x16x32_bf16 v[0:3], v[140:143], v[208:211], v[0:3]\n"
    "buffer_load_dwordx4 a[208:211], v236, %[rs], s41 offen offset:2048 sc1\n"
    "v_mfma_f32_16x16x32_bf16 v[8:11], v[144:147], v[208:211], v[8:11]\n"
    "ds_read_b128 v[140:143], v233 offset:5120\n"
    "v_mfma_f32_16x16x32_bf16 v[12:15], v[144:147], v[212:215], v[12:15]\n"
    "v_mfma_f32_16x16x32_bf16 v[20:23], v[148:151], v[212:215], v[20:23]\n"
    "ds_read_b128 v[144:147], v233 offset:6144\n"
    "v_mfma_f32_16x16x32_bf16 v[16:19], v[148:151], v[208:211], v[16:19]\n"
    "buffer_load_dwordx4 v235, %[rs], s40 offen offset:2048 sc1 lds\n"
    "s_waitcnt lgkmcnt(3)\n"
    "v_mfma_f32_16x16x32_bf16 v[24:27], v[120:123], v[208:211], v[24:27]\n"
    "ds_read_b128 v[148:151], v233 offset:7168\n"
    "v_mfma_f32_16x16x32_bf16 v[28:31], v[120:123], v[212:215], v[28:31]\n"
    "v_mfma_f32_16x16x32_bf16 v[36:39], v[124:127], v[212:215], v[36:39]\n"
    "ds_read_b128 v[120:123], v233 offset:8192\n"
    "v_mfma_f32_16x16x32_bf16 v[32:35], v[124:127], v[208:211], v[32:35]\n"
    "buffer_load_dwordx4 a[212:215], v238, %[rs], s41 offen offset:2048 sc1\n"
    "v_mfma_f32_16x16x32_bf16 v[40:43], v[128:131], v[208:211], v[40:43]\n"
    "ds_read_b128 v[124:127], v233 offset:9216\n"
    "v_mfma_f32_16x16x32_bf16 v[44:47], v[128:131], v[212:215], v[44:47]\n"
    "v_mfma_f32_16x16x32_bf16 v[52:55], v[132:135], v[212:215], v[52:55]\n"
    "ds_read_b128 v[128:131], v233 offset:10240\n"
    "v_mfma_f32_16x16x32_bf16 v[48:51], v[132:135], v[208:211], v[48:51]\n"
    "s_add_u32 m0, s34, 12288\n"
    "s_add_u32 s40, s25, 159744\n"
    "buffer_load_dwordx4 v235, %[rs], s40 offen offset:0 sc1 lds\n"
    "s_waitcnt lgkmcnt(3)\n"
    "v_mfma_f32_16x16x32_bf16 v[0:3], v[136:139], v[216:219], v[0:3]\n"
    "ds_read_b128 v[132:135], v233 offset:11264\n"
    "v_mfma_f32_16x16x32_bf16 v[4:7], v[136:139], v[220:223], v[4:7]\n"
    "v_mfma_f32_16x16x32_bf16 v[12:15], v[140:143], v[220:223], v[12:15]\n"
    "ds_read_b128 v[136:139], v233 offset:12288\n"
    "v_mfma_f32_16x16x32_bf16 v[8:11], v[140:143], v[216:219], v[8:11]\n"
    "s_add_u32 s41, s28, 0\n"
    "buffer_load_dwordx4 a[216:219], v236, %[rs], s41 offen offset:3072 sc1\n"
    "v_mfma_f32_16x16x32_bf16 v[16:19], v[144:147], v[216:219], v[16:19]\n"
    "ds_read_b128 v[140:143], v233 offset:13312\n"
    "v_mfma_f32_16x16x32_bf16 v[20:23], v[144:147], v[220:223], v[20:23]\n"
    "v_mfma_f32_16x16x32_bf16 v[28:31], v[148:151], v[220:223], v[28:31]\n"
    "ds_read_b128 v[144:147], v233 offset:14336\n"
    "v_mfma_f32_16x16x32_bf16 v[24:27], v[148:151], v[216:219], v[24:27]\n"
    "buffer_load_dwordx4 v235, %[rs], s40 offen offset:1024 sc1 lds\n"
    "s_waitcnt lgkmcnt(3)\n"
    "v_mfma_f32_16x16x32_bf16 v[32:35], v[120:123], v[216:219], v[32:35]\n"
    "ds_read_b128 v[148:151], v233 offset:15360\n"
    "v_mfma_f32_16x16x32_bf16 v[36:39], v[120:123], v[220:223], v[36:39]\n"
    "v_mfma_f32_16x16x32_bf16 v[44:47], v[124:127], v[220:223], v[44:47]\n"
    "ds_read_b128 v[120:123], v233 offset:16384\n"
    "v_mfma_f32_16x16x32_bf16 v[40:43], v[124:127], v[216:219], v[40:43]\n"
    "buffer_load_dwordx4 a[220:223], v238, %[rs], s41 offen offset:3072 sc1\n"
    "v_mfma_f32_16x16x32_bf16 v[48:51], v[128:131], v[216:219], v[48:51]\n"
    "ds_read_b128 v[124:127], v233 offset:17408\n"
    "v_mfma_f32_16x16x32_bf16 v[52:55], v[128:131], v[220:223], v[52:55]\n"
    "v_mfma_f32_16x16x32_bf16 v[4:7], v[132:135], a[148:151], v[4:7]\n"
    "ds_read_b128 v[128:131], v233 offset:18432\n"
    "v_mfma_f32_16x16x32_bf16 v[0:3], v[132:135], a[144:147], v[0:3]\n"
    "s_waitcnt lgkmcnt(3)\n"
    "v_mfma_f32_16x16x32_bf16 v[8:11], v[136:139], a[144:147], v[8:11]\n"
    "ds_read_b128 v[132:135], v233 offset:19456\n"
    "v_mfma_f32_16x16x32_bf16 v[12:15], v[136:139], a[148:151], v[12:15]\n"
    "s_add_u32 s41, s28, 4096\n"
    "buffer_load_dwordx4 a[224:227], v236, %[rs], s41 offen offset:0 sc1\n"
    "v_mfma_f32_16x16x32_bf16 v[20:23], v[140:143], a[148:151], v[20:23]\n"
    "s_barrier\n"
    "ds_read_b128 v[136:139], v233 offset:20480\n"
    "v_mfma_f32_16x16x32_bf16 v[16:19], v[140:143], a[144:147], v[16:19]\n"
    "v_mfma_f32_16x16x32_bf16 v[24:27], v[144:147], a[144:147], v[24:27]\n"
    "ds_read_b128 v[140:143], v233 offset:21504\n"
    "v_mfma_f32_16x16x32_bf16 v[28:31], v[144:147], a[148:151], v[28:31]\n"
    "buffer_load_dwordx4 v235, %[rs], s40 offen offset:2048 sc1 lds\n"
    "v_mfma_f32_16x16x32_bf16 v[36:39], v[148:151], a[148:151], v[36:39]\n"
    "ds_read_b128 v[144:147], v233 offset:22528\n"
    "v_mfma_f32_16x16x32_bf16 v[32:35], v[148:151], a[144:147], v[32:35]\n"
    "s_waitcnt lgkmcnt(3)\n"
    "v_mfma_f32_16x16x32_bf16 v[40:43], v[120:123], a[144:147], v[40:43]\n"
    "ds_read_b128 v[148:151], v233 offset:23552\n"
    "v_mfma_f32_16x16x32_bf16 v[44:47], v[120:123], a[148:151], v[44:47]\n"
    "buffer_load_dwordx4 a[228:231], v238, %[rs], s41 offen offset:0 sc1\n"
    "v_mfma_f32_16x16x32_bf16 v[52:55], v[124:127], a[148:151], v[52:55]\n"
    "ds_read_b128 v[120:123], v233 offset:24576\n"
    "v_mfma_f32_16x16x32_bf16 v[48:51], v[124:127], a[144:147], v[48:51]\n"
    "v_mfma_f32_16x16x32_bf16 v[0:3], v[128:131], a[152:155], v[0:3]\n"
    "ds_read_b128 v[124:127], v233 offset:25600\n"
    "v_mfma_f32_16x16x32_bf16 v[4:7], v[128:131], a[156:159], v[4:7]\n"
    "s_add_u32 m0, s34, 24576\n"
    "s_add_u32 s40, s25, 172032\n"
    "buffer_load_dwordx4 v235, %[rs], s40 offen offset:0 sc1 lds\n"
    "v_mfma_f32_16x16x32_bf16 v[12:15], v[132:135], a[156:159], v[12:15]\n"
    "ds_read_b128 v[128:131], v233 offset:26624\n"
    "v_mfma_f32_16x16x32_bf16 v[8:11], v[132:135], a[152:155], v[8:11]\n"
    "s_waitcnt lgkmcnt(3)\n"
    "v_mfma_f32_16x16x32_bf16 v[16:19], v[136:139], a[152:155], v[16:19]\n"
    "ds_read_b128 v[132:135], v233 offset:27648\n"
    "v_mfma_f32_16x16x32_bf16 v[20:23], v[136:139], a[156:159], v[20:23]\n"
    "buffer_load_dwordx4 v235, %[rs], s40 offen offset:1024 sc1 lds\n"
    "v_mfma_f32_16x16x32_bf16 v[28:31], v[140:143], a[156:159], v[28:31]\n"
    "ds_read_b128 v[136:139], v233 offset:28672\n"
    "v_mfma_f32_16x16x32_bf16 v[24:27], v[140:143], a[152:155], v[24:27]\n"
    "v_mfma_f32_16x16x32_bf16 v[32:35], v[144:147], a[152:155], v[32:35]\n"
    "ds_read_b128 v[140:143], v233 offset:29696\n"
    "v_mfma_f32_16x16x32_bf16 v[36:39], v[144:147], a[156:159], v[36:39]\n"
    "buffer_load_dwordx4 v235, %[rs], s40 offen offset:2048 sc1 lds\n"
    "v_mfma_f32_16x16x32_bf16 v[44:47], v[148:151], a[156:159], v[44:47]\n"
    "ds_read_b128 v[144:147], v233 offset:30720\n"
    "v_mfma_f32_16x16x32_bf16 v[40:43], v[148:151], a[152:155], v[40:43]\n"
    "s_waitcnt lgkmcnt(3)\n"
    "v_mfma_f32_16x16x32_bf16 v[48:51], v[120:123], a[152:155], v[48:51]\n"
    "ds_read_b128 v[148:151], v233 offset:31744\n"
    "v_mfma_f32_16x16x32_bf16 v[52:55], v[120:123], a[156:159], v[52:55]\n"
    "s_add_u32 s41, s29, 0\n"
    "buffer_load_dwordx4 a[232:235], v236, %[rs], s41 offen offset:3072 sc1\n"
    "v_mfma_f32_16x16x32_bf16 v[4:7], v[124:127], a[164:167], v[4:7]\n"
    "ds_read_b128 v[120:123], v233 offset:32768\n"
    "v_mfma_f32_16x16x32_bf16 v[0:3], v[124:127], a[160:163], v[0:3]\n"
    "v_mfma_f32_16x16x32_bf16 v[8:11], v[128:131], a[160:163], v[8:11]\n"
    "ds_read_b128 v[124:127], v233 offset:33792\n"
    "v_mfma_f32_16x16x32_bf16 v[12:15], v[128:131], a[164:167], v[12:15]\n"
    "v_mfma_f32_16x16x32_bf16 v[20:23], v[132:135], a[164:167], v[20:23]\n"
    "ds_read_b128 v[128:131], v233 offset:34816\n"
    "v_mfma_f32_16x16x32_bf16 v[16:19], v[132:135], a[160:163], v[16:19]\n"
    "buffer_load_dwordx4 a[236:239], v238, %[rs], s41 offen offset:3072 sc1\n"
    "s_waitcnt lgkmcnt(3)\n"
    "v_mfma_f32_16x16x32_bf16 v[24:27], v[136:139], a[160:163], v[24:27]\n"
    "ds_read_b128 v[132:135], v233 offset:35840\n"
    "v_mfma_f32_16x16x32_bf16 v[28:31], v[136:139], a[164:167], v[28:31]\n"
    "v_mfma_f32_16x16x32_bf16 v[36:39], v[140:143], a[164:167], v[36:39]\n"
    "ds_read_b128 v[136:139], v233 offset:36864\n"
    "v_mfma_f32_16x16x32_bf16 v[32:35], v[140:143], a[160:163], v[32:35]\n"
    "s_add_u32 m0, s34, 36864\n"
    "s_add_u32 s40, s25, 184320\n"
    "buffer_load_dwordx4 v235, %[rs], s40 offen offset:0 sc1 lds\n"
    "v_mfma_f32_16x16x32_bf16 v[40:43], v[144:147], a[160:163], v[40:43]\n"
    "ds_read_b128 v[140:143], v233 offset:37888\n"
    "v_mfma_f32_16x16x32_bf16 v[44:47], v[144:147], a[164:167], v[44:47]\n"
    "v_mfma_f32_16x16x32_bf16 v[52:55], v[148:151], a[164:167], v[52:55]\n"
    "ds_read_b128 v[144:147], v233 offset:38912\n"
    "v_mfma_f32_16x16x32_bf16 v[48:51], v[148:151], a[160:163], v[48:51]\n"
    "s_add_u32 s41, s29, 4096\n"
    "buffer_load_dwordx4 a[240:243], v236, %[rs], s41 offen offset:0 sc1\n"
    "s_waitcnt lgkmcnt(3)\n"
    "v_mfma_f32_16x16x32_bf16 v[0:3], v[120:123], a[168:171], v[0:3]\n"
    "ds_read_b128 v[148:151], v233 offset:39936\n"
    "v_mfma_f32_16x16x32_bf16 v[4:7], v[120:123], a[172:175], v[4:7]\n"
    "v_mfma_f32_16x16x32_bf16 v[12:15], v[124:127], a[172:175], v[12:15]\n"
    "ds_read_b128 v[120:123], v233 offset:40960\n"
    "v_mfma_f32_16x16x32_bf16 v[8:11], v[124:127], a[168:171], v[8:11]\n"
    "buffer_load_dwordx4 v235, %[rs], s40 offen offset:1024 sc1 lds\n"
    "v_mfma_f32_16x16x32_bf16 v[16:19], v[128:131], a[168:171], v[16:19]\n"
    "ds_read_b128 v[124:127], v233 offset:41984\n"
    "v_mfma_f32_16x16x32_bf16 v[20:23], v[128:131], a[172:175], v[20:23]\n"
    "v_mfma_f32_16x16x32_bf16 v[28:31], v[132:135], a[172:175], v[28:31]\n"
    "ds_read_b128 v[128:131], v233 offset:43008\n"
    "v_mfma_f32_16x16x32_bf16 v[24:27], v[132:135], a[168:171], v[24:27]\n"
    "buffer_load_dwordx4 a[244:247], v238, %[rs], s41 offen offset:0 sc1\n"
    "s_waitcnt lgkmcnt(3)\n"
    "v_mfma_f32_16x16x32_bf16 v[32:35], v[136:139], a[168:171], v[32:35]\n"
    "ds_read_b128 v[132:135], v233 offset:44032\n"
    "v_mfma_f32_16x16x32_bf16 v[36:39], v[136:139], a[172:175], v[36:39]\n"
    "v_mfma_f32_16x16x32_bf16 v[44:47], v[140:143], a[172:175], v[44:47]\n"
    "ds_read_b128 v[136:139], v233 offset:45056\n"
    "v_mfma_f32_16x16x32_bf16 v[40:43], v[140:143], a[168:171], v[40:43]\n"
    "buffer_load_dword v224, v239, %[rs], s32 offen offset:256 sc1\n"
    "v_mfma_f32_16x16x32_bf16 v[48:51], v[144:147], a[168:171], v[48:51]\n"
    "ds_read_b128 v[140:143], v233 offset:46080\n"
    "v_mfma_f32_16x16x32_bf16 v[52:55], v[144:147], a[172:175], v[52:55]\n"
    "v_mfma_f32_16x16x32_bf16 v[4:7], v[148:151], a[180:183], v[4:7]\n"
    "ds_read_b128 v[144:147], v233 offset:47104\n"
    "v_mfma_f32_16x16x32_bf16 v[0:3], v[148:151], a[176:179], v[0:3]\n"
    "s_waitcnt lgkmcnt(3)\n"
    "v_mfma_f32_16x16x32_bf16 v[8:11], v[120:123], a[176:179], v[8:11]\n"
    "ds_read_b128 v[148:151], v233 offset:48128\n"
    "v_mfma_f32_16x16x32_bf16 v[12:15], v[120:123], a[180:183], v[12:15]\n"
    "s_add_u32 s41, s30, 0\n"
    "buffer_load_dwordx4 a[248:251], v236, %[rs], s41 offen offset:3072 sc1\n"
    "v_mfma_f32_16x16x32_bf16 v[20:23], v[124:127], a[180:183], v[20:23]\n"
    "ds_read_b128 v[120:123], v233 offset:49152\n"
    "v_mfma_f32_16x16x32_bf16 v[16:19], v[124:127], a[176:179], v[16:19]\n"
    "v_mfma_f32_16x16x32_bf16 v[24:27], v[128:131], a[176:179], v[24:27]\n"
    "ds_read_b128 v[124:127], v233 offset:50176\n"
    "v_mfma_f32_16x16x32_bf16 v[28:31], v[128:131], a[180:183], v[28:31]\n"
    "buffer_load_dwordx4 v235, %[rs], s40 offen offset:2048 sc1 lds\n"
    "v_mfma_f32_16x16x32_bf16 v[36:39], v[132:135], a[180:183], v[36:39]\n"
    "ds_read_b128 v[128:131], v233 offset:51200\n"
    "v_mfma_f32_16x16x32_bf16 v[32:35], v[132:135], a[176:179], v[32:35]\n"
    "s_waitcnt lgkmcnt(3)\n"
    "v_mfma_f32_16x16x32_bf16 v[40:43], v[136:139], a[176:179], v[40:43]\n"
    "ds_read_b128 v[132:135], v233 offset:52224\n"
    "v_mfma_f32_16x16x32_bf16 v[44:47], v[136:139], a[180:183], v[44:47]\n"
    "buffer_load_dwordx4 a[252:255], v238, %[rs], s41 offen offset:3072 sc1\n"
    "v_mfma_f32_16x16x32_bf16 v[52:55], v[140:143], a[180:183], v[52:55]\n"
    "ds_read_b128 v[136:139], v233 offset:53248\n"
    "v_mfma_f32_16x16x32_bf16 v[48:51], v[140:143], a[176:179], v[48:51]\n"
    "v_mfma_f32_16x16x32_bf16 v[0:3], v[144:147], a[184:187], v[0:3]\n"
    "ds_read_b128 v[140:143], v233 offset:54272\n"
    "v_mfma_f32_16x16x32_bf16 v[4:7], v[144:147], a[188:191], v[4:7]\n"
    "s_add_u32 m0, s34, 49152\n"
    "s_add_u32 s40, s25, 196608\n"
    "buffer_load_dwordx4 v235, %[rs], s40 offen offset:0 sc1 lds\n"
    "v_mfma_f32_16x16x32_bf16 v[12:15], v[148:151], a[188:191], v[12:15]\n"
    "ds_read_b128 v[144:147], v233 offset:55296\n"
    "v_mfma_f32_16x16x32_bf16 v[8:11], v[148:151], a[184:187], v[8:11]\n"
    "s_waitcnt lgkmcnt(3)\n"
    "v_mfma_f32_16x16x32_bf16 v[16:19], v[120:123], a[184:187], v[16:19]\n"
    "ds_read_b128 v[148:151], v233 offset:56320\n"
    "v_mfma_f32_16x16x32_bf16 v[20:23], v[120:123], a[188:191], v[20:23]\n"
    "s_add_u32 s41, s30, 4096\n"
    "buffer_load_dwordx4 a[0:3], v236, %[rs], s41 offen offset:0 sc1\n"
    "v_mfma_f32_16x16x32_bf16 v[28:31], v[124:127], a[188:191], v[28:31]\n"
    "s_waitcnt vmcnt(26)\n"
    "s_barrier\n"
    "s_cmp_lg_u32 s35, 0\n"
    "s_cbranch_scc1 Lsig8_%=\n"
    "s_mov_b64 s[42:43], exec\n"
    "s_mov_b64 exec, 1\n"
    "buffer_atomic_add v240, v239, %[rs], s32 offen offset:384\n"
    "s_mov_b64 exec, s[42:43]\n"
    "Lsig8_%=:\n"
    "ds_read_b128 v[120:123], v233 offset:57344\n"
    "v_mfma_f32_16x16x32_bf16 v[24:27], v[124:127], a[184:187], v[24:27]\n"
    "v_mfma_f32_16x16x32_bf16 v[32:35], v[128:131], a[184:187], v[32:35]\n"
    "ds_read_b128 v[124:127], v233 offset:58368\n"
    "v_mfma_f32_16x16x32_bf16 v[36:39], v[128:131], a[188:191], v[36:39]\n"
    "buffer_load_dwordx4 v235, %[rs], s40 offen offset:1024 sc1 lds\n"
    "v_mfma_f32_16x16x32_bf16 v[44:47], v[132:135], a[188:191], v[44:47]\n"
    "ds_read_b128 v[128:131], v233 offset:59392\n"
    "v_mfma_f32_16x16x32_bf16 v[40:43], v[132:135], a[184:187], v[40:43]\n"
    "s_waitcnt lgkmcnt(3)\n"
    "v_mfma_f32_16x16x32_bf16 v[48:51], v[136:139], a[184:187], v[48:51]\n"
    "ds_read_b128 v[132:135], v233 offset:60416\n"
    "v_mfma_f32_16x16x32_bf16 v[52:55], v[136:139], a[188:191], v[52:55]\n"
    "buffer_load_dwordx4 a[4:7], v238, %[rs], s41 offen offset:0 sc1\n"
    "v_mfma_f32_16x16x32_bf16 v[4:7], v[140:143], a[196:199], v[4:7]\n"
    "ds_read_b128 v[136:139], v233 offset:61440\n"
    "v_mfma_f32_16x16x32_bf16 v[0:3], v[140:143], a[192:195], v[0:3]\n"
    "v_mfma_f32_16x16x32_bf16 v[8:11], v[144:147], a[192:195], v[8:11]\n"
    "ds_read_b128 v[140:143], v233 offset:62464\n"
    "v_mfma_f32_16x16x32_bf16 v[12:15], v[144:147], a[196:199], v[12:15]\n"
    "v_mfma_f32_16x16x32_bf16 v[20:23], v[148:151], a[196:199], v[20:23]\n"
    "ds_read_b128 v[144:147], v233 offset:63488\n"
    "v_mfma_f32_16x16x32_bf16 v[16:19], v[148:151], a[192:195], v[16:19]\n"
    "buffer_load_dwordx4 v235, %[rs], s40 offen offset:2048 sc1 lds\n"
    "s_waitcnt lgkmcnt(3)\n"
    "v_mfma_f32_16x16x32_bf16 v[24:27], v[120:123], a[192:195], v[24:27]\n"
    "ds_read_b128 v[148:151], v233 offset:64512\n"
    "v_mfma_f32_16x16x32_bf16 v[28:31], v[120:123], a[196:199], v[28:31]\n"
    "v_mfma_f32_16x16x32_bf16 v[36:39], v[124:127], a[196:199], v[36:39]\n"
    "ds_read_b128 v[120:123], v234 offset:0\n"
    "v_mfma_f32_16x16x32_bf16 v[32:35], v[124:127], a[192:195], v[32:35]\n"
    "s_add_u32 m0, s34, 61440\n"
    "s_add_u32 s40, s25, 208896\n"
    "buffer_load_dwordx4 v235, %[rs], s40 offen offset:0 sc1 lds\n"
    "v_mfma_f32_16x16x32_bf16 v[40:43], v[128:131], a[192:195], v[40:43]\n"
    "ds_read_b128 v[124:127], v234 offset:1024\n"
    "v_mfma_f32_16x16x32_bf16 v[44:47], v[128:131], a[196:199], v[44:47]\n"
    "v_mfma_f32_16x16x32_bf16 v[52:55], v[132:135], a[196:199], v[52:55]\n"
    "ds_read_b128 v[128:131], v234 offset:2048\n"
    "v_mfma_f32_16x16x32_bf16 v[48:51], v[132:135], a[192:195], v[48:51]\n"
    "buffer_load_dwordx4 v235, %[rs], s40 offen offset:1024 sc1 lds\n"
    "s_waitcnt vmcnt(30)\n"
    "s_waitcnt lgkmcnt(3)\n"
    "v_mfma_f32_16x16x32_bf16 v[0:3], v[136:139], a[200:203], v[0:3]\n"
    "ds_read_b128 v[132:135], v234 offset:3072\n"
    "v_mfma_f32_16x16x32_bf16 v[4:7], v[136:139], a[204:207], v[4:7]\n"
    "v_mfma_f32_16x16x32_bf16 v[12:15], v[140:143], a[204:207], v[12:15]\n"
    "ds_read_b128 v[136:139], v234 offset:4096\n"
    "v_mfma_f32_16x16x32_bf16 v[8:11], v[140:143], a[200:203], v[8:11]\n"
    "s_waitcnt vmcnt(10)\n"
    "v_readfirstlane_b32 s45, v224\n"
    "s_mov_b32 s44, 0x10000\n"
    "s_nop 0\n"
    "s_cmp_ge_u32 s45, 4\n"
    "s_cbranch_scc1 Lpe9_%=\n"
    "Lpoll9_%=:\n"
    "buffer_load_dword v224, v239, %[rs], s32 offen offset:256 sc1\n"
    "s_waitcnt vmcnt(0)\n"
    "v_readfirstlane_b32 s45, v224\n"
    "s_sub_u32 s44, s44, 1\n"
    "s_cbranch_scc1 Lpe9_%=\n"
    "s_cmp_lt_u32 s45, 4\n"
    "s_cbranch_scc1 Lpoll9_%=\n"
    "Lpe9_%=:\n"
    "s_add_u32 s41, s28, 4096\n"
    "buffer_load_dwordx4 a[8:11], v236, %[rs], s41 offen offset:1024 sc1\n"
    "v_mfma_f32_16x16x32_bf16 v[16:19], v[144:147], a[200:203], v[16:19]\n"
    "ds_read_b128 v[140:143], v234 offset:5120\n"
    "v_mfma_f32_16x16x32_bf16 v[20:23], v[144:147], a[204:207], v[20:23]\n"
    "v_mfma_f32_16x16x32_bf16 v[28:31], v[148:151], a[204:207], v[28:31]\n"
    "ds_read_b128 v[144:147], v234 offset:6144\n"
    "v_mfma_f32_16x16x32_bf16 v[24:27], v[148:151], a[200:203], v[24:27]\n"
    "buffer_load_dwordx4 v235, %[rs], s40 offen offset:2048 sc1 lds\n"
    "s_waitcnt lgkmcnt(3)\n"
    "v_mfma_f32_16x16x32_bf16 v[32:35], v[120:123], a[200:203], v[32:35]\n"
    "ds_read_b128 v[148:151], v234 offset:7168\n"
    "v_mfma_f32_16x16x32_bf16 v[36:39], v[120:123], a[204:207], v[36:39]\n"
    "v_mfma_f32_16x16x32_bf16 v[44:47], v[124:127], a[204:207], v[44:47]\n"
    "ds_read_b128 v[120:123], v234 offset:8192\n"
    "v_mfma_f32_16x16x32_bf16 v[40:43], v[124:127], a[200:203], v[40:43]\n"
    "buffer_load_dwordx4 a[12:15], v238, %[rs], s41 offen offset:1024 sc1\n"
    "v_mfma_f32_16x16x32_bf16 v[48:51], v[128:131], a[200:203], v[48:51]\n"
    "ds_read_b128 v[124:127], v234 offset:9216\n"
    "v_mfma_f32_16x16x32_bf16 v[52:55], v[128:131], a[204:207], v[52:55]\n"
    "v_mfma_f32_16x16x32_bf16 v[4:7], v[132:135], a[212:215], v[4:7]\n"
    "ds_read_b128 v[128:131], v234 offset:10240\n"
    "v_mfma_f32_16x16x32_bf16 v[0:3], v[132:135], a[208:211], v[0:3]\n"
    "s_add_u32 m0, s34, 73728\n"
    "s_add_u32 s40, s25, 221184\n"
    "buffer_load_dwordx4 v235, %[rs], s40 offen offset:0 sc1 lds\n"
    "s_waitcnt lgkmcnt(3)\n"
    "v_mfma_f32_16x16x32_bf16 v[8:11], v[136:139], a[208:211], v[8:11]\n"
    "ds_read_b128 v[132:135], v234 offset:11264\n"
    "v_mfma_f32_16x16x32_bf16 v[12:15], v[136:139], a[212:215], v[12:15]\n"
    "v_mfma_f32_16x16x32_bf16 v[20:23], v[140:143], a[212:215], v[20:23]\n"
    "ds_read_b128 v[136:139], v234 offset:12288\n"
    "v_mfma_f32_16x16x32_bf16 v[16:19], v[140:143], a[208:211], v[16:19]\n"
    "v_mfma_f32_16x16x32_bf16 v[24:27], v[144:147], a[208:211], v[24:27]\n"
    "ds_read_b128 v[140:143], v234 offset:13312\n"
    "v_mfma_f32_16x16x32_bf16 v[28:31], v[144:147], a[212:215], v[28:31]\n"
    "buffer_load_dwordx4 a[16:19], v236, %[rs], s41 offen offset:2048 sc1\n"
    "v_mfma_f32_16x16x32_bf16 v[36:39], v[148:151], a[212:215], v[36:39]\n"
    "ds_read_b128 v[144:147], v234 offset:14336\n"
    "v_mfma_f32_16x16x32_bf16 v[32:35], v[148:151], a[208:211], v[32:35]\n"
    "s_waitcnt lgkmcnt(3)\n"
    "v_mfma_f32_16x16x32_bf16 v[40:43], v[120:123], a[208:211], v[40:43]\n"
    "ds_read_b128 v[148:151], v234 offset:15360\n"
    "v_mfma_f32_16x16x32_bf16 v[44:47], v[120:123], a[212:215], v[44:47]\n"
    "buffer_load_dwordx4 v235, %[rs], s40 offen offset:1024 sc1 lds\n"
    "v_mfma_f32_16x16x32_bf16 v[52:55], v[124:127], a[212:215], v[52:55]\n"
    "ds_read_b128 v[120:123], v234 offset:16384\n"
    "v_mfma_f32_16x16x32_bf16 v[48:51], v[124:127], a[208:211], v[48:51]\n"
    "v_mfma_f32_16x16x32_bf16 v[0:3], v[128:131], a[216:219], v[0:3]\n"
    "ds_read_b128 v[124:127], v234 offset:17408\n"
    "v_mfma_f32_16x16x32_bf16 v[4:7], v[128:131], a[220:223], v[4:7]\n"
    "buffer_load_dwordx4 a[20:23], v238, %[rs], s41 offen offset:2048 sc1\n"
    "v_mfma_f32_16x16x32_bf16 v[12:15], v[132:135], a[220:223], v[12:15]\n"
    "ds_read_b128 v[128:131], v234 offset:18432\n"
    "v_mfma_f32_16x16x32_bf16 v[8:11], v[132:135], a[216:219], v[8:11]\n"
    "s_waitcnt lgkmcnt(3)\n"
    "v_mfma_f32_16x16x32_bf16 v[16:19], v[136:139], a[216:219], v[16:19]\n"
    "ds_read_b128 v[132:135], v234 offset:19456\n"
    "v_mfma_f32_16x16x32_bf16 v[20:23], v[136:139], a[220:223], v[20:23]\n"
    "buffer_load_dwordx4 v235, %[rs], s40 offen offset:2048 sc1 lds\n"
    "v_mfma_f32_16x16x32_bf16 v[28:31], v[140:143], a[220:223], v[28:31]\n"
    "ds_read_b128 v[136:139], v234 offset:20480\n"
    "v_mfma_f32_16x16x32_bf16 v[24:27], v[140:143], a[216:219], v[24:27]\n"
    "v_mfma_f32_16x16x32_bf16 v[32:35], v[144:147], a[216:219], v[32:35]\n"
    "ds_read_b128 v[140:143], v234 offset:21504\n"
    "v_mfma_f32_16x16x32_bf16 v[36:39], v[144:147], a[220:223], v[36:39]\n"
    "s_add_u32 s41, s29, 4096\n"
    "buffer_load_dwordx4 a[24:27], v236, %[rs], s41 offen offset:1024 sc1\n"
    "v_mfma_f32_16x16x32_bf16 v[44:47], v[148:151], a[220:223], v[44:47]\n"
    "ds_read_b128 v[144:147], v234 offset:22528\n"
    "v_mfma_f32_16x16x32_bf16 v[40:43], v[148:151], a[216:219], v[40:43]\n"
    "s_waitcnt lgkmcnt(3)\n"
    "v_mfma_f32_16x16x32_bf16 v[48:51], v[120:123], a[216:219], v[48:51]\n"
    "ds_read_b128 v[148:151], v234 offset:23552\n"
    "v_mfma_f32_16x16x32_bf16 v[52:55], v[120:123], a[220:223], v[52:55]\n"
    "s_add_u32 m0, s34, 86016\n"
    "s_add_u32 s40, s25, 233472\n"
    "buffer_load_dwordx4 v235, %[rs], s40 offen offset:0 sc1 lds\n"
    "v_mfma_f32_16x16x32_bf16 v[4:7], v[124:127], a[228:231], v[4:7]\n"
    "ds_read_b128 v[120:123], v234 offset:24576\n"
    "v_mfma_f32_16x16x32_bf16 v[0:3], v[124:127], a[224:227], v[0:3]\n"
    "v_mfma_f32_16x16x32_bf16 v[8:11], v[128:131], a[224:227], v[8:11]\n"
    "ds_read_b128 v[124:127], v234 offset:25600\n"
    "v_mfma_f32_16x16x32_bf16 v[12:15], v[128:131], a[228:231], v[12:15]\n"
    "v_mfma_f32_16x16x32_bf16 v[20:23], v[132:135], a[228:231], v[20:23]\n"
    "ds_read_b128 v[128:131], v234 offset:26624\n"
    "v_mfma_f32_16x16x32_bf16 v[16:19], v[132:135], a[224:227], v[16:19]\n"
    "buffer_load_dwordx4 a[28:31], v238, %[rs], s41 offen offset:1024 sc1\n"
    "s_waitcnt lgkmcnt(3)\n"
    "v_mfma_f32_16x16x32_bf16 v[24:27], v[136:139], a[224:227], v[24:27]\n"
    "ds_read_b128 v[132:135], v234 offset:27648\n"
    "v_mfma_f32_16x16x32_bf16 v[28:31], v[136:139], a[228:231], v[28:31]\n"
    "v_mfma_f32_16x16x32_bf16 v[36:39], v[140:143], a[228:231], v[36:39]\n"
    "s_barrier\n"
    "ds_read_b128 v[136:139], v232 offset:0\n"
    "v_mfma_f32_16x16x32_bf16 v[32:35], v[140:143], a[224:227], v[32:35]\n"
    "buffer_load_dwordx4 v235, %[rs], s40 offen offset:1024 sc1 lds\n"
    "v_mfma_f32_16x16x32_bf16 v[40:43], v[144:147], a[224:227], v[40:43]\n"
    "ds_read_b128 v[140:143], v232 offset:1024\n"
    "v_mfma_f32_16x16x32_bf16 v[44:47], v[144:147], a[228:231], v[44:47]\n"
    "v_mfma_f32_16x16x32_bf16 v[52:55], v[148:151], a[228:231], v[52:55]\n"
    "ds_read_b128 v[144:147], v232 offset:2048\n"
    "v_mfma_f32_16x16x32_bf16 v[48:51], v[148:151], a[224:227], v[48:51]\n"
    "buffer_load_dwordx4 a[32:35], v236, %[rs], s41 offen offset:2048 sc1\n"
    "s_waitcnt lgkmcnt(3)\n"
    "v_mfma_f32_16x16x32_bf16 v[0:3], v[120:123], a[232:235], v[0:3]\n"
    "ds_read_b128 v[148:151], v232 offset:3072\n"
    "v_mfma_f32_16x16x32_bf16 v[4:7], v[120:123], a[236:239], v[4:7]\n"
    "v_mfma_f32_16x16x32_bf16 v[12:15], v[124:127], a[236:239], v[12:15]\n"
    "ds_read_b128 v[120:123], v232 offset:4096\n"
    "v_mfma_f32_16x16x32_bf16 v[8:11], v[124:127], a[232:235], v[8:11]\n"
    "buffer_load_dwordx4 v235, %[rs], s40 offen offset:2048 sc1 lds\n"
    "v_mfma_f32_16x16x32_bf16 v[16:19], v[128:131], a[232:235], v[16:19]\n"
    "ds_read_b128 v[124:127], v232 offset:5120\n"
    "v_mfma_f32_16x16x32_bf16 v[20:23], v[128:131], a[236:239], v[20:23]\n"
    "v_mfma_f32_16x16x32_bf16 v[28:31], v[132:135], a[236:239], v[28:31]\n"
    "ds_read_b128 v[128:131], v232 offset:6144\n"
    "v_mfma_f32_16x16x32_bf16 v[24:27], v[132:135], a[232:235], v[24:27]\n"
    "buffer_load_dwordx4 a[36:39], v238, %[rs], s41 offen offset:2048 sc1\n"
    "s_waitcnt lgkmcnt(3)\n"
    "v_mfma_f32_16x16x32_bf16 v[32:35], v[136:139], a[232:235], v[32:35]\n"
    "ds_read_b128 v[132:135], v232 offset:7168\n"
    "v_mfma_f32_16x16x32_bf16 v[36:39], v[136:139], a[236:239], v[36:39]\n"
    "v_mfma_f32_16x16x32_bf16 v[44:47], v[140:143], a[236:239], v[44:47]\n"
    "ds_read_b128 v[136:139], v232 offset:8192\n"
    "v_mfma_f32_16x16x32_bf16 v[40:43], v[140:143], a[232:235], v[40:43]\n"
    "s_add_u32 m0, s34, 98304\n"
    "s_add_u32 s40, s25, 245760\n"
    "buffer_load_dwordx4 v235, %[rs], s40 offen offset:0 sc1 lds\n"
    "v_mfma_f32_16x16x32_bf16 v[48:51], v[144:147], a[232:235], v[48:51]\n"
    "ds_read_b128 v[140:143], v232 offset:9216\n"
    "v_mfma_f32_16x16x32_bf16 v[52:55], v[144:147], a[236:239], v[52:55]\n"
    "v_mfma_f32_16x16x32_bf16 v[4:7], v[148:151], a[244:247], v[4:7]\n"
    "ds_read_b128 v[144:147], v232 offset:10240\n"
    "v_mfma_f32_16x16x32_bf16 v[0:3], v[148:151], a[240:243], v[0:3]\n"
    "s_add_u32 s41, s30, 4096\n"
    "buffer_load_dwordx4 a[40:43], v236, %[rs], s41 offen offset:1024 sc1\n"
    "s_waitcnt lgkmcnt(3)\n"
    "v_mfma_f32_16x16x32_bf16 v[8:11], v[120:123], a[240:243], v[8:11]\n"
    "ds_read_b128 v[148:151], v232 offset:11264\n"
    "v_mfma_f32_16x16x32_bf16 v[12:15], v[120:123], a[244:247], v[12:15]\n"
    "v_mfma_f32_16x16x32_bf16 v[20:23], v[124:127], a[244:247], v[20:23]\n"
    "ds_read_b128 v[120:123], v232 offset:12288\n"
    "v_mfma_f32_16x16x32_bf16 v[16:19], v[124:127], a[240:243], v[16:19]\n"
    "v_mfma_f32_16x16x32_bf16 v[24:27], v[128:131], a[240:243], v[24:27]\n"
    "ds_read_b128 v[124:127], v232 offset:13312\n"
    "v_mfma_f32_16x16x32_bf16 v[28:31], v[128:131], a[244:247], v[28:31]\n"
    "buffer_load_dwordx4 a[44:47], v238, %[rs], s41 offen offset:1024 sc1\n"
    "v_mfma_f32_16x16x32_bf16 v[36:39], v[132:135], a[244:247], v[36:39]\n"
    "ds_read_b128 v[128:131], v232 offset:14336\n"
    "v_mfma_f32_16x16x32_bf16 v[32:35], v[132:135], a[240:243], v[32:35]\n"
    "s_waitcnt lgkmcnt(3)\n"
    "v_mfma_f32_16x16x32_bf16 v[40:43], v[136:139], a[240:243], v[40:43]\n"
    "ds_read_b128 v[132:135], v232 offset:15360\n"
    "v_mfma_f32_16x16x32_bf16 v[44:47], v[136:139], a[244:247], v[44:47]\n"
    "buffer_load_dwordx4 v235, %[rs], s40 offen offset:1024 sc1 lds\n"
    "v_mfma_f32_16x16x32_bf16 v[52:55], v[140:143], a[244:247], v[52:55]\n"
    "ds_read_b128 v[136:139], v232 offset:16384\n"
    "v_mfma_f32_16x16x32_bf16 v[48:51], v[140:143], a[240:243], v[48:51]\n"
    "s_waitcnt vmcnt(26)\n"
    "v_mfma_f32_16x16x32_bf16 v[0:3], v[144:147], a[248:251], v[0:3]\n"
    "ds_read_b128 v[140:143], v232 offset:17408\n"
    "v_mfma_f32_16x16x32_bf16 v[4:7], v[144:147], a[252:255], v[4:7]\n"
    "buffer_load_dwordx4 a[48:51], v236, %[rs], s41 offen offset:2048 sc1\n"
    "v_mfma_f32_16x16x32_bf16 v[12:15], v[148:151], a[252:255], v[12:15]\n"
    "ds_read_b128 v[144:147], v232 offset:18432\n"
    "v_mfma_f32_16x16x32_bf16 v[8:11], v[148:151], a[248:251], v[8:11]\n"
    "s_waitcnt lgkmcnt(3)\n"
    "v_mfma_f32_16x16x32_bf16 v[16:19], v[120:123], a[248:251], v[16:19]\n"
    "ds_read_b128 v[148:151], v232 offset:19456\n"
    "v_mfma_f32_16x16x32_bf16 v[20:23], v[120:123], a[252:255], v[20:23]\n"
    "buffer_load_dwordx4 v235, %[rs], s40 offen offset:2048 sc1 lds\n"
    "v_mfma_f32_16x16x32_bf16 v[28:31], v[124:127], a[252:255], v[28:31]\n"
    "ds_read_b128 v[120:123], v232 offset:20480\n"
    "v_mfma_f32_16x16x32_bf16 v[24:27], v[124:127], a[248:251], v[24:27]\n"
    "v_mfma_f32_16x16x32_bf16 v[32:35], v[128:131], a[248:251], v[32:35]\n"
    "ds_read_b128 v[124:127], v232 offset:21504\n"
    "v_mfma_f32_16x16x32_bf16 v[36:39], v[128:131], a[252:255], v[36:39]\n"
    "buffer_load_dwordx4 a[52:55], v238, %[rs], s41 offen offset:2048 sc1\n"
    "v_mfma_f32_16x16x32_bf16 v[44:47], v[132:135], a[252:255], v[44:47]\n"
    "ds_read_b128 v[128:131], v232 offset:22528\n"
    "v_mfma_f32_16x16x32_bf16 v[40:43], v[132:135], a[248:251], v[40:43]\n"
    "s_waitcnt lgkmcnt(3)\n"
    "v_mfma_f32_16x16x32_bf16 v[48:51], v[136:139], a[248:251], v[48:51]\n"
    "ds_read_b128 v[132:135], v232 offset:23552\n"
    "v_mfma_f32_16x16x32_bf16 v[52:55], v[136:139], a[252:255], v[52:55]\n"
    "s_add_u32 m0, s34, 110592\n"
    "s_add_u32 s40, s25, 258048\n"
    "buffer_load_dwordx4 v235, %[rs], s40 offen offset:0 sc1 lds\n"
    "s_waitcnt vmcnt(26)\n"
    "v_mfma_f32_16x16x32_bf16 v[4:7], v[140:143], a[4:7], v[4:7]\n"
    "ds_read_b128 v[136:139], v232 offset:24576\n"
    "v_mfma_f32_16x16x32_bf16 v[0:3], v[140:143], a[0:3], v[0:3]\n"
    "v_mfma_f32_16x16x32_bf16 v[8:11], v[144:147], a[0:3], v[8:11]\n"
    "ds_read_b128 v[140:143], v232 offset:25600\n"
    "v_mfma_f32_16x16x32_bf16 v[12:15], v[144:147], a[4:7], v[12:15]\n"
    "buffer_load_dword v224, v239, %[rs], s32 offen offset:384 sc1\n"
    "v_mfma_f32_16x16x32_bf16 v[20:23], v[148:151], a[4:7], v[20:23]\n"
    "ds_read_b128 v[144:147], v232 offset:26624\n"
    "v_mfma_f32_16x16x32_bf16 v[16:19], v[148:151], a[0:3], v[16:19]\n"
    "s_waitcnt lgkmcnt(3)\n"
    "v_mfma_f32_16x16x32_bf16 v[24:27], v[120:123], a[0:3], v[24:27]\n"
    "ds_read_b128 v[148:151], v232 offset:27648\n"
    "v_mfma_f32_16x16x32_bf16 v[28:31], v[120:123], a[4:7], v[28:31]\n"
    "v_mfma_f32_16x16x32_bf16 v[36:39], v[124:127], a[4:7], v[36:39]\n"
    "ds_read_b128 v[120:123], v232 offset:28672\n"
    "v_mfma_f32_16x16x32_bf16 v[32:35], v[124:127], a[0:3], v[32:35]\n"
    "buffer_load_dwordx4 v235, %[rs], s40 offen offset:1024 sc1 lds\n"
    "v_mfma_f32_16x16x32_bf16 v[40:43], v[128:131], a[0:3], v[40:43]\n"
    "ds_read_b128 v[124:127], v232 offset:29696\n"
    "v_mfma_f32_16x16x32_bf16 v[44:47], v[128:131], a[4:7], v[44:47]\n"
    "v_mfma_f32_16x16x32_bf16 v[52:55], v[132:135], a[4:7], v[52:55]\n"
    "ds_read_b128 v[128:131], v232 offset:30720\n"
    "v_mfma_f32_16x16x32_bf16 v[48:51], v[132:135], a[0:3], v[48:51]\n"
    "buffer_load_dwordx4 v235, %[rs], s40 offen offset:2048 sc1 lds\n"
    "s_waitcnt vmcnt(23)\n"
    "s_waitcnt lgkmcnt(3)\n"
    "v_mfma_f32_16x16x32_bf16 v[0:3], v[136:139], a[8:11], v[0:3]\n"
    "ds_read_b128 v[132:135], v232 offset:31744\n"
    "v_mfma_f32_16x16x32_bf16 v[4:7], v[136:139], a[12:15], v[4:7]\n"
    "v_mfma_f32_16x16x32_bf16 v[12:15], v[140:143], a[12:15], v[12:15]\n"
    "ds_read_b128 v[136:139], v232 offset:32768\n"
    "v_mfma_f32_16x16x32_bf16 v[8:11], v[140:143], a[8:11], v[8:11]\n"
    "s_add_u32 m0, s34, 122880\n"
    "s_add_u32 s40, s25, 270336\n"
    "buffer_load_dwordx4 v235, %[rs], s40 offen offset:0 sc1 lds\n"
    "v_mfma_f32_16x16x32_bf16 v[16:19], v[144:147], a[8:11], v[16:19]\n"
    "ds_read_b128 v[140:143], v232 offset:33792\n"
    "v_mfma_f32_16x16x32_bf16 v[20:23], v[144:147], a[12:15], v[20:23]\n"
    "v_mfma_f32_16x16x32_bf16 v[28:31], v[148:151], a[12:15], v[28:31]\n"
    "ds_read_b128 v[144:147], v232 offset:34816\n"
    "v_mfma_f32_16x16x32_bf16 v[24:27], v[148:151], a[8:11], v[24:27]\n"
    "buffer_load_dwordx4 v235, %[rs], s40 offen offset:1024 sc1 lds\n"
    "s_waitcnt lgkmcnt(3)\n"
    "v_mfma_f32_16x16x32_bf16 v[32:35], v[120:123], a[8:11], v[32:35]\n"
    "ds_read_b128 v[148:151], v232 offset:35840\n"
    "v_mfma_f32_16x16x32_bf16 v[36:39], v[120:123], a[12:15], v[36:39]\n"
    "v_mfma_f32_16x16x32_bf16 v[44:47], v[124:127], a[12:15], v[44:47]\n"
    "s_barrier\n"
    "ds_read_b128 v[120:123], v232 offset:36864\n"
    "v_mfma_f32_16x16x32_bf16 v[40:43], v[124:127], a[8:11], v[40:43]\n"
    "buffer_load_dwordx4 v235, %[rs], s40 offen offset:2048 sc1 lds\n"
    "v_mfma_f32_16x16x32_bf16 v[48:51], v[128:131], a[8:11], v[48:51]\n"
    "ds_read_b128 v[124:127], v232 offset:37888\n"
    "v_mfma_f32_16x16x32_bf16 v[52:55], v[128:131], a[12:15], v[52:55]\n"
    "s_waitcnt vmcnt(22)\n"
    "v_mfma_f32_16x16x32_bf16 v[4:7], v[132:135], a[20:23], v[4:7]\n"
    "ds_read_b128 v[128:131], v232 offset:38912\n"
    "v_mfma_f32_16x16x32_bf16 v[0:3], v[132:135], a[16:19], v[0:3]\n"
    "s_add_u32 m0, s34, 135168\n"
    "s_add_u32 s40, s25, 282624\n"
    "buffer_load_dwordx4 v235, %[rs], s40 offen offset:0 sc1 lds\n"
    "s_waitcnt lgkmcnt(3)\n"
    "v_mfma_f32_16x16x32_bf16 v[8:11], v[136:139], a[16:19], v[8:11]\n"
    "ds_read_b128 v[132:135], v232 offset:39936\n"
    "v_mfma_f32_16x16x32_bf16 v[12:15], v[136:139], a[20:23], v[12:15]\n"
    "v_mfma_f32_16x16x32_bf16 v[20:23], v[140:143], a[20:23], v[20:23]\n"
    "ds_read_b128 v[136:139], v232 offset:40960\n"
    "v_mfma_f32_16x16x32_bf16 v[16:19], v[140:143], a[16:19], v[16:19]\n"
    "v_mfma_f32_16x16x32_bf16 v[24:27], v[144:147], a[16:19], v[24:27]\n"
    "ds_read_b128 v[140:143], v232 offset:41984\n"
    "v_mfma_f32_16x16x32_bf16 v[28:31], v[144:147], a[20:23], v[28:31]\n"
    "buffer_load_dwordx4 v235, %[rs], s40 offen offset:1024 sc1 lds\n"
    "v_mfma_f32_16x16x32_bf16 v[36:39], v[148:151], a[20:23], v[36:39]\n"
    "ds_read_b128 v[144:147], v232 offset:43008\n"
    "v_mfma_f32_16x16x32_bf16 v[32:35], v[148:151], a[16:19], v[32:35]\n"
    "s_waitcnt lgkmcnt(3)\n"
    "v_mfma_f32_16x16x32_bf16 v[40:43], v[120:123], a[16:19], v[40:43]\n"
    "ds_read_b128 v[148:151], v232 offset:44032\n"
    "v_mfma_f32_16x16x32_bf16 v[44:47], v[120:123], a[20:23], v[44:47]\n"
    "buffer_load_dwordx4 v235, %[rs], s40 offen offset:2048 sc1 lds\n"
    "v_mfma_f32_16x16x32_bf16 v[52:55], v[124:127], a[20:23], v[52:55]\n"
    "ds_read_b128 v[120:123], v232 offset:45056\n"
    "v_mfma_f32_16x16x32_bf16 v[48:51], v[124:127], a[16:19], v[48:51]\n"
    "s_waitcnt vmcnt(21)\n"
    "v_mfma_f32_16x16x32_bf16 v[0:3], v[128:131], a[24:27], v[0:3]\n"
    "ds_read_b128 v[124:127], v232 offset:46080\n"
    "v_mfma_f32_16x16x32_bf16 v[4:7], v[128:131], a[28:31], v[4:7]\n"
    "buffer_load_dwordx4 a[104:107], v236, %[rs], s36 offen sc1\n"
    "v_mfma_f32_16x16x32_bf16 v[12:15], v[132:135], a[28:31], v[12:15]\n"
    "ds_read_b128 v[128:131], v232 offset:47104\n"
    "v_mfma_f32_16x16x32_bf16 v[8:11], v[132:135], a[24:27], v[8:11]\n"
    "s_waitcnt lgkmcnt(3)\n"
    "v_mfma_f32_16x16x32_bf16 v[16:19], v[136:139], a[24:27], v[16:19]\n"
    "ds_read_b128 v[132:135], v232 offset:48128\n"
    "v_mfma_f32_16x16x32_bf16 v[20:23], v[136:139], a[28:31], v[20:23]\n"
    "buffer_load_dwordx2 a[112:113], v236, %[rs], s37 offen sc1\n"
    "v_mfma_f32_16x16x32_bf16 v[28:31], v[140:143], a[28:31], v[28:31]\n"
    "ds_read_b128 v[136:139], v232 offset:49152\n"
    "v_mfma_f32_16x16x32_bf16 v[24:27], v[140:143], a[24:27], v[24:27]\n"
    "v_mfma_f32_16x16x32_bf16 v[32:35], v[144:147], a[24:27], v[32:35]\n"
    "ds_read_b128 v[140:143], v232 offset:50176\n"
    "v_mfma_f32_16x16x32_bf16 v[36:39], v[144:147], a[28:31], v[36:39]\n"
    "s_waitcnt vmcnt(10)\n"
    "v_readfirstlane_b32 s45, v224\n"
    "s_mov_b32 s44, 0x10000\n"
    "s_nop 0\n"
    "s_cmp_ge_u32 s45, 4\n"
    "s_cbranch_scc1 Lpe10_%=\n"
    "Lpoll10_%=:\n"
    "buffer_load_dword v224, v239, %[rs], s32 offen offset:384 sc1\n"
    "s_waitcnt vmcnt(0)\n"
    "v_readfirstlane_b32 s45, v224\n"
    "s_sub_u32 s44, s44, 1\n"
    "s_cbranch_scc1 Lpe10_%=\n"
    "s_cmp_lt_u32 s45, 4\n"
    "s_cbranch_scc1 Lpoll10_%=\n"
    "Lpe10_%=:\n"
    "s_add_u32 s41, s28, 4096\n"
    "buffer_load_dwordx4 a[56:59], v236, %[rs], s41 offen offset:3072 sc1\n"
    "v_mfma_f32_16x16x32_bf16 v[44:47], v[148:151], a[28:31], v[44:47]\n"
    "ds_read_b128 v[144:147], v232 offset:51200\n"
    "v_mfma_f32_16x16x32_bf16 v[40:43], v[148:151], a[24:27], v[40:43]\n"
    "s_waitcnt lgkmcnt(3)\n"
    "v_mfma_f32_16x16x32_bf16 v[48:51], v[120:123], a[24:27], v[48:51]\n"
    "ds_read_b128 v[148:151], v232 offset:52224\n"
    "v_mfma_f32_16x16x32_bf16 v[52:55], v[120:123], a[28:31], v[52:55]\n"
    "buffer_load_dwordx4 a[60:63], v238, %[rs], s41 offen offset:3072 sc1\n"
    "v_mfma_f32_16x16x32_bf16 v[4:7], v[124:127], a[36:39], v[4:7]\n"
    "ds_read_b128 v[120:123], v232 offset:53248\n"
    "v_mfma_f32_16x16x32_bf16 v[0:3], v[124:127], a[32:35], v[0:3]\n"
    "v_mfma_f32_16x16x32_bf16 v[8:11], v[128:131], a[32:35], v[8:11]\n"
    "ds_read_b128 v[124:127], v232 offset:54272\n"
    "v_mfma_f32_16x16x32_bf16 v[12:15], v[128:131], a[36:39], v[12:15]\n"
    "s_add_u32 s41, s28, 8192\n"
    "buffer_load_dwordx4 a[64:67], v236, %[rs], s41 offen offset:0 sc1\n"
    "v_mfma_f32_16x16x32_bf16 v[20:23], v[132:135], a[36:39], v[20:23]\n"
    "ds_read_b128 v[128:131], v232 offset:55296\n"
    "v_mfma_f32_16x16x32_bf16 v[16:19], v[132:135], a[32:35], v[16:19]\n"
    "s_waitcnt lgkmcnt(3)\n"
    "v_mfma_f32_16x16x32_bf16 v[24:27], v[136:139], a[32:35], v[24:27]\n"
    "ds_read_b128 v[132:135], v232 offset:56320\n"
    "v_mfma_f32_16x16x32_bf16 v[28:31], v[136:139], a[36:39], v[28:31]\n"
    "v_mfma_f32_16x16x32_bf16 v[36:39], v[140:143], a[36:39], v[36:39]\n"
    "ds_read_b128 v[136:139], v232 offset:57344\n"
    "v_mfma_f32_16x16x32_bf16 v[32:35], v[140:143], a[32:35], v[32:35]\n"
    "buffer_load_dwordx4 a[68:71], v238, %[rs], s41 offen offset:0 sc1\n"
    "v_mfma_f32_16x16x32_bf16 v[40:43], v[144:147], a[32:35], v[40:43]\n"
    "ds_read_b128 v[140:143], v232 offset:58368\n"
    "v_mfma_f32_16x16x32_bf16 v[44:47], v[144:147], a[36:39], v[44:47]\n"
    "v_mfma_f32_16x16x32_bf16 v[52:55], v[148:151], a[36:39], v[52:55]\n"
    "ds_read_b128 v[144:147], v232 offset:59392\n"
    "v_mfma_f32_16x16x32_bf16 v[48:51], v[148:151], a[32:35], v[48:51]\n"
    "buffer_load_dwordx4 a[116:119], v241, %[rs], s38 offen offset:0 sc1\n"
    "s_waitcnt lgkmcnt(3)\n"
    "v_mfma_f32_16x16x32_bf16 v[0:3], v[120:123], a[40:43], v[0:3]\n"
    "ds_read_b128 v[148:151], v232 offset:60416\n"
    "v_mfma_f32_16x16x32_bf16 v[4:7], v[120:123], a[44:47], v[4:7]\n"
    "v_mfma_f32_16x16x32_bf16 v[12:15], v[124:127], a[44:47], v[12:15]\n"
    "ds_read_b128 v[120:123], v232 offset:61440\n"
    "v_mfma_f32_16x16x32_bf16 v[8:11], v[124:127], a[40:43], v[8:11]\n"
    "s_add_u32 s41, s29, 4096\n"
    "buffer_load_dwordx4 a[72:75], v236, %[rs], s41 offen offset:3072 sc1\n"
    "v_mfma_f32_16x16x32_bf16 v[16:19], v[128:131], a[40:43], v[16:19]\n"
    "ds_read_b128 v[124:127], v232 offset:62464\n"
    "v_mfma_f32_16x16x32_bf16 v[20:23], v[128:131], a[44:47], v[20:23]\n"
    "v_mfma_f32_16x16x32_bf16 v[28:31], v[132:135], a[44:47], v[28:31]\n"
    "ds_read_b128 v[128:131], v232 offset:63488\n"
    "v_mfma_f32_16x16x32_bf16 v[24:27], v[132:135], a[40:43], v[24:27]\n"
    "buffer_load_dwordx4 a[76:79], v238, %[rs], s41 offen offset:3072 sc1\n"
    "s_waitcnt lgkmcnt(3)\n"
    "v_mfma_f32_16x16x32_bf16 v[32:35], v[136:139], a[40:43], v[32:35]\n"
    "ds_read_b128 v[132:135], v232 offset:64512\n"
    "v_mfma_f32_16x16x32_bf16 v[36:39], v[136:139], a[44:47], v[36:39]\n"
    "v_mfma_f32_16x16x32_bf16 v[44:47], v[140:143], a[44:47], v[44:47]\n"
    "ds_read_b128 v[136:139], v233 offset:0\n"
    "v_mfma_f32_16x16x32_bf16 v[40:43], v[140:143], a[40:43], v[40:43]\n"
    "buffer_load_dword a124, v241, %[rs], s38 offen offset:16 sc1\n"
    "v_mfma_f32_16x16x32_bf16 v[48:51], v[144:147], a[40:43], v[48:51]\n"
    "ds_read_b128 v[140:143], v233 offset:1024\n"
    "v_mfma_f32_16x16x32_bf16 v[52:55], v[144:147], a[44:47], v[52:55]\n"
    "v_mfma_f32_16x16x32_bf16 v[4:7], v[148:151], a[52:55], v[4:7]\n"
    "ds_read_b128 v[144:147], v233 offset:2048\n"
    "v_mfma_f32_16x16x32_bf16 v[0:3], v[148:151], a[48:51], v[0:3]\n"
    "s_add_u32 s41, s29, 8192\n"
    "buffer_load_dwordx4 a[80:83], v236, %[rs], s41 offen offset:0 sc1\n"
    "s_waitcnt lgkmcnt(3)\n"
    "v_mfma_f32_16x16x32_bf16 v[8:11], v[120:123], a[48:51], v[8:11]\n"
    "ds_read_b128 v[148:151], v233 offset:3072\n"
    "v_mfma_f32_16x16x32_bf16 v[12:15], v[120:123], a[52:55], v[12:15]\n"
    "v_mfma_f32_16x16x32_bf16 v[20:23], v[124:127], a[52:55], v[20:23]\n"
    "ds_read_b128 v[120:123], v233 offset:4096\n"
    "v_mfma_f32_16x16x32_bf16 v[16:19], v[124:127], a[48:51], v[16:19]\n"
    "buffer_load_dwordx4 a[84:87], v238, %[rs], s41 offen offset:0 sc1\n"
    "v_mfma_f32_16x16x32_bf16 v[24:27], v[128:131], a[48:51], v[24:27]\n"
    "ds_read_b128 v[124:127], v233 offset:5120\n"
    "v_mfma_f32_16x16x32_bf16 v[28:31], v[128:131], a[52:55], v[28:31]\n"
    "v_mfma_f32_16x16x32_bf16 v[36:39], v[132:135], a[52:55], v[36:39]\n"
    "ds_read_b128 v[128:131], v233 offset:6144\n"
    "v_mfma_f32_16x16x32_bf16 v[32:35], v[132:135], a[48:51], v[32:35]\n"
    "s_waitcnt lgkmcnt(3)\n"
    "v_mfma_f32_16x16x32_bf16 v[40:43], v[136:139], a[48:51], v[40:43]\n"
    "ds_read_b128 v[132:135], v233 offset:7168\n"
    "v_mfma_f32_16x16x32_bf16 v[44:47], v[136:139], a[52:55], v[44:47]\n"
    "s_add_u32 s41, s30, 4096\n"
    "buffer_load_dwordx4 a[88:91], v236, %[rs], s41 offen offset:3072 sc1\n"
    "v_mfma_f32_16x16x32_bf16 v[52:55], v[140:143], a[52:55], v[52:55]\n"
    "s_barrier\n"
    "ds_read_b128 v[136:139], v233 offset:8192\n"
    "v_mfma_f32_16x16x32_bf16 v[48:51], v[140:143], a[48:51], v[48:51]\n"
    "s_waitcnt vmcnt(9)\n"
    "v_mfma_f32_16x16x32_bf16 v[0:3], v[144:147], a[56:59], v[0:3]\n"
    "ds_read_b128 v[140:143], v233 offset:9216\n"
    "v_mfma_f32_16x16x32_bf16 v[4:7], v[144:147], a[60:63], v[4:7]\n"
    "buffer_load_dwordx4 a[92:95], v238, %[rs], s41 offen offset:3072 sc1\n"
    "v_mfma_f32_16x16x32_bf16 v[12:15], v[148:151], a[60:63], v[12:15]\n"
    "ds_read_b128 v[144:147], v233 offset:10240\n"
    "v_mfma_f32_16x16x32_bf16 v[8:11], v[148:151], a[56:59], v[8:11]\n"
    "s_waitcnt lgkmcnt(3)\n"
    "v_mfma_f32_16x16x32_bf16 v[16:19], v[120:123], a[56:59], v[16:19]\n"
    "ds_read_b128 v[148:151], v233 offset:11264\n"
    "v_mfma_f32_16x16x32_bf16 v[20:23], v[120:123], a[60:63], v[20:23]\n"
    "buffer_load_dword a126, v242, %[rs], s39 offen offset:0 sc1\n"
    "v_mfma_f32_16x16x32_bf16 v[28:31], v[124:127], a[60:63], v[28:31]\n"
    "ds_read_b128 v[120:123], v233 offset:12288\n"
    "v_mfma_f32_16x16x32_bf16 v[24:27], v[124:127], a[56:59], v[24:27]\n"
    "v_mfma_f32_16x16x32_bf16 v[32:35], v[128:131], a[56:59], v[32:35]\n"
    "ds_read_b128 v[124:127], v233 offset:13312\n"
    "v_mfma_f32_16x16x32_bf16 v[36:39], v[128:131], a[60:63], v[36:39]\n"
    "s_add_u32 s41, s30, 8192\n"
    "buffer_load_dwordx4 a[96:99], v236, %[rs], s41 offen offset:0 sc1\n"
    "v_mfma_f32_16x16x32_bf16 v[44:47], v[132:135], a[60:63], v[44:47]\n"
    "ds_read_b128 v[128:131], v233 offset:14336\n"
    "v_mfma_f32_16x16x32_bf16 v[40:43], v[132:135], a[56:59], v[40:43]\n"
    "s_waitcnt lgkmcnt(3)\n"
    "v_mfma_f32_16x16x32_bf16 v[48:51], v[136:139], a[56:59], v[48:51]\n"
    "ds_read_b128 v[132:135], v233 offset:15360\n"
    "v_mfma_f32_16x16x32_bf16 v[52:55], v[136:139], a[60:63], v[52:55]\n"
    "buffer_load_dwordx4 a[108:111], v237, %[rs], s36 offen sc1\n"
    "s_waitcnt vmcnt(11)\n"
    "v_mfma_f32_16x16x32_bf16 v[4:7], v[140:143], a[68:71], v[4:7]\n"
    "ds_read_b128 v[136:139], v233 offset:16384\n"
    "v_mfma_f32_16x16x32_bf16 v[0:3], v[140:143], a[64:67], v[0:3]\n"
    "v_mfma_f32_16x16x32_bf16 v[8:11], v[144:147], a[64:67], v[8:11]\n"
    "ds_read_b128 v[140:143], v233 offset:17408\n"
    "v_mfma_f32_16x16x32_bf16 v[12:15], v[144:147], a[68:71], v[12:15]\n"
    "buffer_load_dwordx4 a[100:103], v238, %[rs], s41 offen offset:0 sc1\n"
    "v_mfma_f32_16x16x32_bf16 v[20:23], v[148:151], a[68:71], v[20:23]\n"
    "ds_read_b128 v[144:147], v233 offset:18432\n"
    "v_mfma_f32_16x16x32_bf16 v[16:19], v[148:151], a[64:67], v[16:19]\n"
    "s_waitcnt lgkmcnt(3)\n"
    "v_mfma_f32_16x16x32_bf16 v[24:27], v[120:123], a[64:67], v[24:27]\n"
    "ds_read_b128 v[148:151], v233 offset:19456\n"
    "v_mfma_f32_16x16x32_bf16 v[28:31], v[120:123], a[68:71], v[28:31]\n"
    "v_mfma_f32_16x16x32_bf16 v[36:39], v[124:127], a[68:71], v[36:39]\n"
    "ds_read_b128 v[120:123], v233 offset:20480\n"
    "v_mfma_f32_16x16x32_bf16 v[32:35], v[124:127], a[64:67], v[32:35]\n"
    "buffer_load_dwordx2 a[114:115], v237, %[rs], s37 offen sc1\n"
    "v_mfma_f32_16x16x32_bf16 v[40:43], v[128:131], a[64:67], v[40:43]\n"
    "ds_read_b128 v[124:127], v233 offset:21504\n"
    "v_mfma_f32_16x16x32_bf16 v[44:47], v[128:131], a[68:71], v[44:47]\n"
    "v_mfma_f32_16x16x32_bf16 v[52:55], v[132:135], a[68:71], v[52:55]\n"
    "ds_read_b128 v[128:131], v233 offset:22528\n"
    "v_mfma_f32_16x16x32_bf16 v[48:51], v[132:135], a[64:67], v[48:51]\n"
    "buffer_load_dwordx4 a[120:123], v241, %[rs], s38 offen offset:2048 sc1\n"
    "s_waitcnt vmcnt(11)\n"
    "s_waitcnt lgkmcnt(3)\n"
    "v_mfma_f32_16x16x32_bf16 v[0:3], v[136:139], a[72:75], v[0:3]\n"
    "ds_read_b128 v[132:135], v233 offset:23552\n"
    "v_mfma_f32_16x16x32_bf16 v[4:7], v[136:139], a[76:79], v[4:7]\n"
    "v_mfma_f32_16x16x32_bf16 v[12:15], v[140:143], a[76:79], v[12:15]\n"
    "ds_read_b128 v[136:139], v233 offset:24576\n"
    "v_mfma_f32_16x16x32_bf16 v[8:11], v[140:143], a[72:75], v[8:11]\n"
    "buffer_load_dword a125, v241, %[rs], s38 offen offset:2064 sc1\n"
    "v_mfma_f32_16x16x32_bf16 v[16:19], v[144:147], a[72:75], v[16:19]\n"
    "ds_read_b128 v[140:143], v233 offset:25600\n"
    "v_mfma_f32_16x16x32_bf16 v[20:23], v[144:147], a[76:79], v[20:23]\n"
    "v_mfma_f32_16x16x32_bf16 v[28:31], v[148:151], a[76:79], v[28:31]\n"
    "ds_read_b128 v[144:147], v233 offset:26624\n"
    "v_mfma_f32_16x16x32_bf16 v[24:27], v[148:151], a[72:75], v[24:27]\n"
    "buffer_load_dword a127, v242, %[rs], s39 offen offset:256 sc1\n"
    "s_waitcnt lgkmcnt(3)\n"
    "v_mfma_f32_16x16x32_bf16 v[32:35], v[120:123], a[72:75], v[32:35]\n"
    "ds_read_b128 v[148:151], v233 offset:27648\n"
    "v_mfma_f32_16x16x32_bf16 v[36:39], v[120:123], a[76:79], v[36:39]\n"
    "v_mfma_f32_16x16x32_bf16 v[44:47], v[124:127], a[76:79], v[44:47]\n"
    "ds_read_b128 v[120:123], v233 offset:28672\n"
    "v_mfma_f32_16x16x32_bf16 v[40:43], v[124:127], a[72:75], v[40:43]\n"
    "s_add_u32 m0, s34, 147456\n"
    "s_add_u32 s40, s25, 294912\n"
    "buffer_load_dwordx4 v235, %[rs], s40 offen offset:0 sc1 lds\n"
    "v_mfma_f32_16x16x32_bf16 v[48:51], v[128:131], a[72:75], v[48:51]\n"
    "ds_read_b128 v[124:127], v233 offset:29696\n"
    "v_mfma_f32_16x16x32_bf16 v[52:55], v[128:131], a[76:79], v[52:55]\n"
    "s_waitcnt vmcnt(11)\n"
    "v_mfma_f32_16x16x32_bf16 v[4:7], v[132:135], a[84:87], v[4:7]\n"
    "ds_read_b128 v[128:131], v233 offset:30720\n"
    "v_mfma_f32_16x16x32_bf16 v[0:3], v[132:135], a[80:83], v[0:3]\n"
    "buffer_load_dwordx4 v235, %[rs], s40 offen offset:1024 sc1 lds\n"
    "s_waitcnt lgkmcnt(3)\n"
    "v_mfma_f32_16x16x32_bf16 v[8:11], v[136:139], a[80:83], v[8:11]\n"
    "ds_read_b128 v[132:135], v233 offset:31744\n"
    "v_mfma_f32_16x16x32_bf16 v[12:15], v[136:139], a[84:87], v[12:15]\n"
    "v_mfma_f32_16x16x32_bf16 v[20:23], v[140:143], a[84:87], v[20:23]\n"
    "ds_read_b128 v[136:139], v233 offset:32768\n"
    "v_mfma_f32_16x16x32_bf16 v[16:19], v[140:143], a[80:83], v[16:19]\n"
    "buffer_load_dwordx4 v235, %[rs], s40 offen offset:2048 sc1 lds\n"
    "v_mfma_f32_16x16x32_bf16 v[24:27], v[144:147], a[80:83], v[24:27]\n"
    "ds_read_b128 v[140:143], v233 offset:33792\n"
    "v_mfma_f32_16x16x32_bf16 v[28:31], v[144:147], a[84:87], v[28:31]\n"
    "v_mfma_f32_16x16x32_bf16 v[36:39], v[148:151], a[84:87], v[36:39]\n"
    "ds_read_b128 v[144:147], v233 offset:34816\n"
    "v_mfma_f32_16x16x32_bf16 v[32:35], v[148:151], a[80:83], v[32:35]\n"
    "s_waitcnt lgkmcnt(3)\n"
    "v_mfma_f32_16x16x32_bf16 v[40:43], v[120:123], a[80:83], v[40:43]\n"
    "ds_read_b128 v[148:151], v233 offset:35840\n"
    "v_mfma_f32_16x16x32_bf16 v[44:47], v[120:123], a[84:87], v[44:47]\n"
    "s_add_u32 m0, s34, 0\n"
    "s_add_u32 s40, s25, 307200\n"
    "buffer_load_dwordx4 v235, %[rs], s40 offen offset:0 sc1 lds\n"
    "v_mfma_f32_16x16x32_bf16 v[52:55], v[124:127], a[84:87], v[52:55]\n"
    "ds_read_b128 v[120:123], v233 offset:36864\n"
    "v_mfma_f32_16x16x32_bf16 v[48:51], v[124:127], a[80:83], v[48:51]\n"
    "s_waitcnt vmcnt(12)\n"
    "v_mfma_f32_16x16x32_bf16 v[0:3], v[128:131], a[88:91], v[0:3]\n"
    "ds_read_b128 v[124:127], v233 offset:37888\n"
    "v_mfma_f32_16x16x32_bf16 v[4:7], v[128:131], a[92:95], v[4:7]\n"
    "buffer_load_dwordx4 v235, %[rs], s40 offen offset:1024 sc1 lds\n"
    "v_mfma_f32_16x16x32_bf16 v[12:15], v[132:135], a[92:95], v[12:15]\n"
    "ds_read_b128 v[128:131], v233 offset:38912\n"
    "v_mfma_f32_16x16x32_bf16 v[8:11], v[132:135], a[88:91], v[8:11]\n"
    "s_waitcnt lgkmcnt(3)\n"
    "v_mfma_f32_16x16x32_bf16 v[16:19], v[136:139], a[88:91], v[16:19]\n"
    "ds_read_b128 v[132:135], v233 offset:39936\n"
    "v_mfma_f32_16x16x32_bf16 v[20:23], v[136:139], a[92:95], v[20:23]\n"
    "buffer_load_dwordx4 v235, %[rs], s40 offen offset:2048 sc1 lds\n"
    "v_mfma_f32_16x16x32_bf16 v[28:31], v[140:143], a[92:95], v[28:31]\n"
    "ds_read_b128 v[136:139], v233 offset:40960\n"
    "v_mfma_f32_16x16x32_bf16 v[24:27], v[140:143], a[88:91], v[24:27]\n"
    "v_mfma_f32_16x16x32_bf16 v[32:35], v[144:147], a[88:91], v[32:35]\n"
    "ds_read_b128 v[140:143], v233 offset:41984\n"
    "v_mfma_f32_16x16x32_bf16 v[36:39], v[144:147], a[92:95], v[36:39]\n"
    "s_add_u32 m0, s34, 12288\n"
    "s_add_u32 s40, s25, 319488\n"
    "buffer_load_dwordx4 v235, %[rs], s40 offen offset:0 sc1 lds\n"
    "v_mfma_f32_16x16x32_bf16 v[44:47], v[148:151], a[92:95], v[44:47]\n"
    "ds_read_b128 v[144:147], v233 offset:43008\n"
    "v_mfma_f32_16x16x32_bf16 v[40:43], v[148:151], a[88:91], v[40:43]\n"
    "s_waitcnt lgkmcnt(3)\n"
    "v_mfma_f32_16x16x32_bf16 v[48:51], v[120:123], a[88:91], v[48:51]\n"
    "ds_read_b128 v[148:151], v233 offset:44032\n"
    "v_mfma_f32_16x16x32_bf16 v[52:55], v[120:123], a[92:95], v[52:55]\n"
    "buffer_load_dwordx4 v235, %[rs], s40 offen offset:1024 sc1 lds\n"
    "s_waitcnt vmcnt(12)\n"
    "v_mfma_f32_16x16x32_bf16 v[4:7], v[124:127], a[100:103], v[4:7]\n"
    "s_barrier\n"
    "ds_read_b128 v[120:123], v233 offset:45056\n"
    "v_mfma_f32_16x16x32_bf16 v[0:3], v[124:127], a[96:99], v[0:3]\n"
    "v_mfma_f32_16x16x32_bf16 v[8:11], v[128:131], a[96:99], v[8:11]\n"
    "ds_read_b128 v[124:127], v233 offset:46080\n"
    "v_mfma_f32_16x16x32_bf16 v[12:15], v[128:131], a[100:103], v[12:15]\n"
    "buffer_load_dwordx4 v235, %[rs], s40 offen offset:2048 sc1 lds\n"
    "v_mfma_f32_16x16x32_bf16 v[20:23], v[132:135], a[100:103], v[20:23]\n"
    "ds_read_b128 v[128:131], v233 offset:47104\n"
    "v_mfma_f32_16x16x32_bf16 v[16:19], v[132:135], a[96:99], v[16:19]\n"
    "s_waitcnt lgkmcnt(3)\n"
    "v_mfma_f32_16x16x32_bf16 v[24:27], v[136:139], a[96:99], v[24:27]\n"
    "ds_read_b128 v[132:135], v233 offset:48128\n"
    "v_mfma_f32_16x16x32_bf16 v[28:31], v[136:139], a[100:103], v[28:31]\n"
    "v_cvt_pk_bf16_f32 v72, v0, v1\n"
    "v_cvt_pk_bf16_f32 v73, v2, v3\n"
    "v_mfma_f32_16x16x32_bf16 v[36:39], v[140:143], a[100:103], v[36:39]\n"
    "ds_read_b128 v[136:139], v233 offset:49152\n"
    "v_mfma_f32_16x16x32_bf16 v[32:35], v[140:143], a[96:99], v[32:35]\n"
    "v_cvt_pk_bf16_f32 v74, v4, v5\n"
    "v_cvt_pk_bf16_f32 v75, v6, v7\n"
    "v_mfma_f32_16x16x32_bf16 v[40:43], v[144:147], a[96:99], v[40:43]\n"
    "ds_read_b128 v[140:143], v233 offset:50176\n"
    "v_mfma_f32_16x16x32_bf16 v[44:47], v[144:147], a[100:103], v[44:47]\n"
    "s_add_u32 s46, s31, 0\n"
    "buffer_store_dwordx4 v[72:75], v236, %[rs], s46 offen offset:0 sc1\n"
    "v_cvt_pk_bf16_f32 v76, v8, v9\n"
    "v_mfma_f32_16x16x32_bf16 v[52:55], v[148:151], a[100:103], v[52:55]\n"
    "ds_read_b128 v[144:147], v233 offset:51200\n"
    "v_mfma_f32_16x16x32_bf16 v[48:51], v[148:151], a[96:99], v[48:51]\n"
    "v_cvt_pk_bf16_f32 v77, v10, v11\n"
    "v_cvt_pk_bf16_f32 v78, v12, v13\n"
    "v_cvt_pk_bf16_f32 v79, v14, v15\n"
    "s_waitcnt lgkmcnt(3)\n"
    "v_mfma_f32_16x16x32_bf16 v[56:59], v[120:123], v[152:155], 0\n"
    "ds_read_b128 v[148:151], v233 offset:52224\n"
    "v_mfma_f32_16x16x32_bf16 v[60:63], v[120:123], v[156:159], 0\n"
    "buffer_store_dwordx4 v[76:79], v236, %[rs], s46 offen offset:1024 sc1\n"
    "v_cvt_pk_bf16_f32 v80, v16, v17\n"
    "v_mfma_f32_16x16x32_bf16 v[68:71], v[124:127], v[156:159], 0\n"
    "ds_read_b128 v[120:123], v233 offset:53248\n"
    "v_mfma_f32_16x16x32_bf16 v[64:67], v[124:127], v[152:155], 0\n"
    "v_cvt_pk_bf16_f32 v81, v18, v19\n"
    "v_cvt_pk_bf16_f32 v82, v20, v21\n"
    "v_cvt_pk_bf16_f32 v83, v22, v23\n"
    "v_mfma_f32_16x16x32_bf16 v[56:59], v[128:131], v[160:163], v[56:59]\n"
    "ds_read_b128 v[124:127], v233 offset:54272\n"
    "v_mfma_f32_16x16x32_bf16 v[60:63], v[128:131], v[164:167], v[60:63]\n"
    "buffer_store_dwordx4 v[80:83], v236, %[rs], s46 offen offset:2048 sc1\n"
    "v_cvt_pk_bf16_f32 v84, v24, v25\n"
    "v_cvt_pk_bf16_f32 v85, v26, v27\n"
    "v_mfma_f32_16x16x32_bf16 v[68:71], v[132:135], v[164:167], v[68:71]\n"
    "ds_read_b128 v[128:131], v233 offset:55296\n"
    "v_mfma_f32_16x16x32_bf16 v[64:67], v[132:135], v[160:163], v[64:67]\n"
    "v_cvt_pk_bf16_f32 v86, v28, v29\n"
    "v_cvt_pk_bf16_f32 v87, v30, v31\n"
    "s_waitcnt lgkmcnt(3)\n"
    "v_mfma_f32_16x16x32_bf16 v[56:59], v[136:139], v[168:171], v[56:59]\n"
    "ds_read_b128 v[132:135], v233 offset:56320\n"
    "v_mfma_f32_16x16x32_bf16 v[60:63], v[136:139], v[172:175], v[60:63]\n"
    "buffer_store_dwordx4 v[84:87], v236, %[rs], s46 offen offset:3072 sc1\n"
    "v_cvt_pk_bf16_f32 v72, v32, v33\n"
    "v_mfma_f32_16x16x32_bf16 v[68:71], v[140:143], v[172:175], v[68:71]\n"
    "ds_read_b128 v[136:139], v233 offset:57344\n"
    "v_mfma_f32_16x16x32_bf16 v[64:67], v[140:143], v[168:171], v[64:67]\n"
    "v_cvt_pk_bf16_f32 v73, v34, v35\n"
    "v_cvt_pk_bf16_f32 v74, v36, v37\n"
    "v_cvt_pk_bf16_f32 v75, v38, v39\n"
    "v_mfma_f32_16x16x32_bf16 v[56:59], v[144:147], v[176:179], v[56:59]\n"
    "ds_read_b128 v[140:143], v233 offset:58368\n"
    "v_mfma_f32_16x16x32_bf16 v[60:63], v[144:147], v[180:183], v[60:63]\n"
    "s_add_u32 s46, s31, 4096\n"
    "buffer_store_dwordx4 v[72:75], v236, %[rs], s46 offen offset:0 sc1\n"
    "v_cvt_pk_bf16_f32 v76, v40, v41\n"
    "v_mfma_f32_16x16x32_bf16 v[68:71], v[148:151], v[180:183], v[68:71]\n"
    "ds_read_b128 v[144:147], v233 offset:59392\n"
    "v_mfma_f32_16x16x32_bf16 v[64:67], v[148:151], v[176:179], v[64:67]\n"
    "v_cvt_pk_bf16_f32 v77, v42, v43\n"
    "v_cvt_pk_bf16_f32 v78, v44, v45\n"
    "v_cvt_pk_bf16_f32 v79, v46, v47\n"
    "s_waitcnt lgkmcnt(3)\n"
    "v_mfma_f32_16x16x32_bf16 v[56:59], v[120:123], v[184:187], v[56:59]\n"
    "ds_read_b128 v[148:151], v233 offset:60416\n"
    "v_mfma_f32_16x16x32_bf16 v[60:63], v[120:123], v[188:191], v[60:63]\n"
    "buffer_store_dwordx4 v[76:79], v236, %[rs], s46 offen offset:1024 sc1\n"
    "v_mfma_f32_16x16x32_bf16 v[68:71], v[124:127], v[188:191], v[68:71]\n"
    "ds_read_b128 v[120:123], v233 offset:61440\n"
    "v_mfma_f32_16x16x32_bf16 v[64:67], v[124:127], v[184:187], v[64:67]\n"
    "v_cvt_pk_bf16_f32 v80, v48, v49\n"
    "v_cvt_pk_bf16_f32 v81, v50, v51\n"
    "v_cvt_pk_bf16_f32 v82, v52, v53\n"
    "v_mfma_f32_16x16x32_bf16 v[56:59], v[128:131], v[192:195], v[56:59]\n"
    "ds_read_b128 v[124:127], v233 offset:62464\n"
    "v_mfma_f32_16x16x32_bf16 v[60:63], v[128:131], v[196:199], v[60:63]\n"
    "v_cvt_pk_bf16_f32 v83, v54, v55\n"
    "v_mfma_f32_16x16x32_bf16 v[68:71], v[132:135], v[196:199], v[68:71]\n"
    "ds_read_b128 v[128:131], v233 offset:63488\n"
    "v_mfma_f32_16x16x32_bf16 v[64:67], v[132:135], v[192:195], v[64:67]\n"
    "buffer_store_dwordx4 v[80:83], v236, %[rs], s46 offen offset:2048 sc1\n"
    "s_waitcnt lgkmcnt(3)\n"
    "v_mfma_f32_16x16x32_bf16 v[56:59], v[136:139], v[200:203], v[56:59]\n"
    "ds_read_b128 v[132:135], v233 offset:64512\n"
    "v_mfma_f32_16x16x32_bf16 v[60:63], v[136:139], v[204:207], v[60:63]\n"
    "v_mfma_f32_16x16x32_bf16 v[68:71], v[140:143], v[204:207], v[68:71]\n"
    "ds_read_b128 v[136:139], v234 offset:0\n"
    "v_mfma_f32_16x16x32_bf16 v[64:67], v[140:143], v[200:203], v[64:67]\n"
    "v_mfma_f32_16x16x32_bf16 v[56:59], v[144:147], v[208:211], v[56:59]\n"
    "ds_read_b128 v[140:143], v234 offset:1024\n"
    "v_mfma_f32_16x16x32_bf16 v[60:63], v[144:147], v[212:215], v[60:63]\n"
    "v_mfma_f32_16x16x32_bf16 v[68:71], v[148:151], v[212:215], v[68:71]\n"
    "ds_read_b128 v[144:147], v234 offset:2048\n"
    "v_mfma_f32_16x16x32_bf16 v[64:67], v[148:151], v[208:211], v[64:67]\n"
    "s_waitcnt lgkmcnt(3)\n"
    "v_mfma_f32_16x16x32_bf16 v[56:59], v[120:123], v[216:219], v[56:59]\n"
    "ds_read_b128 v[148:151], v234 offset:3072\n"
    "v_mfma_f32_16x16x32_bf16 v[60:63], v[120:123], v[220:223], v[60:63]\n"
    "v_mfma_f32_16x16x32_bf16 v[68:71], v[124:127], v[220:223], v[68:71]\n"
    "ds_read_b128 v[120:123], v234 offset:4096\n"
    "v_mfma_f32_16x16x32_bf16 v[64:67], v[124:127], v[216:219], v[64:67]\n"
    "v_mfma_f32_16x16x32_bf16 v[56:59], v[128:131], a[144:147], v[56:59]\n"
    "ds_read_b128 v[124:127], v234 offset:5120\n"
    "v_mfma_f32_16x16x32_bf16 v[60:63], v[128:131], a[148:151], v[60:63]\n"
    "v_mfma_f32_16x16x32_bf16 v[68:71], v[132:135], a[148:151], v[68:71]\n"
    "ds_read_b128 v[128:131], v234 offset:6144\n"
    "v_mfma_f32_16x16x32_bf16 v[64:67], v[132:135], a[144:147], v[64:67]\n"
    "s_waitcnt lgkmcnt(3)\n"
    "v_mfma_f32_16x16x32_bf16 v[56:59], v[136:139], a[152:155], v[56:59]\n"
    "ds_read_b128 v[132:135], v234 offset:7168\n"
    "v_mfma_f32_16x16x32_bf16 v[60:63], v[136:139], a[156:159], v[60:63]\n"
    "v_mfma_f32_16x16x32_bf16 v[68:71], v[140:143], a[156:159], v[68:71]\n"
    "ds_read_b128 v[136:139], v234 offset:8192\n"
    "v_mfma_f32_16x16x32_bf16 v[64:67], v[140:143], a[152:155], v[64:67]\n"
    "v_mfma_f32_16x16x32_bf16 v[56:59], v[144:147], a[160:163], v[56:59]\n"
    "ds_read_b128 v[140:143], v234 offset:9216\n"
    "v_mfma_f32_16x16x32_bf16 v[60:63], v[144:147], a[164:167], v[60:63]\n"
    "v_mfma_f32_16x16x32_bf16 v[68:71], v[148:151], a[164:167], v[68:71]\n"
    "ds_read_b128 v[144:147], v234 offset:10240\n"
    "v_mfma_f32_16x16x32_bf16 v[64:67], v[148:151], a[160:163], v[64:67]\n"
    "s_waitcnt lgkmcnt(3)\n"
    "v_mfma_f32_16x16x32_bf16 v[56:59], v[120:123], a[168:171], v[56:59]\n"
    "ds_read_b128 v[148:151], v234 offset:11264\n"
    "v_mfma_f32_16x16x32_bf16 v[60:63], v[120:123], a[172:175], v[60:63]\n"
    "v_mfma_f32_16x16x32_bf16 v[68:71], v[124:127], a[172:175], v[68:71]\n"
    "ds_read_b128 v[120:123], v234 offset:12288\n"
    "v_mfma_f32_16x16x32_bf16 v[64:67], v[124:127], a[168:171], v[64:67]\n"
    "v_mfma_f32_16x16x32_bf16 v[56:59], v[128:131], a[176:179], v[56:59]\n"
    "ds_read_b128 v[124:127], v234 offset:13312\n"
    "v_mfma_f32_16x16x32_bf16 v[60:63], v[128:131], a[180:183], v[60:63]\n"
    "v_mfma_f32_16x16x32_bf16 v[68:71], v[132:135], a[180:183], v[68:71]\n"
    "ds_read_b128 v[128:131], v234 offset:14336\n"
    "v_mfma_f32_16x16x32_bf16 v[64:67], v[132:135], a[176:179], v[64:67]\n"
    "s_waitcnt lgkmcnt(3)\n"
    "v_mfma_f32_16x16x32_bf16 v[56:59], v[136:139], a[184:187], v[56:59]\n"
    "ds_read_b128 v[132:135], v234 offset:15360\n"
    "v_mfma_f32_16x16x32_bf16 v[60:63], v[136:139], a[188:191], v[60:63]\n"
    "v_mfma_f32_16x16x32_bf16 v[68:71], v[140:143], a[188:191], v[68:71]\n"
    "s_waitcnt vmcnt(7)\n"
    "s_barrier\n"
    "ds_read_b128 v[136:139], v234 offset:16384\n"
    "v_mfma_f32_16x16x32_bf16 v[64:67], v[140:143], a[184:187], v[64:67]\n"
    "v_mfma_f32_16x16x32_bf16 v[56:59], v[144:147], a[192:195], v[56:59]\n"
    "ds_read_b128 v[140:143], v234 offset:17408\n"
    "v_mfma_f32_16x16x32_bf16 v[60:63], v[144:147], a[196:199], v[60:63]\n"
    "v_mfma_f32_16x16x32_bf16 v[68:71], v[148:151], a[196:199], v[68:71]\n"
    "ds_read_b128 v[144:147], v234 offset:18432\n"
    "v_mfma_f32_16x16x32_bf16 v[64:67], v[148:151], a[192:195], v[64:67]\n"
    "s_waitcnt lgkmcnt(3)\n"
    "v_mfma_f32_16x16x32_bf16 v[56:59], v[120:123], a[200:203], v[56:59]\n"
    "ds_read_b128 v[148:151], v234 offset:19456\n"
    "v_mfma_f32_16x16x32_bf16 v[60:63], v[120:123], a[204:207], v[60:63]\n"
    "v_mfma_f32_16x16x32_bf16 v[68:71], v[124:127], a[204:207], v[68:71]\n"
    "ds_read_b128 v[120:123], v234 offset:20480\n"
    "v_mfma_f32_16x16x32_bf16 v[64:67], v[124:127], a[200:203], v[64:67]\n"
    "v_mfma_f32_16x16x32_bf16 v[56:59], v[128:131], a[208:211], v[56:59]\n"
    "ds_read_b128 v[124:127], v234 offset:21504\n"
    "v_mfma_f32_16x16x32_bf16 v[60:63], v[128:131], a[212:215], v[60:63]\n"
    "v_mfma_f32_16x16x32_bf16 v[68:71], v[132:135], a[212:215], v[68:71]\n"
    "ds_read_b128 v[128:131], v234 offset:22528\n"
    "v_mfma_f32_16x16x32_bf16 v[64:67], v[132:135], a[208:211], v[64:67]\n"
    "s_waitcnt lgkmcnt(3)\n"
    "v_mfma_f32_16x16x32_bf16 v[56:59], v[136:139], a[216:219], v[56:59]\n"
    "ds_read_b128 v[132:135], v234 offset:23552\n"
    "v_mfma_f32_16x16x32_bf16 v[60:63], v[136:139], a[220:223], v[60:63]\n"
    "v_mfma_f32_16x16x32_bf16 v[68:71], v[140:143], a[220:223], v[68:71]\n"
    "ds_read_b128 v[136:139], v234 offset:24576\n"
    "v_mfma_f32_16x16x32_bf16 v[64:67], v[140:143], a[216:219], v[64:67]\n"
    "v_mfma_f32_16x16x32_bf16 v[56:59], v[144:147], a[224:227], v[56:59]\n"
    "ds_read_b128 v[140:143], v234 offset:25600\n"
    "v_mfma_f32_16x16x32_bf16 v[60:63], v[144:147], a[228:231], v[60:63]\n"
    "v_mfma_f32_16x16x32_bf16 v[68:71], v[148:151], a[228:231], v[68:71]\n"
    "ds_read_b128 v[144:147], v234 offset:26624\n"
    "v_mfma_f32_16x16x32_bf16 v[64:67], v[148:151], a[224:227], v[64:67]\n"
    "s_waitcnt lgkmcnt(3)\n"
    "v_mfma_f32_16x16x32_bf16 v[56:59], v[120:123], a[232:235], v[56:59]\n"
    "ds_read_b128 v[148:151], v234 offset:27648\n"
    "v_mfma_f32_16x16x32_bf16 v[60:63], v[120:123], a[236:239], v[60:63]\n"
    "v_mfma_f32_16x16x32_bf16 v[68:71], v[124:127], a[236:239], v[68:71]\n"
    "ds_read_b128 v[120:123], v232 offset:0\n"
    "v_mfma_f32_16x16x32_bf16 v[64:67], v[124:127], a[232:235], v[64:67]\n"
    "v_mfma_f32_16x16x32_bf16 v[56:59], v[128:131], a[240:243], v[56:59]\n"
    "ds_read_b128 v[124:127], v232 offset:1024\n"
    "v_mfma_f32_16x16x32_bf16 v[60:63], v[128:131], a[244:247], v[60:63]\n"
    "v_mfma_f32_16x16x32_bf16 v[68:71], v[132:135], a[244:247], v[68:71]\n"
    "ds_read_b128 v[128:131], v232 offset:2048\n"
    "v_mfma_f32_16x16x32_bf16 v[64:67], v[132:135], a[240:243], v[64:67]\n"
    "s_waitcnt lgkmcnt(3)\n"
    "v_mfma_f32_16x16x32_bf16 v[56:59], v[136:139], a[248:251], v[56:59]\n"
    "ds_read_b128 v[132:135], v232 offset:3072\n"
    "v_mfma_f32_16x16x32_bf16 v[60:63], v[136:139], a[252:255], v[60:63]\n"
    "v_mfma_f32_16x16x32_bf16 v[68:71], v[140:143], a[252:255], v[68:71]\n"
    "ds_read_b128 v[136:139], v232 offset:4096\n"
    "v_mfma_f32_16x16x32_bf16 v[64:67], v[140:143], a[248:251], v[64:67]\n"
    "s_waitcnt vmcnt(0)\n"
    "s_barrier\n"
    "s_cmp_lg_u32 s35, 0\n"
    "s_cbranch_scc1 Lsig11_%=\n"
    "s_mov_b64 s[42:43], exec\n"
    "s_mov_b64 exec, 1\n"
    "buffer_store_dword v240, v239, %[rs], s33 offen offset:0 sc1\n"
    "s_mov_b64 exec, s[42:43]\n"
    "Lsig11_%=:\n"
    "v_mfma_f32_16x16x32_bf16 v[56:59], v[144:147], a[0:3], v[56:59]\n"
    "ds_read_b128 v[140:143], v232 offset:5120\n"
    "v_mfma_f32_16x16x32_bf16 v[60:63], v[144:147], a[4:7], v[60:63]\n"
    "v_mfma_f32_16x16x32_bf16 v[68:71], v[148:151], a[4:7], v[68:71]\n"
    "ds_read_b128 v[144:147], v232 offset:6144\n"
    "v_mfma_f32_16x16x32_bf16 v[64:67], v[148:151], a[0:3], v[64:67]\n"
    "s_waitcnt lgkmcnt(3)\n"
    "v_mfma_f32_16x16x32_bf16 v[56:59], v[120:123], a[8:11], v[56:59]\n"
    "ds_read_b128 v[148:151], v232 offset:7168\n"
    "v_mfma_f32_16x16x32_bf16 v[60:63], v[120:123], a[12:15], v[60:63]\n"
    "v_mfma_f32_16x16x32_bf16 v[68:71], v[124:127], a[12:15], v[68:71]\n"
    "ds_read_b128 v[120:123], v232 offset:8192\n"
    "v_mfma_f32_16x16x32_bf16 v[64:67], v[124:127], a[8:11], v[64:67]\n"
    "v_mfma_f32_16x16x32_bf16 v[56:59], v[128:131], a[16:19], v[56:59]\n"
    "ds_read_b128 v[124:127], v232 offset:9216\n"
    "v_mfma_f32_16x16x32_bf16 v[60:63], v[128:131], a[20:23], v[60:63]\n"
    "v_mfma_f32_16x16x32_bf16 v[68:71], v[132:135], a[20:23], v[68:71]\n"
    "ds_read_b128 v[128:131], v232 offset:10240\n"
    "v_mfma_f32_16x16x32_bf16 v[64:67], v[132:135], a[16:19], v[64:67]\n"
    "s_waitcnt lgkmcnt(3)\n"
    "v_mfma_f32_16x16x32_bf16 v[56:59], v[136:139], a[24:27], v[56:59]\n"
    "ds_read_b128 v[132:135], v232 offset:11264\n"
    "v_mfma_f32_16x16x32_bf16 v[60:63], v[136:139], a[28:31], v[60:63]\n"
    "v_mfma_f32_16x16x32_bf16 v[68:71], v[140:143], a[28:31], v[68:71]\n"
    "ds_read_b128 v[136:139], v232 offset:12288\n"
    "v_mfma_f32_16x16x32_bf16 v[64:67], v[140:143], a[24:27], v[64:67]\n"
    "v_mfma_f32_16x16x32_bf16 v[56:59], v[144:147], a[32:35], v[56:59]\n"
    "ds_read_b128 v[140:143], v232 offset:13312\n"
    "v_mfma_f32_16x16x32_bf16 v[60:63], v[144:147], a[36:39], v[60:63]\n"
    "v_mfma_f32_16x16x32_bf16 v[68:71], v[148:151], a[36:39], v[68:71]\n"
    "ds_read_b128 v[144:147], v232 offset:14336\n"
    "v_mfma_f32_16x16x32_bf16 v[64:67], v[148:151], a[32:35], v[64:67]\n"
    "s_waitcnt lgkmcnt(3)\n"
    "v_mfma_f32_16x16x32_bf16 v[56:59], v[120:123], a[40:43], v[56:59]\n"
    "ds_read_b128 v[148:151], v232 offset:15360\n"
    "v_mfma_f32_16x16x32_bf16 v[60:63], v[120:123], a[44:47], v[60:63]\n"
    "v_mfma_f32_16x16x32_bf16 v[68:71], v[124:127], a[44:47], v[68:71]\n"
    "ds_read_b128 v[120:123], v232 offset:16384\n"
    "v_mfma_f32_16x16x32_bf16 v[64:67], v[124:127], a[40:43], v[64:67]\n"
    "v_mfma_f32_16x16x32_bf16 v[56:59], v[128:131], a[48:51], v[56:59]\n"
    "ds_read_b128 v[124:127], v232 offset:17408\n"
    "v_mfma_f32_16x16x32_bf16 v[60:63], v[128:131], a[52:55], v[60:63]\n"
    "v_mfma_f32_16x16x32_bf16 v[68:71], v[132:135], a[52:55], v[68:71]\n"
    "ds_read_b128 v[128:131], v232 offset:18432\n"
    "v_mfma_f32_16x16x32_bf16 v[64:67], v[132:135], a[48:51], v[64:67]\n"
    "s_waitcnt lgkmcnt(3)\n"
    "v_mfma_f32_16x16x32_bf16 v[56:59], v[136:139], a[56:59], v[56:59]\n"
    "ds_read_b128 v[132:135], v232 offset:19456\n"
    "v_mfma_f32_16x16x32_bf16 v[60:63], v[136:139], a[60:63], v[60:63]\n"
    "v_mfma_f32_16x16x32_bf16 v[68:71], v[140:143], a[60:63], v[68:71]\n"
    "ds_read_b128 v[136:139], v232 offset:20480\n"
    "v_mfma_f32_16x16x32_bf16 v[64:67], v[140:143], a[56:59], v[64:67]\n"
    "v_mfma_f32_16x16x32_bf16 v[56:59], v[144:147], a[64:67], v[56:59]\n"
    "ds_read_b128 v[140:143], v232 offset:21504\n"
    "v_mfma_f32_16x16x32_bf16 v[60:63], v[144:147], a[68:71], v[60:63]\n"
    "v_mfma_f32_16x16x32_bf16 v[68:71], v[148:151], a[68:71], v[68:71]\n"
    "ds_read_b128 v[144:147], v232 offset:22528\n"
    "v_mfma_f32_16x16x32_bf16 v[64:67], v[148:151], a[64:67], v[64:67]\n"
    "s_waitcnt lgkmcnt(3)\n"
    "v_mfma_f32_16x16x32_bf16 v[56:59], v[120:123], a[72:75], v[56:59]\n"
    "ds_read_b128 v[148:151], v232 offset:23552\n"
    "v_mfma_f32_16x16x32_bf16 v[60:63], v[120:123], a[76:79], v[60:63]\n"
    "v_mfma_f32_16x16x32_bf16 v[68:71], v[124:127], a[76:79], v[68:71]\n"
    "v_mfma_f32_16x16x32_bf16 v[64:67], v[124:127], a[72:75], v[64:67]\n"
    "v_mfma_f32_16x16x32_bf16 v[56:59], v[128:131], a[80:83], v[56:59]\n"
    "v_mfma_f32_16x16x32_bf16 v[60:63], v[128:131], a[84:87], v[60:63]\n"
    "v_mfma_f32_16x16x32_bf16 v[68:71], v[132:135], a[84:87], v[68:71]\n"
    "v_mfma_f32_16x16x32_bf16 v[64:67], v[132:135], a[80:83], v[64:67]\n"
    "s_waitcnt lgkmcnt(0)\n"
    "v_mfma_f32_16x16x32_bf16 v[56:59], v[136:139], a[88:91], v[56:59]\n"
    "v_mfma_f32_16x16x32_bf16 v[60:63], v[136:139], a[92:95], v[60:63]\n"
    "v_mfma_f32_16x16x32_bf16 v[68:71], v[140:143], a[92:95], v[68:71]\n"
    "v_mfma_f32_16x16x32_bf16 v[64:67], v[140:143], a[88:91], v[64:67]\n"
    "v_mfma_f32_16x16x32_bf16 v[56:59], v[144:147], a[96:99], v[56:59]\n"
    "v_mfma_f32_16x16x32_bf16 v[60:63], v[144:147], a[100:103], v[60:63]\n"
    "v_mfma_f32_16x16x32_bf16 v[68:71], v[148:151], a[100:103], v[68:71]\n"
    "v_mfma_f32_16x16x32_bf16 v[64:67], v[148:151], a[96:99], v[64:67]\n"
    "s_nop 7\n"
    : "=&{v[48:55]}"(csh), "=&{v[56:71]}"(cown), "=&{a[104:111]}"(hp), "=&{a[112:115]}"(hs), "=&{a[116:123]}"(sq), "=&{a[124:127]}"(sm)
    : "{v[232:239]}"(vin), "{v[240:243]}"(vin2), "{s[24:31]}"(sin), "{s[32:39]}"(sin2), [rs]"s"(rws), [hf]"s"(ohf), [hp]"s"(ohp)
    : "v0","v1","v2","v3","v4","v5","v6","v7","v8","v9","v10","v11","v12","v13","v14","v15","v16","v17","v18","v19","v20","v21","v22","v23","v24","v25","v26","v27","v28","v29","v30","v31","v32","v33","v34","v35","v36","v37","v38","v39","v40","v41","v42","v43","v44","v45","v46","v47","v72","v73","v74","v75","v76","v77","v78","v79","v80","v81","v82","v83","v84","v85","v86","v87","v88","v89","v90","v91","v92","v93","v94","v95","v96","v97","v98","v99","v100","v101","v102","v103","v104","v105","v106","v107","v108","v109","v110","v111","v112","v113","v114","v115","v116","v117","v118","v119","v120","v121","v122","v123","v124","v125","v126","v127","v128","v129","v130","v131","v132","v133","v134","v135","v136","v137","v138","v139","v140","v141","v142","v143","v144","v145","v146","v147","v148","v149","v150","v151","v152","v153","v154","v155","v156","v157","v158","v159","v160","v161","v162","v163","v164","v165","v166","v167","v168","v169","v170","v171","v172","v173","v174","v175","v176","v177","v178","v179","v180","v181","v182","v183","v184","v185","v186","v187","v188","v189","v190","v191","v192","v193","v194","v195","v196","v197","v198","v199","v200","v201","v202","v203","v204","v205","v206","v207","v208","v209","v210","v211","v212","v213","v214","v215","v216","v217","v218","v219","v220","v221","v222","v223","v224","v225","a0","a1","a2","a3","a4","a5","a6","a7","a8","a9","a10","a11","a12","a13","a14","a15","a16","a17","a18","a19","a20","a21","a22","a23","a24","a25","a26","a27","a28","a29","a30","a31","a32","a33","a34","a35","a36","a37","a38","a39","a40","a41","a42","a43","a44","a45","a46","a47","a48","a49","a50","a51","a52","a53","a54","a55","a56","a57","a58","a59","a60","a61","a62","a63","a64","a65","a66","a67","a68","a69","a70","a71","a72","a73","a74","a75","a76","a77","a78","a79","a80","a81","a82","a83","a84","a85","a86","a87","a88","a89","a90","a91","a92","a93","a94","a95","a96","a97","a98","a99","a100","a101","a102","a103","a128","a129","a130","a131","a132","a133","a134","a135","a136","a137","a138","a139","a140","a141","a142","a143","a144","a145","a146","a147","a148","a149","a150","a151","a152","a153","a154","a155","a156","a157","a158","a159","a160","a161","a162","a163","a164","a165","a166","a167","a168","a169","a170","a171","a172","a173","a174","a175","a176","a177","a178","a179","a180","a181","a182","a183","a184","a185","a186","a187","a188","a189","a190","a191","a192","a193","a194","a195","a196","a197","a198","a199","a200","a201","a202","a203","a204","a205","a206","a207","a208","a209","a210","a211","a212","a213","a214","a215","a216","a217","a218","a219","a220","a221","a222","a223","a224","a225","a226","a227","a228","a229","a230","a231","a232","a233","a234","a235","a236","a237","a238","a239","a240","a241","a242","a243","a244","a245","a246","a247","a248","a249","a250","a251","a252","a253","a254","a255","s40","s41","s42","s43","s44","s45","s46","s47","s96","s97","s98","s99","vcc","scc","memory");
  f32x2 y[2][6]; float ri[2];
#pragma unroll
  for (int m = 0; m < 2; ++m) {
    ri[m] = __builtin_amdgcn_rcpf(rbf(__builtin_amdgcn_sqrtf(rbf(rbf(swapadd32(swapadd16((sq[4 * m] + sq[4 * m + 1]) + (sq[4 * m + 2] + sq[4 * m + 3]) + sm[m])) * (1.0f / D_)) + 1e-5f))));
#pragma unroll
    for (int e = 0; e < 6; ++e) { const int k = e >> 1, q = 2 * m + (e & 1); const u32 hh = k < 2 ? hp[4 * m + e] : hs[q];
      y[m][e] = (k < 2 ? f32x2{cown[8 * k + 2 * q], cown[8 * k + 2 * q + 1]} : f32x2{csh[2 * q], csh[2 * q + 1]}) * ri[m] + (f32x2{bflo(hh), bfhi(hh)} + sm[2 + m]);
      asm volatile("" : "+v"(y[m][e])); } }
  { const rsrc_t rF = mkr(ws + OFF_CNT + (256 + grp * 4) * 128, 512);
    for (int spin = 0; spin < (1 << 16); ++spin) { if (__all(__builtin_amdgcn_raw_buffer_load_b32(rF, (lane & 3) * 128, 0, 16) == 1u)) break; __builtin_amdgcn_s_sleep(1); }
    asm volatile("" ::: "memory"); }
  const rsrc_t rs = mkr(ws + OFF_SLAB + (size_t)grp * 114688, 114688), ro = mkr((char*)out + (size_t)t16 * 18432, 36864);
  u32x4 p[3][3];
#pragma unroll
  for (int k = 0; k < 3; ++k)
#pragma unroll
    for (int s = 0; s < 3; ++s) { const int src = (kq + 1 + s) & 3, pos = k < 2 ? ((2 * kq + k - 2 * src - 2) & 7) : 6;
      p[k][s] = __builtin_amdgcn_raw_buffer_load_b128(rs, l16, (k < 2 || w == kq) ? ((src * 4 + w) * 7 + pos) * 1024 : 114688, 16); }
  u32 z[2][6];
#pragma unroll
  for (int e = 0; e < 6; ++e)
#pragma unroll
    for (int m = 0; m < 2; ++m) { const int k = e >> 1, q = 2 * m + (e & 1);
      const f32x2 v = ((f32x2{bflo(p[k][0][q]), bfhi(p[k][0][q])} + f32x2{bflo(p[k][1][q]), bfhi(p[k][1][q])}) + f32x2{bflo(p[k][2][q]), bfhi(p[k][2][q])}) * ri[m] + y[m][e];
      z[m][e] = cvtpk(v[0], v[1]);
      if (e == 3) __builtin_amdgcn_raw_buffer_store_b128(u32x4{z[m][0], z[m][1], z[m][2], z[m][3]}, ro, (16 * m + tok) * 1152 + (ub + kq) * 64 + g * 16, 0, 16);
      if (e == 5 && w == kq) __builtin_amdgcn_raw_buffer_store_b64(u32x2{z[m][4], z[m][5]}, ro, (16 * m + tok) * 1152 + us * 64 + g * 16 + hs_ * 8, 0, 16); }
  asm volatile("k2_code_end:");
#endif
}

extern "C" void kernel_launch(void* const* d_in, const int* in_sizes, int n_in, void* d_out, int out_size, void* d_ws, size_t ws_size, hipStream_t stream) {
  const u16* x = (const u16*)d_in[0]; const u16* wq = (const u16*)d_in[1]; const u16* wk = (const u16*)d_in[2]; const u16* wv = (const u16*)d_in[3];
  const u16* wo = (const u16*)d_in[4]; const u16* w1 = (const u16*)d_in[6]; const u16* w2 = (const u16*)d_in[7];
  char* ws = (char*)d_ws;
  (void)hipFuncSetAttribute((const void*)k1_qkv, hipFuncAttributeMaxDynamicSharedMemorySize, K1_LDS);
  (void)hipFuncSetAttribute((const void*)k2_rest, hipFuncAttributeMaxDynamicSharedMemorySize, K2_LDS);
  k1_qkv<<<256, 704, K1_LDS, stream>>>(x, wq, wk, wv, wo, ws);
  k2_rest<<<256, 256, K2_LDS, stream>>>(x, w1, w2, ws, (u16*)d_out);
}
```
